```python
import torch

def _softmax(x, axis=-1):
    return torch.softmax(x, dim=axis)

def _gelu(x):
    return torch.nn.functional.gelu(x, approximate='tanh')

def _silu(x):
    return torch.nn.functional.silu(x)

def _sigmoid(x):
    return torch.sigmoid(x)

def _layernorm(x, axis=-1, eps=1e-05):
    m = x.mean(dim=axis, keepdim=True)
    v = x.var(dim=axis, keepdim=True, unbiased=False)
    return (x - m) / torch.sqrt(v + eps)

def _scatter_add(tgt, idx, src, axis=0):
    return tgt.clone().index_add_(axis, idx, src)

def _segment_sum(src, seg, n):
    out = torch.zeros((n,) + src.shape[1:], dtype=src.dtype)
    return out.index_add_(0, seg, src)

def _pad(x, pw):
    flat = tuple((v for pair in reversed(pw) for v in pair))
    return torch.nn.functional.pad(x, flat)
kernel_name = 'sequential_preLN__memory_aug_recurrent_mem_tr__swiglu_ffn__prompt'

def setup_inputs():

    _w = lambda a: (lambda t: t.to(torch.bfloat16) if t.is_floating_point() else t)(torch.as_tensor(a))

    def _A_setup():
        B, S, Dm = (1, 2048, 640)
        H, Dh = (32, 128)
        M, seg = (8, 32)
        seg = min(seg, S)
        S = S // seg * seg
        return {'x': _w((lambda *s: torch.randn(*s, dtype=torch.bfloat16) * 0.05)(B, S, Dm)), 'mem0': _w((lambda *s: torch.randn(*s, dtype=torch.bfloat16) * 0.2)(B, M, Dm)), 'Wq': _w((lambda *s: torch.randn(*s, dtype=torch.bfloat16) * 1.6)(Dm, H, Dh) * Dm ** (-0.5)), 'Wk': _w((lambda *s: torch.randn(*s, dtype=torch.bfloat16) * 1.6)(Dm, H, Dh) * Dm ** (-0.5)), 'Wv': _w((lambda *s: torch.randn(*s, dtype=torch.bfloat16) * 0.2)(Dm, H, Dh) * Dm ** (-0.5)), 'Wo': _w((lambda *s: torch.randn(*s, dtype=torch.bfloat16) * 0.2)(H, Dh, Dm) * (H * Dh) ** (-0.5)), 'seg_len': seg}
    _A_in = _A_setup()

    def _B_setup():
        B, S, D, F = (1, 2048, 640, 1728)
        return {'x': _w((lambda *s: torch.randn(*s, dtype=torch.bfloat16) * 0.05)(B, S, D)), 'w_gate': _w((lambda *s: torch.randn(*s, dtype=torch.bfloat16) * 0.05)(D, F)), 'w_up': _w((lambda *s: torch.randn(*s, dtype=torch.bfloat16) * 0.05)(D, F)), 'w_down': _w((lambda *s: torch.randn(*s, dtype=torch.bfloat16) * 0.05)(F, D))}
    _B_in = _B_setup()
    return {'A_x': _A_in['x'], 'A_mem0': _A_in['mem0'], 'A_Wq': _A_in['Wq'], 'A_Wk': _A_in['Wk'], 'A_Wv': _A_in['Wv'], 'A_Wo': _A_in['Wo'], 'A_seg_len': _A_in['seg_len'], 'B_x': _B_in['x'], 'B_w_gate': _B_in['w_gate'], 'B_w_up': _B_in['w_up'], 'B_w_down': _B_in['w_down']}

def reference(A_x, A_mem0, A_Wq, A_Wk, A_Wv, A_Wo, A_seg_len, B_x, B_w_gate, B_w_up, B_w_down):

    def _A_ref(x, mem0, Wq, Wk, Wv, Wo, seg_len):
        B, S, Dm = x.shape
        H, Dh = (Wq.shape[1], Wq.shape[2])
        M = mem0.shape[1]
        scale = Dh ** (-0.5)
        n_seg = S // seg_len
        L = M + seg_len
        idx = torch.arange(L)
        mask = (idx[:, None] < M) | (idx[:, None] >= idx[None, :])
        mem = mem0
        outs = []
        for s in range(n_seg):
            xs = x[:, s * seg_len:(s + 1) * seg_len, :].clone()
            h = torch.concatenate([mem, xs], axis=1)
            q = torch.einsum('btd,dhe->bhte', h, Wq)
            k = torch.einsum('btd,dhe->bhte', h, Wk)
            v = torch.einsum('btd,dhe->bhte', h, Wv)
            scores = torch.einsum('bhqe,bhke->bhqk', q, k) * scale
            scores = torch.where(mask[None, None], scores, -1000000000.0)
            p = _softmax(scores, axis=-1)
            o = torch.einsum('bhqk,bhke->bhqe', p, v)
            o = torch.einsum('bhte,hed->btd', o, Wo)
            mem = o[:, :M, :].clone()
            outs.append(o[:, M:, :])
        return torch.concatenate(outs, axis=1)

    def _B_ref(x, w_gate, w_up, w_down):
        g = _silu(x @ w_gate)
        u = x @ w_up
        return g * u @ w_down
    x = A_x
    h = x + _A_ref(_layernorm(x, axis=-1), A_mem0, A_Wq, A_Wk, A_Wv, A_Wo, A_seg_len)
    h2 = h + _B_ref(_layernorm(h, axis=-1), B_w_gate, B_w_up, B_w_down)
    return h2
```

```cpp
#include <hip/hip_runtime.h>
#include <hip/hip_bf16.h>
#include <stdint.h>

#define DI __device__ __forceinline__
#define LAS __attribute__((address_space(3)))
typedef unsigned short u16;
typedef unsigned int u32;
typedef unsigned long long u64;
typedef u32 u2_t __attribute__((ext_vector_type(2)));
typedef u32 u4_t __attribute__((ext_vector_type(4)));
typedef LAS char* lptr;

constexpr int DM = 640, NH = 32, HD = 4096, FF = 1728, NSEG = 64;
constexpr u32 SENT = 0xFF81FF81u;
constexpr u32 C_O = 0;
constexpr u32 C_MEM = 64;
constexpr u32 C_HINT = 84;
constexpr u32 C_V = 85;
constexpr u32 C_END = 117;
constexpr u32 CTRL = 2u << 20;
constexpr u32 F_EP = CTRL, F_ERR = CTRL + 1280, F_INIT = CTRL + 49152;
constexpr u32 F_QKV = CTRL + 4096, F_OX = CTRL + 20480, F_H = CTRL + 24576, F_ACT = CTRL + 32768, CTRL_BYTES = 40960;
constexpr u32 PROBE = C_END << 14;
constexpr u32 O_XN = 3u << 20;
constexpr u32 O_HF = 6u << 20;
constexpr u32 O_ST = 9u << 20;
constexpr u32 O_ACT = 10u << 20;
constexpr u32 O_QF = 20u << 20;
constexpr u32 O_OF = 116u << 20;
constexpr u32 CODE_BYTES = 65 * 1024;
constexpr u32 LDS_BYTES = 163840;

#define ROLEMASK 31
struct Params {
  const u16* x; const u16* mem0; const u16* wq; const u16* wk; const u16* wv; const u16* wo;
  const u16* wg; const u16* wu; const u16* wd; u16* out; char* ws;
  int rolemask; int dA; int dB; int dP;
};

#define VMW(n) asm volatile("s_waitcnt vmcnt(" #n ")" ::: "memory")

DI int lane_id() { int l; asm volatile("v_mbcnt_lo_u32_b32 %0, -1, 0\n\tv_mbcnt_hi_u32_b32 %0, -1, %0" : "=v"(l)); return l; }
DI void gst4m(char* sb, u32 vo, u32 v, u64 mask) { asm volatile("s_mov_b64 exec, %3\n\tglobal_store_dword %0, %1, %2 sc1\n\ts_mov_b64 exec, -1" :: "v"(vo), "v"(v), "s"(sb), "s"(mask) : "memory"); }

DI void gst16(char* sb, u32 vo, u4_t v) { asm volatile("global_store_dwordx4 %0, %1, %2 sc1\n\ts_nop 1" :: "v"(vo), "v"(v), "s"(sb) : "memory"); }
DI void gst4(char* sb, u32 vo, u32 v) { asm volatile("global_store_dword %0, %1, %2 sc1" :: "v"(vo), "v"(v), "s"(sb) : "memory"); }
DI void post_init(char* ws, int i, u64 ep, u32 payload, int w) {
  VMW(0); __syncthreads();
  if (w == 0) gst16(ws + F_INIT + i * 128, 0, u4_t{(u32)ep, (u32)(ep >> 32) | payload, 0u, 0u});
}

#define STAMP(i) do {} while (0)
#define DUMP() do {} while (0)
DI u32 wlane(int lane, u32 rowb, int GS) { const int G = lane >> 4; return (u32)(GS * G + ((lane >> 2) & 3)) * rowb + (u32)(((lane & 3) ^ ((G & 1) << 1)) * 16); }

DI u32 csrc256(int lane, int w, u32 rowb) { const int r = lane >> 4, ch = lane & 15, g = r | ((w & 1) << 2); return (u32)(8 * w + r) * rowb + (u32)(((((ch >> 1) ^ g) << 1) | (ch & 1)) * 16); }
DI u32 crd256(int lane, int cb) { const int G = lane >> 4, q = (lane >> 2) & 3; return G * 2048 + q * 256 + ((cb ^ (q | ((G & 1) << 2))) * 32) + (lane & 3) * 8; }
DI u32 csrc128(int lane, int w, u32 rowb) { const int r = lane >> 3, ch = lane & 7, g = ((r & 3) >> 1) | ((w & 1) << 1); return (u32)(8 * w + r) * rowb + (u32)(((((ch >> 1) ^ g) << 1) | (ch & 1)) * 16); }
DI u32 crd128(int lane, int cb) { const int G = lane >> 4, q = (lane >> 2) & 3; return G * 1024 + q * 128 + ((cb ^ ((q >> 1) | ((G & 1) << 1))) * 32) + (lane & 3) * 8; }

constexpr u32 A_QI = 10240;
DI void role_a(const Params& p, int h, int x, lptr L, int lane, int w) {
  const u32 lb = __builtin_amdgcn_readfirstlane((u32)(size_t)L);
  const int G = lane >> 4, tok = lane & 7, ph = (lane >> 3) & 1;
  const size_t col = (size_t)h * 256;
  u32 vw = csrc256(lane, w, 8192), vrd0 = lb + crd256(lane, 2 * w), vrd1 = lb + crd256(lane, 2 * w + 1);
  int vdg = (lane & 15) - 4 * G;
  const u32 vlm = lb + lane * 16;
  const u32 viw = lb + A_QI + (32 * w + 16 * (lane >> 5) + (lane & 15)) * 16 + (G & 1) * 8;
  const u32 vir = lb + A_QI + (4 * G + ((lane >> 2) & 3)) * 16 + (lane & 1) * 8;
  const u32 vmv0 = (x << 14) + w * 1024 + G * 256 + (tok >> 2) * 128 + ((tok & 3) * 2 + ph) * 16;
  const u32 vmv2 = (4 << 14) + x * 2048 + (w >> 1) * 1024 + (2 * (w & 1) + (G & 1)) * 256 + (tok >> 2) * 128 + ((tok & 3) * 2 + ph) * 16;
  const u32 vov = (tok >> 2) * (8 << 14) + w * 256 + G * 64 + (tok & 3) * 16;
  const u32 vg16 = lane * 16, vflv = F_QKV + (u32)(min(lane, 2) * 32 + h) * 128;
  const u32 vidx = ph ? 0xEEEEu : 0x4444u;
  asm volatile(
    "s_mov_b32 s99, m0\n"
    "s_mov_b32 s46, 2097152\n"
    "s_load_dwordx2 s[46:47], %[ws], s46\n"
    "s_lshr_b32 s41, %[h], 3\n"
    "s_lshl_b32 s41, s41, 14\n"
    "s_and_b32 s92, %[h], 7\n"
    "s_lshl_b32 s92, s92, 10\n"
    "s_add_u32 s89, s41, s92\n"
    "s_mov_b32 s72, 0x00ff00ff\n"
    "s_mov_b32 s73, 0x00ff00ff\n"
    "v_mov_b32 v224, 0xff81ff81\n"
    "v_mov_b32 v225, 0xff81ff81\n"
    "v_mov_b32 v226, 0xff81ff81\n"
    "v_mov_b32 v227, 0xff81ff81\n"
    "s_mov_b64 s[44:45], %[ws]\n"
    "s_add_u32 s44, s44, s89\n"
    "s_addc_u32 s45, s45, 0\n"
    "s_mov_b64 exec, s[72:73]\n"
    "s_add_u32 s56, s44, 0\n"
    "s_addc_u32 s57, s45, 0\n"
    "global_store_dwordx4 %[vov], v[224:227], s[56:57] offset:0 sc1\n"
    "s_add_u32 s56, s44, 8192\n"
    "s_addc_u32 s57, s45, 0\n"
    "global_store_dwordx4 %[vov], v[224:227], s[56:57] offset:0 sc1\n"
    "s_add_u32 s56, s44, 262144\n"
    "s_addc_u32 s57, s45, 0\n"
    "global_store_dwordx4 %[vov], v[224:227], s[56:57] offset:0 sc1\n"
    "s_add_u32 s56, s44, 270336\n"
    "s_addc_u32 s57, s45, 0\n"
    "global_store_dwordx4 %[vov], v[224:227], s[56:57] offset:0 sc1\n"
    "s_mov_b64 exec, -1\n"
    "s_lshl_b32 s41, %[w], 11\n"
    "s_add_u32 s41, s41, %[lb]\n"
    "v_add_u32 v232, 31744, %[vw]\n"
    "v_cmp_le_i32 s[76:77], 0, %[vdg]\n"
    "v_cmp_le_i32 s[78:79], 1, %[vdg]\n"
    "v_cmp_le_i32 s[80:81], 2, %[vdg]\n"
    "v_cmp_le_i32 s[82:83], 3, %[vdg]\n"
    "v_add_u32 v233, 65536, %[vrd0]\n"
    "v_add_u32 v234, 65536, %[vrd1]\n"
    "s_mov_b64 s[44:45], %[wq]\n"
    "s_add_u32 m0, s41, 0\n"
    "s_nop 0\n"
    "global_load_lds_dwordx4 %[vw], s[44:45] offset:0 nt\n"
    "global_load_lds_dwordx4 v232, s[44:45] offset:1024 nt\n"
    "s_add_u32 s44, s44, 262144\n"
    "s_addc_u32 s45, s45, 0\n"
    "s_add_u32 m0, s41, 8192\n"
    "s_nop 0\n"
    "global_load_lds_dwordx4 %[vw], s[44:45] offset:0 nt\n"
    "global_load_lds_dwordx4 v232, s[44:45] offset:1024 nt\n"
    "s_add_u32 s44, s44, 262144\n"
    "s_addc_u32 s45, s45, 0\n"
    "s_add_u32 m0, s41, 16384\n"
    "s_nop 0\n"
    "global_load_lds_dwordx4 %[vw], s[44:45] offset:0 nt\n"
    "global_load_lds_dwordx4 v232, s[44:45] offset:1024 nt\n"
    "s_add_u32 s44, s44, 262144\n"
    "s_addc_u32 s45, s45, 0\n"
    "s_add_u32 m0, s41, 24576\n"
    "s_nop 0\n"
    "global_load_lds_dwordx4 %[vw], s[44:45] offset:0 nt\n"
    "global_load_lds_dwordx4 v232, s[44:45] offset:1024 nt\n"
    "s_add_u32 s44, s44, 262144\n"
    "s_addc_u32 s45, s45, 0\n"
    "s_add_u32 m0, s41, 32768\n"
    "s_nop 0\n"
    "global_load_lds_dwordx4 %[vw], s[44:45] offset:0 nt\n"
    "global_load_lds_dwordx4 v232, s[44:45] offset:1024 nt\n"
    "s_add_u32 s44, s44, 262144\n"
    "s_addc_u32 s45, s45, 0\n"
    "s_add_u32 m0, s41, 40960\n"
    "s_nop 0\n"
    "global_load_lds_dwordx4 %[vw], s[44:45] offset:0 nt\n"
    "global_load_lds_dwordx4 v232, s[44:45] offset:1024 nt\n"
    "s_add_u32 s44, s44, 262144\n"
    "s_addc_u32 s45, s45, 0\n"
    "s_add_u32 m0, s41, 49152\n"
    "s_nop 0\n"
    "global_load_lds_dwordx4 %[vw], s[44:45] offset:0 nt\n"
    "global_load_lds_dwordx4 v232, s[44:45] offset:1024 nt\n"
    "s_add_u32 s44, s44, 262144\n"
    "s_addc_u32 s45, s45, 0\n"
    "s_add_u32 m0, s41, 57344\n"
    "s_nop 0\n"
    "global_load_lds_dwordx4 %[vw], s[44:45] offset:0 nt\n"
    "global_load_lds_dwordx4 v232, s[44:45] offset:1024 nt\n"
    "s_add_u32 s44, s44, 262144\n"
    "s_addc_u32 s45, s45, 0\n"
    "s_add_u32 m0, s41, 65536\n"
    "s_nop 0\n"
    "global_load_lds_dwordx4 %[vw], s[44:45] offset:0 nt\n"
    "global_load_lds_dwordx4 v232, s[44:45] offset:1024 nt\n"
    "s_add_u32 s44, s44, 262144\n"
    "s_addc_u32 s45, s45, 0\n"
    "s_add_u32 m0, s41, 73728\n"
    "s_nop 0\n"
    "global_load_lds_dwordx4 %[vw], s[44:45] offset:0 nt\n"
    "global_load_lds_dwordx4 v232, s[44:45] offset:1024 nt\n"
    "s_add_u32 s44, s44, 262144\n"
    "s_addc_u32 s45, s45, 0\n"
    "s_add_u32 m0, s41, 81920\n"
    "s_nop 0\n"
    "global_load_lds_dwordx4 %[vw], s[44:45] offset:0 nt\n"
    "global_load_lds_dwordx4 v232, s[44:45] offset:1024 nt\n"
    "s_add_u32 s44, s44, 262144\n"
    "s_addc_u32 s45, s45, 0\n"
    "s_add_u32 m0, s41, 90112\n"
    "s_nop 0\n"
    "global_load_lds_dwordx4 %[vw], s[44:45] offset:0 nt\n"
    "global_load_lds_dwordx4 v232, s[44:45] offset:1024 nt\n"
    "s_add_u32 s44, s44, 262144\n"
    "s_addc_u32 s45, s45, 0\n"
    "s_waitcnt vmcnt(16)\n"
    "s_waitcnt lgkmcnt(0)\n"
    "s_barrier\n"
    "s_add_u32 m0, s41, 98304\n"
    "s_nop 0\n"
    "global_load_lds_dwordx4 %[vw], s[44:45] offset:0 nt\n"
    "global_load_lds_dwordx4 v232, s[44:45] offset:1024 nt\n"
    "s_add_u32 s44, s44, 262144\n"
    "s_addc_u32 s45, s45, 0\n"
    "s_add_u32 m0, s41, 106496\n"
    "s_nop 0\n"
    "global_load_lds_dwordx4 %[vw], s[44:45] offset:0 nt\n"
    "global_load_lds_dwordx4 v232, s[44:45] offset:1024 nt\n"
    "s_add_u32 s44, s44, 262144\n"
    "s_addc_u32 s45, s45, 0\n"
    "s_add_u32 m0, s41, 114688\n"
    "s_nop 0\n"
    "global_load_lds_dwordx4 %[vw], s[44:45] offset:0 nt\n"
    "global_load_lds_dwordx4 v232, s[44:45] offset:1024 nt\n"
    "s_add_u32 s44, s44, 262144\n"
    "s_addc_u32 s45, s45, 0\n"
    "s_add_u32 m0, s41, 122880\n"
    "s_nop 0\n"
    "global_load_lds_dwordx4 %[vw], s[44:45] offset:0 nt\n"
    "global_load_lds_dwordx4 v232, s[44:45] offset:1024 nt\n"
    "s_add_u32 s44, s44, 262144\n"
    "s_addc_u32 s45, s45, 0\n"
    "ds_read_b64_tr_b16 a[0:1], %[vrd0] offset:0\n"
    "ds_read_b64_tr_b16 a[2:3], %[vrd0] offset:1024\n"
    "ds_read_b64_tr_b16 a[80:81], %[vrd1] offset:0\n"
    "ds_read_b64_tr_b16 a[82:83], %[vrd1] offset:1024\n"
    "ds_read_b64_tr_b16 a[4:5], %[vrd0] offset:8192\n"
    "ds_read_b64_tr_b16 a[6:7], %[vrd0] offset:9216\n"
    "ds_read_b64_tr_b16 a[84:85], %[vrd1] offset:8192\n"
    "ds_read_b64_tr_b16 a[86:87], %[vrd1] offset:9216\n"
    "ds_read_b64_tr_b16 a[8:9], %[vrd0] offset:16384\n"
    "ds_read_b64_tr_b16 a[10:11], %[vrd0] offset:17408\n"
    "ds_read_b64_tr_b16 a[88:89], %[vrd1] offset:16384\n"
    "ds_read_b64_tr_b16 a[90:91], %[vrd1] offset:17408\n"
    "ds_read_b64_tr_b16 a[12:13], %[vrd0] offset:24576\n"
    "ds_read_b64_tr_b16 a[14:15], %[vrd0] offset:25600\n"
    "ds_read_b64_tr_b16 a[92:93], %[vrd1] offset:24576\n"
    "ds_read_b64_tr_b16 a[94:95], %[vrd1] offset:25600\n"
    "s_waitcnt vmcnt(16)\n"
    "s_waitcnt lgkmcnt(0)\n"
    "s_barrier\n"
    "s_add_u32 m0, s41, 0\n"
    "s_nop 0\n"
    "global_load_lds_dwordx4 %[vw], s[44:45] offset:0 nt\n"
    "global_load_lds_dwordx4 v232, s[44:45] offset:1024 nt\n"
    "s_add_u32 s44, s44, 262144\n"
    "s_addc_u32 s45, s45, 0\n"
    "s_add_u32 m0, s41, 8192\n"
    "s_nop 0\n"
    "global_load_lds_dwordx4 %[vw], s[44:45] offset:0 nt\n"
    "global_load_lds_dwordx4 v232, s[44:45] offset:1024 nt\n"
    "s_add_u32 s44, s44, 262144\n"
    "s_addc_u32 s45, s45, 0\n"
    "s_add_u32 m0, s41, 16384\n"
    "s_nop 0\n"
    "global_load_lds_dwordx4 %[vw], s[44:45] offset:0 nt\n"
    "global_load_lds_dwordx4 v232, s[44:45] offset:1024 nt\n"
    "s_add_u32 s44, s44, 262144\n"
    "s_addc_u32 s45, s45, 0\n"
    "s_add_u32 m0, s41, 24576\n"
    "s_nop 0\n"
    "global_load_lds_dwordx4 %[vw], s[44:45] offset:0 nt\n"
    "global_load_lds_dwordx4 v232, s[44:45] offset:1024 nt\n"
    "s_add_u32 s44, s44, 262144\n"
    "s_addc_u32 s45, s45, 0\n"
    "ds_read_b64_tr_b16 a[16:17], %[vrd0] offset:32768\n"
    "ds_read_b64_tr_b16 a[18:19], %[vrd0] offset:33792\n"
    "ds_read_b64_tr_b16 a[96:97], %[vrd1] offset:32768\n"
    "ds_read_b64_tr_b16 a[98:99], %[vrd1] offset:33792\n"
    "ds_read_b64_tr_b16 a[20:21], %[vrd0] offset:40960\n"
    "ds_read_b64_tr_b16 a[22:23], %[vrd0] offset:41984\n"
    "ds_read_b64_tr_b16 a[100:101], %[vrd1] offset:40960\n"
    "ds_read_b64_tr_b16 a[102:103], %[vrd1] offset:41984\n"
    "ds_read_b64_tr_b16 a[24:25], %[vrd0] offset:49152\n"
    "ds_read_b64_tr_b16 a[26:27], %[vrd0] offset:50176\n"
    "ds_read_b64_tr_b16 a[104:105], %[vrd1] offset:49152\n"
    "ds_read_b64_tr_b16 a[106:107], %[vrd1] offset:50176\n"
    "ds_read_b64_tr_b16 a[28:29], %[vrd0] offset:57344\n"
    "ds_read_b64_tr_b16 a[30:31], %[vrd0] offset:58368\n"
    "ds_read_b64_tr_b16 a[108:109], %[vrd1] offset:57344\n"
    "ds_read_b64_tr_b16 a[110:111], %[vrd1] offset:58368\n"
    "s_waitcnt vmcnt(16)\n"
    "s_waitcnt lgkmcnt(0)\n"
    "s_barrier\n"
    "s_mov_b64 s[44:45], %[wk]\n"
    "s_add_u32 m0, s41, 32768\n"
    "s_nop 0\n"
    "global_load_lds_dwordx4 %[vw], s[44:45] offset:0 nt\n"
    "global_load_lds_dwordx4 v232, s[44:45] offset:1024 nt\n"
    "s_add_u32 s44, s44, 262144\n"
    "s_addc_u32 s45, s45, 0\n"
    "s_add_u32 m0, s41, 40960\n"
    "s_nop 0\n"
    "global_load_lds_dwordx4 %[vw], s[44:45] offset:0 nt\n"
    "global_load_lds_dwordx4 v232, s[44:45] offset:1024 nt\n"
    "s_add_u32 s44, s44, 262144\n"
    "s_addc_u32 s45, s45, 0\n"
    "s_add_u32 m0, s41, 49152\n"
    "s_nop 0\n"
    "global_load_lds_dwordx4 %[vw], s[44:45] offset:0 nt\n"
    "global_load_lds_dwordx4 v232, s[44:45] offset:1024 nt\n"
    "s_add_u32 s44, s44, 262144\n"
    "s_addc_u32 s45, s45, 0\n"
    "s_add_u32 m0, s41, 57344\n"
    "s_nop 0\n"
    "global_load_lds_dwordx4 %[vw], s[44:45] offset:0 nt\n"
    "global_load_lds_dwordx4 v232, s[44:45] offset:1024 nt\n"
    "s_add_u32 s44, s44, 262144\n"
    "s_addc_u32 s45, s45, 0\n"
    "ds_read_b64_tr_b16 a[32:33], v233 offset:0\n"
    "ds_read_b64_tr_b16 a[34:35], v233 offset:1024\n"
    "ds_read_b64_tr_b16 a[112:113], v234 offset:0\n"
    "ds_read_b64_tr_b16 a[114:115], v234 offset:1024\n"
    "ds_read_b64_tr_b16 a[36:37], v233 offset:8192\n"
    "ds_read_b64_tr_b16 a[38:39], v233 offset:9216\n"
    "ds_read_b64_tr_b16 a[116:117], v234 offset:8192\n"
    "ds_read_b64_tr_b16 a[118:119], v234 offset:9216\n"
    "ds_read_b64_tr_b16 a[40:41], v233 offset:16384\n"
    "ds_read_b64_tr_b16 a[42:43], v233 offset:17408\n"
    "ds_read_b64_tr_b16 a[120:121], v234 offset:16384\n"
    "ds_read_b64_tr_b16 a[122:123], v234 offset:17408\n"
    "ds_read_b64_tr_b16 a[44:45], v233 offset:24576\n"
    "ds_read_b64_tr_b16 a[46:47], v233 offset:25600\n"
    "ds_read_b64_tr_b16 a[124:125], v234 offset:24576\n"
    "ds_read_b64_tr_b16 a[126:127], v234 offset:25600\n"
    "s_waitcnt vmcnt(16)\n"
    "s_waitcnt lgkmcnt(0)\n"
    "s_barrier\n"
    "s_add_u32 m0, s41, 65536\n"
    "s_nop 0\n"
    "global_load_lds_dwordx4 %[vw], s[44:45] offset:0 nt\n"
    "global_load_lds_dwordx4 v232, s[44:45] offset:1024 nt\n"
    "s_add_u32 s44, s44, 262144\n"
    "s_addc_u32 s45, s45, 0\n"
    "s_add_u32 m0, s41, 73728\n"
    "s_nop 0\n"
    "global_load_lds_dwordx4 %[vw], s[44:45] offset:0 nt\n"
    "global_load_lds_dwordx4 v232, s[44:45] offset:1024 nt\n"
    "s_add_u32 s44, s44, 262144\n"
    "s_addc_u32 s45, s45, 0\n"
    "s_add_u32 m0, s41, 81920\n"
    "s_nop 0\n"
    "global_load_lds_dwordx4 %[vw], s[44:45] offset:0 nt\n"
    "global_load_lds_dwordx4 v232, s[44:45] offset:1024 nt\n"
    "s_add_u32 s44, s44, 262144\n"
    "s_addc_u32 s45, s45, 0\n"
    "s_add_u32 m0, s41, 90112\n"
    "s_nop 0\n"
    "global_load_lds_dwordx4 %[vw], s[44:45] offset:0 nt\n"
    "global_load_lds_dwordx4 v232, s[44:45] offset:1024 nt\n"
    "s_add_u32 s44, s44, 262144\n"
    "s_addc_u32 s45, s45, 0\n"
    "ds_read_b64_tr_b16 a[48:49], v233 offset:32768\n"
    "ds_read_b64_tr_b16 a[50:51], v233 offset:33792\n"
    "ds_read_b64_tr_b16 a[128:129], v234 offset:32768\n"
    "ds_read_b64_tr_b16 a[130:131], v234 offset:33792\n"
    "ds_read_b64_tr_b16 a[52:53], v233 offset:40960\n"
    "ds_read_b64_tr_b16 a[54:55], v233 offset:41984\n"
    "ds_read_b64_tr_b16 a[132:133], v234 offset:40960\n"
    "ds_read_b64_tr_b16 a[134:135], v234 offset:41984\n"
    "ds_read_b64_tr_b16 a[56:57], v233 offset:49152\n"
    "ds_read_b64_tr_b16 a[58:59], v233 offset:50176\n"
    "ds_read_b64_tr_b16 a[136:137], v234 offset:49152\n"
    "ds_read_b64_tr_b16 a[138:139], v234 offset:50176\n"
    "ds_read_b64_tr_b16 a[60:61], v233 offset:57344\n"
    "ds_read_b64_tr_b16 a[62:63], v233 offset:58368\n"
    "ds_read_b64_tr_b16 a[140:141], v234 offset:57344\n"
    "ds_read_b64_tr_b16 a[142:143], v234 offset:58368\n"
    "s_waitcnt vmcnt(16)\n"
    "s_waitcnt lgkmcnt(0)\n"
    "s_barrier\n"
    "s_add_u32 m0, s41, 98304\n"
    "s_nop 0\n"
    "global_load_lds_dwordx4 %[vw], s[44:45] offset:0 nt\n"
    "global_load_lds_dwordx4 v232, s[44:45] offset:1024 nt\n"
    "s_add_u32 s44, s44, 262144\n"
    "s_addc_u32 s45, s45, 0\n"
    "s_add_u32 m0, s41, 106496\n"
    "s_nop 0\n"
    "global_load_lds_dwordx4 %[vw], s[44:45] offset:0 nt\n"
    "global_load_lds_dwordx4 v232, s[44:45] offset:1024 nt\n"
    "s_add_u32 s44, s44, 262144\n"
    "s_addc_u32 s45, s45, 0\n"
    "s_add_u32 m0, s41, 114688\n"
    "s_nop 0\n"
    "global_load_lds_dwordx4 %[vw], s[44:45] offset:0 nt\n"
    "global_load_lds_dwordx4 v232, s[44:45] offset:1024 nt\n"
    "s_add_u32 s44, s44, 262144\n"
    "s_addc_u32 s45, s45, 0\n"
    "s_add_u32 m0, s41, 122880\n"
    "s_nop 0\n"
    "global_load_lds_dwordx4 %[vw], s[44:45] offset:0 nt\n"
    "global_load_lds_dwordx4 v232, s[44:45] offset:1024 nt\n"
    "s_add_u32 s44, s44, 262144\n"
    "s_addc_u32 s45, s45, 0\n"
    "ds_read_b64_tr_b16 a[64:65], %[vrd0] offset:0\n"
    "ds_read_b64_tr_b16 a[66:67], %[vrd0] offset:1024\n"
    "ds_read_b64_tr_b16 a[144:145], %[vrd1] offset:0\n"
    "ds_read_b64_tr_b16 a[146:147], %[vrd1] offset:1024\n"
    "ds_read_b64_tr_b16 a[68:69], %[vrd0] offset:8192\n"
    "ds_read_b64_tr_b16 a[70:71], %[vrd0] offset:9216\n"
    "ds_read_b64_tr_b16 a[148:149], %[vrd1] offset:8192\n"
    "ds_read_b64_tr_b16 a[150:151], %[vrd1] offset:9216\n"
    "ds_read_b64_tr_b16 a[72:73], %[vrd0] offset:16384\n"
    "ds_read_b64_tr_b16 a[74:75], %[vrd0] offset:17408\n"
    "ds_read_b64_tr_b16 a[152:153], %[vrd1] offset:16384\n"
    "ds_read_b64_tr_b16 a[154:155], %[vrd1] offset:17408\n"
    "ds_read_b64_tr_b16 a[76:77], %[vrd0] offset:24576\n"
    "ds_read_b64_tr_b16 a[78:79], %[vrd0] offset:25600\n"
    "ds_read_b64_tr_b16 a[156:157], %[vrd1] offset:24576\n"
    "ds_read_b64_tr_b16 a[158:159], %[vrd1] offset:25600\n"
    "s_waitcnt vmcnt(16)\n"
    "s_waitcnt lgkmcnt(0)\n"
    "s_barrier\n"
    "s_add_u32 m0, s41, 0\n"
    "s_nop 0\n"
    "global_load_lds_dwordx4 %[vw], s[44:45] offset:0 nt\n"
    "global_load_lds_dwordx4 v232, s[44:45] offset:1024 nt\n"
    "s_add_u32 s44, s44, 262144\n"
    "s_addc_u32 s45, s45, 0\n"
    "s_add_u32 m0, s41, 8192\n"
    "s_nop 0\n"
    "global_load_lds_dwordx4 %[vw], s[44:45] offset:0 nt\n"
    "global_load_lds_dwordx4 v232, s[44:45] offset:1024 nt\n"
    "s_add_u32 s44, s44, 262144\n"
    "s_addc_u32 s45, s45, 0\n"
    "s_add_u32 m0, s41, 16384\n"
    "s_nop 0\n"
    "global_load_lds_dwordx4 %[vw], s[44:45] offset:0 nt\n"
    "global_load_lds_dwordx4 v232, s[44:45] offset:1024 nt\n"
    "s_add_u32 s44, s44, 262144\n"
    "s_addc_u32 s45, s45, 0\n"
    "s_add_u32 m0, s41, 24576\n"
    "s_nop 0\n"
    "global_load_lds_dwordx4 %[vw], s[44:45] offset:0 nt\n"
    "global_load_lds_dwordx4 v232, s[44:45] offset:1024 nt\n"
    "s_add_u32 s44, s44, 262144\n"
    "s_addc_u32 s45, s45, 0\n"
    "ds_read_b64_tr_b16 a[160:161], %[vrd0] offset:32768\n"
    "ds_read_b64_tr_b16 a[162:163], %[vrd0] offset:33792\n"
    "ds_read_b64_tr_b16 a[208:209], %[vrd1] offset:32768\n"
    "ds_read_b64_tr_b16 a[210:211], %[vrd1] offset:33792\n"
    "ds_read_b64_tr_b16 a[164:165], %[vrd0] offset:40960\n"
    "ds_read_b64_tr_b16 a[166:167], %[vrd0] offset:41984\n"
    "ds_read_b64_tr_b16 a[212:213], %[vrd1] offset:40960\n"
    "ds_read_b64_tr_b16 a[214:215], %[vrd1] offset:41984\n"
    "ds_read_b64_tr_b16 a[168:169], %[vrd0] offset:49152\n"
    "ds_read_b64_tr_b16 a[170:171], %[vrd0] offset:50176\n"
    "ds_read_b64_tr_b16 a[216:217], %[vrd1] offset:49152\n"
    "ds_read_b64_tr_b16 a[218:219], %[vrd1] offset:50176\n"
    "ds_read_b64_tr_b16 a[172:173], %[vrd0] offset:57344\n"
    "ds_read_b64_tr_b16 a[174:175], %[vrd0] offset:58368\n"
    "ds_read_b64_tr_b16 a[220:221], %[vrd1] offset:57344\n"
    "ds_read_b64_tr_b16 a[222:223], %[vrd1] offset:58368\n"
    "s_waitcnt vmcnt(16)\n"
    "s_waitcnt lgkmcnt(0)\n"
    "s_barrier\n"
    "s_add_u32 m0, s41, 32768\n"
    "s_nop 0\n"
    "global_load_lds_dwordx4 %[vw], s[44:45] offset:0 nt\n"
    "global_load_lds_dwordx4 v232, s[44:45] offset:1024 nt\n"
    "s_add_u32 s44, s44, 262144\n"
    "s_addc_u32 s45, s45, 0\n"
    "s_add_u32 m0, s41, 40960\n"
    "s_nop 0\n"
    "global_load_lds_dwordx4 %[vw], s[44:45] offset:0 nt\n"
    "global_load_lds_dwordx4 v232, s[44:45] offset:1024 nt\n"
    "s_add_u32 s44, s44, 262144\n"
    "s_addc_u32 s45, s45, 0\n"
    "s_add_u32 m0, s41, 49152\n"
    "s_nop 0\n"
    "global_load_lds_dwordx4 %[vw], s[44:45] offset:0 nt\n"
    "global_load_lds_dwordx4 v232, s[44:45] offset:1024 nt\n"
    "s_add_u32 s44, s44, 262144\n"
    "s_addc_u32 s45, s45, 0\n"
    "s_add_u32 m0, s41, 57344\n"
    "s_nop 0\n"
    "global_load_lds_dwordx4 %[vw], s[44:45] offset:0 nt\n"
    "global_load_lds_dwordx4 v232, s[44:45] offset:1024 nt\n"
    "s_add_u32 s44, s44, 262144\n"
    "s_addc_u32 s45, s45, 0\n"
    "ds_read_b64_tr_b16 a[176:177], v233 offset:0\n"
    "ds_read_b64_tr_b16 a[178:179], v233 offset:1024\n"
    "ds_read_b64_tr_b16 a[224:225], v234 offset:0\n"
    "ds_read_b64_tr_b16 a[226:227], v234 offset:1024\n"
    "ds_read_b64_tr_b16 a[180:181], v233 offset:8192\n"
    "ds_read_b64_tr_b16 a[182:183], v233 offset:9216\n"
    "ds_read_b64_tr_b16 a[228:229], v234 offset:8192\n"
    "ds_read_b64_tr_b16 a[230:231], v234 offset:9216\n"
    "ds_read_b64_tr_b16 a[184:185], v233 offset:16384\n"
    "ds_read_b64_tr_b16 a[186:187], v233 offset:17408\n"
    "ds_read_b64_tr_b16 a[232:233], v234 offset:16384\n"
    "ds_read_b64_tr_b16 a[234:235], v234 offset:17408\n"
    "ds_read_b64_tr_b16 a[188:189], v233 offset:24576\n"
    "ds_read_b64_tr_b16 a[190:191], v233 offset:25600\n"
    "ds_read_b64_tr_b16 a[236:237], v234 offset:24576\n"
    "ds_read_b64_tr_b16 a[238:239], v234 offset:25600\n"
    "s_waitcnt vmcnt(16)\n"
    "s_waitcnt lgkmcnt(0)\n"
    "s_barrier\n"
    "ds_read_b64_tr_b16 a[192:193], v233 offset:32768\n"
    "ds_read_b64_tr_b16 a[194:195], v233 offset:33792\n"
    "ds_read_b64_tr_b16 a[240:241], v234 offset:32768\n"
    "ds_read_b64_tr_b16 a[242:243], v234 offset:33792\n"
    "ds_read_b64_tr_b16 a[196:197], v233 offset:40960\n"
    "ds_read_b64_tr_b16 a[198:199], v233 offset:41984\n"
    "ds_read_b64_tr_b16 a[244:245], v234 offset:40960\n"
    "ds_read_b64_tr_b16 a[246:247], v234 offset:41984\n"
    "ds_read_b64_tr_b16 a[200:201], v233 offset:49152\n"
    "ds_read_b64_tr_b16 a[202:203], v233 offset:50176\n"
    "ds_read_b64_tr_b16 a[248:249], v234 offset:49152\n"
    "ds_read_b64_tr_b16 a[250:251], v234 offset:50176\n"
    "ds_read_b64_tr_b16 a[204:205], v233 offset:57344\n"
    "ds_read_b64_tr_b16 a[206:207], v233 offset:58368\n"
    "ds_read_b64_tr_b16 a[252:253], v234 offset:57344\n"
    "ds_read_b64_tr_b16 a[254:255], v234 offset:58368\n"
    "s_waitcnt vmcnt(8)\n"
    "s_waitcnt lgkmcnt(0)\n"
    "s_barrier\n"
    "ds_read_b64_tr_b16 v[56:57], %[vrd0] offset:0\n"
    "ds_read_b64_tr_b16 v[58:59], %[vrd0] offset:1024\n"
    "ds_read_b64_tr_b16 v[88:89], %[vrd1] offset:0\n"
    "ds_read_b64_tr_b16 v[90:91], %[vrd1] offset:1024\n"
    "ds_read_b64_tr_b16 v[60:61], %[vrd0] offset:8192\n"
    "ds_read_b64_tr_b16 v[62:63], %[vrd0] offset:9216\n"
    "ds_read_b64_tr_b16 v[92:93], %[vrd1] offset:8192\n"
    "ds_read_b64_tr_b16 v[94:95], %[vrd1] offset:9216\n"
    "ds_read_b64_tr_b16 v[64:65], %[vrd0] offset:16384\n"
    "ds_read_b64_tr_b16 v[66:67], %[vrd0] offset:17408\n"
    "ds_read_b64_tr_b16 v[96:97], %[vrd1] offset:16384\n"
    "ds_read_b64_tr_b16 v[98:99], %[vrd1] offset:17408\n"
    "ds_read_b64_tr_b16 v[68:69], %[vrd0] offset:24576\n"
    "ds_read_b64_tr_b16 v[70:71], %[vrd0] offset:25600\n"
    "ds_read_b64_tr_b16 v[100:101], %[vrd1] offset:24576\n"
    "ds_read_b64_tr_b16 v[102:103], %[vrd1] offset:25600\n"
    "s_waitcnt vmcnt(0)\n"
    "s_waitcnt lgkmcnt(0)\n"
    "s_barrier\n"
    "ds_read_b64_tr_b16 v[72:73], %[vrd0] offset:32768\n"
    "ds_read_b64_tr_b16 v[74:75], %[vrd0] offset:33792\n"
    "ds_read_b64_tr_b16 v[104:105], %[vrd1] offset:32768\n"
    "ds_read_b64_tr_b16 v[106:107], %[vrd1] offset:33792\n"
    "ds_read_b64_tr_b16 v[76:77], %[vrd0] offset:40960\n"
    "ds_read_b64_tr_b16 v[78:79], %[vrd0] offset:41984\n"
    "ds_read_b64_tr_b16 v[108:109], %[vrd1] offset:40960\n"
    "ds_read_b64_tr_b16 v[110:111], %[vrd1] offset:41984\n"
    "ds_read_b64_tr_b16 v[80:81], %[vrd0] offset:49152\n"
    "ds_read_b64_tr_b16 v[82:83], %[vrd0] offset:50176\n"
    "ds_read_b64_tr_b16 v[112:113], %[vrd1] offset:49152\n"
    "ds_read_b64_tr_b16 v[114:115], %[vrd1] offset:50176\n"
    "ds_read_b64_tr_b16 v[84:85], %[vrd0] offset:57344\n"
    "ds_read_b64_tr_b16 v[86:87], %[vrd0] offset:58368\n"
    "ds_read_b64_tr_b16 v[116:117], %[vrd1] offset:57344\n"
    "ds_read_b64_tr_b16 v[118:119], %[vrd1] offset:58368\n"
    "s_waitcnt lgkmcnt(0)\n"
    "s_mov_b64 s[44:45], %[ws]\n"
    "s_lshl_b32 s41, %[h], 6\n"
    "s_add_u32 s90, s41, 1376256\n"
    "s_lshl_b32 s41, %[h], 7\n"
    "s_add_u32 s41, s41, 2117632\n"
    "v_mov_b32 v229, s41\n"
    "s_mov_b32 s67, 800\n"
    "s_mov_b32 s68, 568\n"
    "s_mov_b32 s39, 13\n"
    "s_add_u32 s69, s67, %[da]\n"
    "s_add_u32 s69, s69, s39\n"
    "s_lshr_b32 s69, s69, 3\n"
    "s_add_u32 s35, s68, %[db]\n"
    "s_add_u32 s35, s35, s39\n"
    "s_lshr_b32 s35, s35, 3\n"
    "s_add_u32 s35, s35, s69\n"
    "s_lshl_b32 s41, %[h], 14\n"
    "s_lshl_b32 s92, %[w], 11\n"
    "s_add_u32 s41, s41, s92\n"
    "s_add_u32 s41, s41, 20971520\n"
    "s_add_u32 s94, s44, s41\n"
    "s_addc_u32 s95, s45, 0\n"
    "s_add_u32 s86, s92, %[lb]\n"
    "s_add_u32 s87, s86, 16384\n"
    "s_lshr_b32 s41, %[h], 1\n"
    "s_lshl_b32 s41, s41, 14\n"
    "s_and_b32 s92, %[h], 1\n"
    "s_lshl_b32 s92, s92, 12\n"
    "s_add_u32 s41, s41, s92\n"
    "s_lshl_b32 s92, %[w], 10\n"
    "s_add_u32 s41, s41, s92\n"
    "s_add_u32 s41, s41, 121634816\n"
    "s_and_b32 s91, %[w], 1\n"
    "s_lshl_b32 s91, s91, 18\n"
    "s_add_u32 s41, s41, s91\n"
    "s_add_u32 s96, s44, s41\n"
    "s_addc_u32 s97, s45, 0\n"
    "s_add_u32 s84, s92, %[lb]\n"
    "s_lshr_b32 s41, %[h], 1\n"
    "s_lshl_b32 s41, s41, 14\n"
    "s_add_u32 s93, s41, s92\n"
    "s_and_b32 s41, %[h], 1\n"
    "s_lshl_b32 s41, s41, 12\n"
    "s_add_u32 s93, s93, s41\n"
    "s_add_u32 s93, s93, 1392640\n"
    "s_lshl_b32 s92, %[w], 9\n"
    "s_add_u32 s85, s92, %[lb]\n"
    "s_add_u32 s85, s85, 8192\n"
    "s_mov_b32 s40, 0\n"
    "s_mov_b32 s88, 0\n"
    "s_mov_b32 s42, 0\n"
    "s_mov_b32 s43, 0\n"
    "v_add_u32 v230, 1024, %[vhv]\n"
    "v_mov_b32 v231, %[vhv]\n"
    "s_mov_b32 exec_lo, 0\n"
    "v_and_b32 v231, 0xfffffdff, %[vhv]\n"
    "v_add_u32 v231, 2048, v231\n"
    "s_mov_b32 exec_lo, -1\n"
    "v_mov_b32 %[vrd0], 0\n"
    "v_lshrrev_b32 %[vdg], 1, %[vg16]\n"
    "v_mov_b32 v212, 0x3f803f80\n"
    "v_mov_b32 v213, 0x3f803f80\n"
    "v_mov_b32 v214, 0x3f803f80\n"
    "v_mov_b32 v215, 0x3f803f80\n"
    "v_mov_b32 v216, 0x3f003f00\n"
    "v_mov_b32 v217, 0x3f003f00\n"
    "v_lshrrev_b32 %[vrd1], 1, %[vg16]\n"
    "v_add_u32 %[vrd1], s90, %[vrd1]\n"
    "v_mov_b32 v218, 0xd028965f\n"
    "v_mov_b32 v219, 0xbe0293ee\n"
    "s_waitcnt lgkmcnt(0)\n"
    "s_add_u32 s46, s46, 1\n"
    "s_and_b32 s47, s47, -8\n"
    "v_lshlrev_b32 v236, 3, %[vg16]\n"
    "v_min_u32 v236, 896, v236\n"
    "v_add_u32 v236, 2146304, v236\n"
    "s_branch Ainb_%=\n"
    "Ain_%=:\n"
    "s_sleep 4\n"
    "s_add_u32 s42, s42, 1\n"
    "s_cmp_gt_u32 s42, 524288\n"
    "s_cbranch_scc1 Adead_%=\n"
    "Ainb_%=:\n"
    "global_load_dwordx2 v[232:233], v236, %[ws] offset:0 sc1\n"
    "s_waitcnt vmcnt(0)\n"
    "v_mov_b32 v234, v233\n"
    "v_and_b32 v233, -8, v233\n"
    "v_cmp_ne_u64 vcc, s[46:47], v[232:233]\n"
    "s_cbranch_vccnz Ain_%=\n"
    "v_readlane_b32 s41, v234, 1\n"
    "s_nop 4\n"
    "s_and_b32 s41, s41, 1\n"
    "s_lshl_b32 s41, s41, 13\n"
    "s_add_u32 s44, s44, s41\n"
    "s_addc_u32 s45, s45, 0\n"
    "s_xor_b32 s41, s41, 8192\n"
    "s_add_u32 s94, s94, s41\n"
    "s_addc_u32 s95, s95, 0\n"
    "s_add_u32 s96, s96, s41\n"
    "s_addc_u32 s97, s97, 0\n"
    "s_add_u32 s46, s44, 1048576\n"
    "s_addc_u32 s47, s45, 0\n"
    "s_add_u32 s48, s46, 4096\n"
    "s_addc_u32 s49, s47, 0\n"
    "s_barrier\n"
    "s_mov_b32 s91, 1\n"
    "Af0_%=:\n"
    "v_cmp_gt_u32 vcc, s91, %[vrd0]\n"
    "s_cbranch_vccz Af0d_%=\n"
    "s_add_u32 s42, s42, 1\n"
    "s_cmp_gt_u32 s42, 524288\n"
    "s_cbranch_scc1 Adead_%=\n"
    "global_load_dword %[vrd0], %[vflv], %[ws] sc1\n"
    "s_waitcnt vmcnt(0)\n"
    "s_branch Af0_%=\n"
    "Af0d_%=:\n"
    "s_mov_b64 s[50:51], s[94:95]\n"
    "s_add_u32 s54, s50, 33554432\n"
    "s_addc_u32 s55, s51, 0\n"
    "s_add_u32 s100, s50, 67108864\n"
    "s_addc_u32 s101, s51, 0\n"
    "s_mov_b32 s86, s87\n"
    "s_add_u32 m0, s86, 0\n"
    "s_nop 0\n"
    "global_load_lds_dwordx4 %[vg16], s[54:55] offset:0 sc1\n"
    "global_load_lds_dwordx4 %[vg16], s[54:55] offset:1024 sc1\n"
    "s_add_u32 m0, s86, 8192\n"
    "s_nop 0\n"
    "global_load_lds_dwordx4 %[vg16], s[100:101] offset:0 sc1\n"
    "global_load_lds_dwordx4 %[vg16], s[100:101] offset:1024 sc1\n"
    "s_add_u32 m0, s86, 16384\n"
    "s_nop 0\n"
    "global_load_lds_dwordx4 %[vg16], s[50:51] offset:0 sc1\n"
    "global_load_lds_dwordx4 %[vg16], s[50:51] offset:1024 sc1\n"
    "s_cmp_eq_u32 %[w], 0\n"
    "s_cbranch_scc1 An2_%=\n"
    "s_waitcnt vmcnt(0)\n"
    "An2_%=:\n"
    "s_cmp_lg_u32 %[w], 0\n"
    "s_cbranch_scc1 Aloop_%=\n"
    "s_branch Ab1_%=\n"
    "Ab0_%=:\n"
    "s_sleep 4\n"
    "Ab1_%=:\n"
    "global_load_dwordx2 v[220:221], %[vhv], s[44:45] offset:0 sc1\n"
    "global_load_dwordx2 v[160:161], v230, s[44:45] offset:0 sc1\n"
    "global_load_dwordx2 v[162:163], v231, s[44:45] offset:0 sc1\n"
    "s_add_u32 s42, s42, 1\n"
    "s_cmp_gt_u32 s42, 524288\n"
    "s_cbranch_scc1 Adead_%=\n"
    "s_waitcnt vmcnt(0)\n"
    "v_min3_u32 v220, v220, v160, v162\n"
    "v_cmp_eq_u32 vcc, 0, v220\n"
    "s_cmp_lg_u32 vcc_hi, 0\n"
    "s_cbranch_scc1 Ab0_%=\n"
    "Aloop_%=:\n"
    "s_lshl_b32 s92, %[w], 11\n"
    "v_add_u32 %[vw], s88, %[vlm]\n"
    "v_add_u32 v247, s92, %[vw]\n"
    "v_mov_b32 v228, s40\n"
    "v_mov_b64 v[196:197], 0\n"
    "v_mov_b64 v[198:199], 0\n"
    "v_mov_b64 v[200:201], 0\n"
    "v_mov_b64 v[202:203], 0\n"
    "v_mov_b64 v[204:205], 0\n"
    "v_mov_b64 v[206:207], 0\n"
    "v_mov_b64 v[208:209], 0\n"
    "v_mov_b64 v[210:211], 0\n"
    "s_cmp_lg_u32 %[w], 0\n"
    "s_cbranch_scc1 Arel_%=\n"
    "s_cmp_eq_u32 s40, 0\n"
    "s_cbranch_scc1 Afirst_%=\n"
    "v_cmp_gt_u32 vcc, s41, v220\n"
    "s_cbranch_vccnz Ahp_%=\n"
    "Ahc_%=:\n"
    "v_cmp_lt_u32 vcc, s41, v160\n"
    "v_readlane_b32 s38, v221, 0\n"
    "v_readlane_b32 s63, v221, 32\n"
    "v_readlane_b32 s92, v221, 48\n"
    "v_readlane_b32 s36, v221, 16\n"
    "s_max_i32 s63, s63, s92\n"
    "s_add_u32 s37, s63, s43\n"
    "s_add_u32 s36, s36, s43\n"
    "s_sub_u32 s63, s63, s38\n"
    "s_add_u32 s38, s38, s43\n"
    "s_and_b32 vcc_lo, vcc_lo, 0xffff\n"
    "s_cselect_b32 s92, 2, s63\n"
    "s_cmp_gt_i32 s92, 1\n"
    "s_cbranch_scc1 E648a_%=\n"
    "E648b_%=:\n"
    "s_add_u32 s43, s38, s35\n"
    "s_sub_u32 s43, s43, 4\n"
    "Afw_%=:\n"
    "s_memrealtime s[64:65]\n"
    "Afwl_%=:\n"
    "s_memrealtime vcc\n"
    "s_waitcnt lgkmcnt(1)\n"
    "s_sub_u32 s62, s43, s64\n"
    "s_cmp_ge_u32 s62, 20000\n"
    "s_cbranch_scc1 Afwy_%=\n"
    "s_memrealtime s[64:65]\n"
    "s_waitcnt lgkmcnt(1)\n"
    "s_sub_u32 s62, s43, vcc_lo\n"
    "s_cmp_ge_u32 s62, 20000\n"
    "s_cbranch_scc1 Afwy_%=\n"
    "s_memrealtime vcc\n"
    "s_waitcnt lgkmcnt(1)\n"
    "s_sub_u32 s62, s43, s64\n"
    "s_cmp_ge_u32 s62, 20000\n"
    "s_cbranch_scc1 Afwy_%=\n"
    "s_memrealtime s[64:65]\n"
    "s_waitcnt lgkmcnt(1)\n"
    "s_sub_u32 s62, s43, vcc_lo\n"
    "s_cmp_ge_u32 s62, 20000\n"
    "s_cbranch_scc1 Afwy_%=\n"
    "s_memrealtime vcc\n"
    "s_waitcnt lgkmcnt(1)\n"
    "s_sub_u32 s62, s43, s64\n"
    "s_cmp_ge_u32 s62, 20000\n"
    "s_cbranch_scc1 Afwy_%=\n"
    "s_memrealtime s[64:65]\n"
    "s_waitcnt lgkmcnt(1)\n"
    "s_sub_u32 s62, s43, vcc_lo\n"
    "s_cmp_ge_u32 s62, 20000\n"
    "s_cbranch_scc1 Afwy_%=\n"
    "s_memrealtime vcc\n"
    "s_waitcnt lgkmcnt(1)\n"
    "s_sub_u32 s62, s43, s64\n"
    "s_cmp_ge_u32 s62, 20000\n"
    "s_cbranch_scc1 Afwy_%=\n"
    "s_memrealtime s[64:65]\n"
    "s_waitcnt lgkmcnt(1)\n"
    "s_sub_u32 s62, s43, vcc_lo\n"
    "s_cmp_ge_u32 s62, 20000\n"
    "s_cbranch_scc1 Afwy_%=\n"
    "s_branch Afwl_%=\n"
    "Afwy_%=:\n"
    "s_cmp_lt_u32 s62, -3\n"
    "s_cbranch_scc1 Afwx_%=\n"
    "s_nop 2\n"
    "s_cmp_lt_u32 s62, -2\n"
    "s_cbranch_scc1 Afwx_%=\n"
    "s_nop 2\n"
    "s_cmp_lt_u32 s62, -1\n"
    "s_cbranch_scc1 Afwx_%=\n"
    "s_nop 2\n"
    "s_branch Afwx_%=\n"
    "Afwx_%=:\n"
    "s_waitcnt lgkmcnt(0)\n"
    "s_sub_u32 s43, s43, s62\n"
    "s_max_u32 s62, s62, -4\n"
    "s_add_u32 s43, s43, s62\n"
    "s_add_u32 s43, s43, 4\n"
    "Arel_%=:\n"
    "s_waitcnt vmcnt(3)\n"
    "s_barrier\n"
    "s_mov_b32 m0, s84\n"
    "s_nop 0\n"
    "global_load_lds_dwordx4 %[vmv0], s[46:47] offset:0 sc1\n"
    "s_add_u32 m0, s84, 4096\n"
    "s_nop 0\n"
    "global_load_lds_dwordx4 %[vmv0], s[48:49] offset:0 sc1\n"
    "s_mov_b32 m0, s85\n"
    "s_mov_b32 exec_hi, 0\n"
    "global_load_lds_dwordx4 %[vmv2], s[46:47] offset:0 sc1\n"
    "s_mov_b32 exec_hi, -1\n"
    "s_memrealtime s[64:65]\n"
    "s_lshl1_add_u32 s41, s40, 2\n"
    "s_and_b32 s92, s40, 1\n"
    "s_lshl_b32 s92, s92, 4\n"
    "s_add_u32 s60, s44, s92\n"
    "s_addc_u32 s61, s45, 0\n"
    "s_waitcnt lgkmcnt(0)\n"
    "s_sub_u32 s92, s64, s43\n"
    "s_sub_u32 s92, s92, 6\n"
    "s_cmp_gt_i32 s92, 4\n"
    "s_cselect_b32 s92, s92, 0\n"
    "s_add_u32 s43, s43, s92\n"
    "v_mov_b32 v222, s41\n"
    "v_mov_b32 v223, s43\n"
    "s_cmp_lg_u32 %[w], 0\n"
    "s_cbranch_scc1 Anohint_%=\n"
    "v_writelane_b32 v223, s90, 1\n"
    "s_mov_b64 exec, 3\n"
    "global_store_dwordx2 %[vrd1], v[222:223], s[60:61] offset:0 sc1\n"
    "s_mov_b64 exec, -1\n"
    "s_branch Ahint2_%=\n"
    "Anohint_%=:\n"
    "global_load_dword v251, %[vg16], %[wq] offset:0\n"
    "Ahint2_%=:\n"
    "ds_read_b128 v[120:123], %[vw] offset:16384\n"
    "s_lshl_b32 s92, s40, 19\n"
    "ds_read_b128 v[124:127], %[vw] offset:17408\n"
    "s_add_u32 s58, s96, s92\n"
    "s_addc_u32 s59, s97, 0\n"
    "ds_read_b128 v[128:131], %[vw] offset:18432\n"
    "s_lshr_b32 s92, s92, 8\n"
    "s_and_b32 s92, s92, 8192\n"
    "s_xor_b32 s58, s58, s92\n"
    "ds_read_b128 v[132:135], %[vw] offset:19456\n"
    "s_xor_b32 s70, s58, 1024\n"
    "s_mov_b32 s71, s59\n"
    "ds_read_b128 v[136:139], %[vw] offset:20480\n"
    "s_and_b32 s92, s40, 1\n"
    "s_lshl_b32 s92, s92, 18\n"
    "s_add_u32 s92, s92, s93\n"
    "ds_read_b128 v[140:143], %[vw] offset:21504\n"
    "s_add_u32 s52, s44, s92\n"
    "s_addc_u32 s53, s45, 0\n"
    "ds_read_b128 v[144:147], %[vw] offset:22528\n"
    "s_and_b32 s92, s40, 3\n"
    "s_lshl_b32 s92, s92, 18\n"
    "s_add_u32 s92, s92, s89\n"
    "ds_read_b128 v[148:151], %[vw] offset:23552\n"
    "s_add_u32 s56, s44, s92\n"
    "s_addc_u32 s57, s45, 0\n"
    "ds_read_b128 v[184:187], v247 offset:24576\n"
    "s_xor_b32 s92, s92, 524288\n"
    "s_add_u32 s74, s44, s92\n"
    "s_addc_u32 s75, s45, 0\n"
    "ds_read_b128 v[188:191], v247 offset:25600\n"
    "s_mov_b32 s98, 0\n"
    "s_add_u32 s92, s40, 1\n"
    "s_min_u32 s92, s92, 63\n"
    "s_add_u32 s91, s92, 1\n"
    "s_lshl_b32 s92, s92, 19\n"
    "s_add_u32 s50, s94, s92\n"
    "s_addc_u32 s51, s95, 0\n"
    "s_lshr_b32 s92, s92, 8\n"
    "s_and_b32 s92, s92, 8192\n"
    "s_xor_b32 s50, s50, s92\n"
    "s_add_u32 s54, s50, 33554432\n"
    "s_addc_u32 s55, s51, 0\n"
    "s_add_u32 s100, s50, 67108864\n"
    "s_addc_u32 s101, s51, 0\n"
    "s_xor_b32 s88, s88, 32768\n"
    "s_add_u32 s86, s87, s88\n"
    "s_waitcnt lgkmcnt(0)\n"
    ".p2align 5\n"
    "s_nop 0\n"
    "s_nop 0\n"
    "s_nop 0\n"
    "s_waitcnt vmcnt(3)\n"
    "Ago_%=:\n"
    "s_barrier\n"
    "ds_read_b128 v[16:19], %[vlm] offset:0\n"
    "ds_read_b128 v[20:23], %[vlm] offset:1024\n"
    "ds_read_b128 v[24:27], %[vlm] offset:2048\n"
    "ds_read_b128 v[28:31], %[vlm] offset:3072\n"
    "s_waitcnt lgkmcnt(3)\n"
    "v_smfmac_f32_16x16x64_bf16 v[196:199], v[16:19], a[0:7], %[vidx]\n"
    "v_smfmac_f32_16x16x64_bf16 v[200:203], v[16:19], a[80:87], %[vidx]\n"
    "s_waitcnt lgkmcnt(2)\n"
    "v_smfmac_f32_16x16x64_bf16 v[196:199], v[20:23], a[8:15], %[vidx]\n"
    "v_smfmac_f32_16x16x64_bf16 v[200:203], v[20:23], a[88:95], %[vidx]\n"
    "s_waitcnt lgkmcnt(1)\n"
    "v_smfmac_f32_16x16x64_bf16 v[196:199], v[24:27], a[16:23], %[vidx]\n"
    "s_waitcnt vmcnt(1)\n"
    "s_barrier\n"
    "ds_read_b128 v[32:35], %[vlm] offset:4096\n"
    "v_smfmac_f32_16x16x64_bf16 v[200:203], v[24:27], a[96:103], %[vidx]\n"
    "s_waitcnt lgkmcnt(1)\n"
    "ds_read_b128 v[36:39], %[vlm] offset:5120\n"
    "v_smfmac_f32_16x16x64_bf16 v[196:199], v[28:31], a[24:31], %[vidx]\n"
    "ds_read_b128 v[40:43], %[vlm] offset:6144\n"
    "v_smfmac_f32_16x16x64_bf16 v[200:203], v[28:31], a[104:111], %[vidx]\n"
    "s_waitcnt lgkmcnt(2)\n"
    "v_smfmac_f32_16x16x64_bf16 v[196:199], v[32:35], a[32:39], %[vidx]\n"
    "ds_read_b128 v[44:47], %[vlm] offset:7168\n"
    "v_smfmac_f32_16x16x64_bf16 v[200:203], v[32:35], a[112:119], %[vidx]\n"
    "s_waitcnt lgkmcnt(2)\n"
    "v_smfmac_f32_16x16x64_bf16 v[196:199], v[36:39], a[40:47], %[vidx]\n"
    "ds_read_b128 v[48:51], %[vlm] offset:8192\n"
    "v_smfmac_f32_16x16x64_bf16 v[200:203], v[36:39], a[120:127], %[vidx]\n"
    "s_waitcnt lgkmcnt(2)\n"
    "v_smfmac_f32_16x16x64_bf16 v[196:199], v[40:43], a[48:55], %[vidx]\n"
    "ds_read_b128 v[52:55], %[vlm] offset:9216\n"
    "v_smfmac_f32_16x16x64_bf16 v[200:203], v[40:43], a[128:135], %[vidx]\n"
    "s_waitcnt lgkmcnt(2)\n"
    "v_smfmac_f32_16x16x64_bf16 v[196:199], v[44:47], a[56:63], %[vidx]\n"
    "v_smfmac_f32_16x16x64_bf16 v[200:203], v[44:47], a[136:143], %[vidx]\n"
    "s_waitcnt lgkmcnt(1)\n"
    "v_smfmac_f32_16x16x64_bf16 v[196:199], v[48:51], a[64:71], %[vidx]\n"
    "v_smfmac_f32_16x16x64_bf16 v[200:203], v[48:51], a[144:151], %[vidx]\n"
    "s_waitcnt lgkmcnt(0)\n"
    "v_smfmac_f32_16x16x64_bf16 v[196:199], v[52:55], a[72:79], %[vidx]\n"
    "v_smfmac_f32_16x16x64_bf16 v[200:203], v[52:55], a[152:159], %[vidx]\n"
    "s_nop 1\n"
    "v_smfmac_f32_16x16x64_bf16 v[204:207], v[16:19], a[160:167], %[vidx]\n"
    "s_nop 2\n"
    "v_smfmac_f32_16x16x64_bf16 v[208:211], v[16:19], a[208:215], %[vidx]\n"
    "s_nop 1\n"
    "v_permlane32_swap_b32 v196, v200\n"
    "v_smfmac_f32_16x16x64_bf16 v[204:207], v[20:23], a[168:175], %[vidx]\n"
    "v_permlane32_swap_b32 v197, v201\n"
    "v_smfmac_f32_16x16x64_bf16 v[208:211], v[20:23], a[216:223], %[vidx]\n"
    "v_permlane32_swap_b32 v198, v202\n"
    "v_smfmac_f32_16x16x64_bf16 v[204:207], v[24:27], a[176:183], %[vidx]\n"
    "v_permlane32_swap_b32 v199, v203\n"
    "v_smfmac_f32_16x16x64_bf16 v[208:211], v[24:27], a[224:231], %[vidx]\n"
    "v_add_f32 v196, v196, v200\n"
    "v_add_f32 v197, v197, v201\n"
    "v_smfmac_f32_16x16x64_bf16 v[204:207], v[28:31], a[184:191], %[vidx]\n"
    "v_add_f32 v198, v198, v202\n"
    "v_add_f32 v199, v199, v203\n"
    "v_smfmac_f32_16x16x64_bf16 v[208:211], v[28:31], a[232:239], %[vidx]\n"
    "v_cvt_pk_bf16_f32 v200, v196, v197\n"
    "v_cvt_pk_bf16_f32 v201, v198, v199\n"
    "v_smfmac_f32_16x16x64_bf16 v[204:207], v[32:35], a[192:199], %[vidx]\n"
    "ds_write_b64 %[viw], v[200:201] offset:0\n"
    "v_cmp_u_f32 s[64:65], v196, v197\n"
    "v_smfmac_f32_16x16x64_bf16 v[208:211], v[32:35], a[240:247], %[vidx]\n"
    "v_cmp_u_f32 vcc, v198, v199\n"
    "v_smfmac_f32_16x16x64_bf16 v[204:207], v[36:39], a[200:207], %[vidx]\n"
    "s_or_b64 vcc, vcc, s[64:65]\n"
    "v_smfmac_f32_16x16x64_bf16 v[208:211], v[36:39], a[248:255], %[vidx]\n"
    "s_cbranch_scc1 Aretry_%=\n"
    "Acont_%=:\n"
    "v_smfmac_f32_16x16x64_bf16 v[204:207], v[40:43], v[56:63], %[vidx]\n"
    "s_waitcnt lgkmcnt(0)\n"
    "s_barrier\n"
    "ds_read_b64_tr_b16 v[152:153], %[vir] offset:0\n"
    "v_smfmac_f32_16x16x64_bf16 v[208:211], v[40:43], v[88:95], %[vidx]\n"
    "ds_read_b64_tr_b16 v[154:155], %[vir] offset:256\n"
    "ds_read_b64_tr_b16 v[156:157], %[vir] offset:512\n"
    "v_smfmac_f32_16x16x64_bf16 v[204:207], v[44:47], v[64:71], %[vidx]\n"
    "ds_read_b64_tr_b16 v[158:159], %[vir] offset:768\n"
    "v_smfmac_f32_16x16x64_bf16 v[208:211], v[44:47], v[96:103], %[vidx]\n"
    "ds_read_b64_tr_b16 v[160:161], %[vir] offset:1024\n"
    "v_smfmac_f32_16x16x64_bf16 v[204:207], v[48:51], v[72:79], %[vidx]\n"
    "ds_read_b64_tr_b16 v[162:163], %[vir] offset:1280\n"
    "v_smfmac_f32_16x16x64_bf16 v[208:211], v[48:51], v[104:111], %[vidx]\n"
    "ds_read_b64_tr_b16 v[164:165], %[vir] offset:1536\n"
    "s_waitcnt lgkmcnt(5)\n"
    "v_mfma_f32_16x16x32_bf16 v[196:199], v[120:123], v[152:155], 0\n"
    "ds_read_b64_tr_b16 v[166:167], %[vir] offset:1792\n"
    "v_mfma_f32_16x16x32_bf16 v[200:203], v[136:139], v[152:155], 0\n"
    "s_waitcnt lgkmcnt(4)\n"
    "v_mfma_f32_16x16x32_bf16 v[196:199], v[124:127], v[156:159], v[196:199]\n"
    "v_mfma_f32_16x16x32_bf16 v[200:203], v[140:143], v[156:159], v[200:203]\n"
    "v_smfmac_f32_16x16x64_bf16 v[204:207], v[52:55], v[80:87], %[vidx]\n"
    "v_smfmac_f32_16x16x64_bf16 v[208:211], v[52:55], v[112:119], %[vidx]\n"
    "s_waitcnt lgkmcnt(2)\n"
    "s_waitcnt lgkmcnt(0)\n"
    "v_mfma_f32_16x16x32_bf16 v[196:199], v[128:131], v[160:163], v[196:199]\n"
    "s_nop 0\n"
    "global_load_dwordx2 v[192:193], %[vdg], s[52:53] offset:0 sc1\n"
    "s_nop 0\n"
    "v_mfma_f32_16x16x32_bf16 v[200:203], v[144:147], v[160:163], v[200:203]\n"
    "s_nop 0\n"
    "global_load_dwordx2 v[194:195], %[vdg], s[52:53] offset:512 sc1\n"
    "v_permlane32_swap_b32 v204, v208\n"
    "v_mfma_f32_16x16x32_bf16 v[196:199], v[132:135], v[164:167], v[196:199]\n"
    "v_permlane32_swap_b32 v205, v209\n"
    "v_mfma_f32_16x16x32_bf16 v[200:203], v[148:151], v[164:167], v[200:203]\n"
    "v_permlane32_swap_b32 v206, v210\n"
    "v_permlane32_swap_b32 v207, v211\n"
    "v_add_f32 v204, v204, v208\n"
    "v_add_f32 v205, v205, v209\n"
    "v_add_f32 v206, v206, v210\n"
    "v_add_f32 v207, v207, v211\n"
    "v_cvt_pk_bf16_f32 v208, v204, v205\n"
    "v_cvt_pk_bf16_f32 v209, v206, v207\n"
    "ds_write_b64 %[viw], v[208:209] offset:2048\n"
    "v_cndmask_b32 v196, v200, v196, s[72:73]\n"
    "v_cndmask_b32 v197, v201, v197, s[72:73]\n"
    "v_cndmask_b32 v198, v202, v198, s[72:73]\n"
    "v_cndmask_b32 v199, v203, v199, s[72:73]\n"
    "v_max3_f32 v248, v196, v197, v198\n"
    "v_max_f32 v248, v199, v248\n"
    "s_nop 1\n"
    "v_max_f32_dpp v248, v248, v248 row_ror:8 row_mask:0xf bank_mask:0xf\n"
    "v_mov_b32 v249, v248\n"
    "s_nop 1\n"
    "v_permlane16_swap_b32 v248, v249\n"
    "v_max_f32 v248, v248, v249\n"
    "v_mov_b32 v249, v248\n"
    "s_cmp_eq_u32 s40, 63\n"
    "v_mfma_f32_16x16x32_bf16 v[24:27], v[212:215], v[212:215], v[24:27]\n"
    "s_waitcnt lgkmcnt(0)\n"
    "s_barrier\n"
    "ds_read_b64_tr_b16 v[168:169], %[vir] offset:2048\n"
    "ds_read_b64_tr_b16 v[170:171], %[vir] offset:2304\n"
    "v_permlane32_swap_b32 v248, v249\n"
    "ds_read_b64_tr_b16 v[172:173], %[vir] offset:2560\n"
    "ds_read_b64_tr_b16 v[174:175], %[vir] offset:2816\n"
    "v_max_f32 v248, v248, v249\n"
    "ds_read_b64_tr_b16 v[176:177], %[vir] offset:3072\n"
    "ds_read_b64_tr_b16 v[178:179], %[vir] offset:3328\n"
    "v_mul_f32 v250, v219, v248\n"
    "ds_read_b64_tr_b16 v[180:181], %[vir] offset:3584\n"
    "ds_read_b64_tr_b16 v[182:183], %[vir] offset:3840\n"
    "v_fmamk_f32 v196, v196, 0x3e0293ee, v250\n"
    "s_waitcnt lgkmcnt(6)\n"
    "v_mfma_f32_16x16x32_bf16 v[204:207], v[168:171], v[152:155], 0\n"
    "v_fmamk_f32 v197, v197, 0x3e0293ee, v250\n"
    "v_fmamk_f32 v198, v198, 0x3e0293ee, v250\n"
    "s_waitcnt lgkmcnt(4)\n"
    "v_mfma_f32_16x16x32_bf16 v[204:207], v[172:175], v[156:159], v[204:207]\n"
    "v_fmamk_f32 v199, v199, 0x3e0293ee, v250\n"
    "s_waitcnt lgkmcnt(2)\n"
    "v_mfma_f32_16x16x32_bf16 v[204:207], v[176:179], v[160:163], v[204:207]\n"
    "v_exp_f32 v196, v196\n"
    "s_waitcnt lgkmcnt(0)\n"
    "v_mfma_f32_16x16x32_bf16 v[204:207], v[180:183], v[164:167], v[204:207]\n"
    "s_cbranch_scc1 Alast_%=\n"
    "v_exp_f32 v197, v197\n"
    "v_exp_f32 v198, v198\n"
    "v_exp_f32 v199, v199\n"
    "v_cvt_pk_bf16_f32 v252, v196, v197\n"
    "v_cvt_pk_bf16_f32 v253, v198, v199\n"
    "s_nop 0\n"
    "v_mov_b32_dpp v254, v252 row_ror:8 row_mask:0xf bank_mask:0xf\n"
    "v_mov_b32_dpp v255, v253 row_ror:8 row_mask:0xf bank_mask:0xf\n"
    "v_fmamk_f32 v204, v204, 0x3e0293ee, v250\n"
    "s_nop 0\n"
    "v_mfma_f32_16x16x32_bf16 v[16:19], v[212:215], v[252:255], 0\n"
    "v_fmamk_f32 v205, v205, 0x3e0293ee, v250\n"
    "v_fmamk_f32 v206, v206, 0x3e0293ee, v250\n"
    "v_mfma_f32_16x16x32_bf16 v[236:239], v[184:187], v[252:255], 0\n"
    "v_fmamk_f32 v207, v207, 0x3e0293ee, v250\n"
    "v_exp_f32 v20, v204\n"
    "v_mfma_f32_16x16x32_bf16 v[240:243], v[188:191], v[252:255], 0\n"
    "v_exp_f32 v21, v205\n"
    "v_exp_f32 v22, v206\n"
    "v_exp_f32 v23, v207\n"
    "v_cvt_pk_bf16_f32 v234, v20, v21\n"
    "v_cvt_pk_bf16_f32 v235, v22, v23\n"
    "v_max3_f32 v232, v204, v205, v206\n"
    "Agen2_%=:\n"
    "s_nop 0\n"
    "v_mfma_f32_16x16x16_bf16 v[208:211], v[216:217], v[234:235], v[16:19]\n"
    "s_waitcnt vmcnt(0)\n"
    "Avok_%=:\n"
    "v_max_f32 v232, v207, v232\n"
    "v_max3_u32 v246, v192, v193, v194\n"
    "v_mfma_f32_16x16x16_bf16 v[196:199], v[192:193], v[234:235], v[236:239]\n"
    "v_max_u32 v246, v195, v246\n"
    "v_cmp_lt_f32 s[64:65], 4.0, v232\n"
    "v_mfma_f32_16x16x16_bf16 v[200:203], v[194:195], v[234:235], v[240:243]\n"
    "v_cmp_le_u32 vcc, v226, v246\n"
    "s_or_b64 vcc, vcc, s[64:65]\n"
    "s_cbranch_scc1 Avretry_%=\n"
    "Avok2_%=:\n"
    "s_mov_b64 exec, s[72:73]\n"
    "v_rcp_f32 v248, v208\n"
    "s_nop 0\n"
    "v_mul_f32 v196, v196, v248\n"
    "v_mul_f32 v197, v197, v248\n"
    "v_mul_f32 v198, v198, v248\n"
    "v_mul_f32 v199, v199, v248\n"
    "v_cvt_pk_bf16_f32 v252, v196, v197\n"
    "v_cvt_pk_bf16_f32 v253, v198, v199\n"
    "v_pk_mul_f32 v[200:201], v[200:201], v[248:249] op_sel_hi:[1,0]\n"
    "v_pk_mul_f32 v[202:203], v[202:203], v[248:249] op_sel_hi:[1,0]\n"
    "v_cvt_pk_bf16_f32 v254, v200, v201\n"
    "v_cvt_pk_bf16_f32 v255, v202, v203\n"
    "global_store_dwordx4 %[vov], v[252:255], s[56:57] offset:0 sc1\n"
    "s_mov_b64 exec, -1\n"
    "s_memrealtime s[64:65]\n"
    "s_cmp_lg_u32 %[w], 0\n"
    "s_cbranch_scc1 Anoh_%=\n"
    "global_load_dwordx2 v[220:221], %[vhv], s[60:61] offset:0 sc1\n"
    "global_load_dwordx2 v[160:161], v230, s[60:61] offset:0 sc1\n"
    "global_load_dwordx2 v[162:163], v231, s[60:61] offset:0 sc1\n"
    "Anoh_%=:\n"
    "s_cmp_lg_u32 %[w], 3\n"
    "s_cbranch_scc1 Anof_%=\n"
    "s_mov_b64 exec, 1\n"
    "global_store_dword v229, v228, %[ws] offset:0 sc1\n"
    "s_mov_b64 exec, -1\n"
    "Anof_%=:\n"
    "Af1_%=:\n"
    "v_cmp_gt_u32 vcc, s91, %[vrd0]\n"
    "s_cbranch_vccz Af1d_%=\n"
    "s_add_u32 s42, s42, 1\n"
    "s_cmp_gt_u32 s42, 524288\n"
    "s_cbranch_scc1 Adead_%=\n"
    "global_load_dword %[vrd0], %[vflv], %[ws] sc1\n"
    "s_waitcnt vmcnt(0)\n"
    "s_branch Af1_%=\n"
    "Af1d_%=:\n"
    "s_add_u32 s92, s91, 3\n"
    "s_min_u32 s92, s92, 64\n"
    "v_cmp_gt_u32 vcc, s92, %[vrd0]\n"
    "s_cbranch_vccz Anfl_%=\n"
    "global_load_dword %[vrd0], %[vflv], %[ws] sc1\n"
    "Anfl_%=:\n"
    "s_bitcmp1_b32 %[w], 0\n"
    "s_cbranch_scc1 Ax1_%=\n"
    "ds_read_b128 v[16:19], %[vw] offset:32768\n"
    "ds_read_b128 v[20:23], %[vw] offset:33792\n"
    "ds_read_b128 v[24:27], %[vw] offset:34816\n"
    "ds_read_b128 v[28:31], %[vw] offset:35840\n"
    "s_add_u32 m0, s86, 0\n"
    "s_nop 0\n"
    "global_load_lds_dwordx4 %[vg16], s[54:55] offset:0 sc1\n"
    "v_add_u32 v245, s84, %[vg16]\n"
    "ds_write_b128 v245, v[192:195] offset:0\n"
    "ds_read_b128 v[32:35], v247 offset:26624\n"
    "ds_read_b128 v[36:39], v247 offset:27648\n"
    "global_load_lds_dwordx4 %[vg16], s[54:55] offset:1024 sc1\n"
    "s_sub_u32 s37, s37, s34\n"
    "s_sub_u32 s36, s36, s66\n"
    "s_lshl_b32 s36, s36, 3\n"
    "s_sub_u32 s36, s36, s67\n"
    "s_cmp_lt_i32 s36, -160\n"
    "s_cselect_b32 s36, 0, s36\n"
    "s_waitcnt lgkmcnt(6)\n"
    "v_mfma_f32_16x16x32_bf16 v[44:47], v[168:171], v[16:19], 0\n"
    "s_add_u32 s40, s40, 1\n"
    "s_and_b32 s92, s40, 3\n"
    "s_cmp_le_u32 s41, 6\n"
    "s_cselect_b32 s36, 0, s36\n"
    "v_mfma_f32_16x16x32_bf16 v[48:51], v[120:123], v[16:19], 0\n"
    "s_add_u32 m0, s86, 8192\n"
    "s_nop 0\n"
    "global_load_lds_dwordx4 %[vg16], s[100:101] offset:0 sc1\n"
    "s_min_i32 s36, s36, 32\n"
    "s_waitcnt lgkmcnt(5)\n"
    "v_mfma_f32_16x16x32_bf16 v[44:47], v[172:175], v[20:23], v[44:47]\n"
    "s_mul_i32 s92, s92, 81920\n"
    "s_add_u32 s92, s92, 1048576\n"
    "s_max_i32 s36, s36, -48\n"
    "v_mfma_f32_16x16x32_bf16 v[48:51], v[124:127], v[20:23], v[48:51]\n"
    "s_add_u32 s46, s44, s92\n"
    "s_addc_u32 s47, s45, 0\n"
    "s_ashr_i32 s36, s36, 2\n"
    "s_waitcnt lgkmcnt(4)\n"
    "v_mfma_f32_16x16x32_bf16 v[44:47], v[176:179], v[24:27], v[44:47]\n"
    "global_load_lds_dwordx4 %[vg16], s[100:101] offset:1024 sc1\n"
    "s_add_u32 s67, s67, s36\n"
    "s_sub_u32 s37, s37, s66\n"
    "v_mfma_f32_16x16x32_bf16 v[48:51], v[128:131], v[24:27], v[48:51]\n"
    "s_lshl_b32 s37, s37, 3\n"
    "s_sub_u32 s37, s37, s68\n"
    "s_waitcnt lgkmcnt(0)\n"
    "s_barrier\n"
    "ds_read_b128 v[40:43], v245 offset:1024\n"
    "v_mfma_f32_16x16x32_bf16 v[44:47], v[180:183], v[28:31], v[44:47]\n"
    "s_add_u32 m0, s86, 16384\n"
    "s_nop 0\n"
    "global_load_lds_dwordx4 %[vg16], s[50:51] offset:0 sc1\n"
    "v_mfma_f32_16x16x32_bf16 v[48:51], v[132:135], v[28:31], v[48:51]\n"
    "s_add_u32 s48, s46, 4096\n"
    "s_addc_u32 s49, s47, 0\n"
    "global_load_lds_dwordx4 %[vg16], s[50:51] offset:1024 sc1\n"
    "s_cmp_le_u32 s41, 6\n"
    "s_cselect_b32 s37, 0, s37\n"
    "s_min_i32 s37, s37, 32\n"
    "s_max_i32 s37, s37, -48\n"
    "s_ashr_i32 s37, s37, 2\n"
    "v_mov_b32 v234, 0x3e0293ee\n"
    "v_mov_b32 v154, 0\n"
    "v_mov_b32 v155, 0\n"
    "v_cndmask_b32 v48, v218, v48, s[76:77]\n"
    "v_cndmask_b32 v49, v218, v49, s[78:79]\n"
    "v_cndmask_b32 v50, v218, v50, s[80:81]\n"
    "v_cndmask_b32 v51, v218, v51, s[82:83]\n"
    "v_max3_f32 v248, v44, v45, v46\n"
    "v_max3_f32 v248, v47, v48, v248\n"
    "v_max3_f32 v248, v49, v50, v248\n"
    "v_max_f32 v248, v51, v248\n"
    "v_mov_b32 v250, v248\n"
    "s_add_u32 s68, s68, s37\n"
    "s_mov_b32 s66, s38\n"
    "v_permlane16_swap_b32 v248, v250\n"
    "v_max_f32 v248, v248, v250\n"
    "v_mov_b32 v250, v248\n"
    "s_mov_b32 s34, s69\n"
    "s_sub_u32 s39, s39, 1\n"
    "v_permlane32_swap_b32 v248, v250\n"
    "v_max_f32 v248, v248, v250\n"
    "s_max_i32 s39, s39, 0\n"
    "s_add_u32 s69, s67, %[da]\n"
    "s_add_u32 s69, s69, s39\n"
    "s_lshr_b32 s69, s69, 3\n"
    "s_add_u32 s35, s68, %[db]\n"
    "s_add_u32 s35, s35, s39\n"
    "s_lshr_b32 s35, s35, 3\n"
    "s_add_u32 s35, s35, s69\n"
    "v_mul_f32 v248, v219, v248\n"
    "v_pk_fma_f32 v[44:45], v[44:45], v[234:235], v[248:249] op_sel_hi:[1,0,0]\n"
    "v_pk_fma_f32 v[46:47], v[46:47], v[234:235], v[248:249] op_sel_hi:[1,0,0]\n"
    "v_pk_fma_f32 v[48:49], v[48:49], v[234:235], v[248:249] op_sel_hi:[1,0,0]\n"
    "v_pk_fma_f32 v[50:51], v[50:51], v[234:235], v[248:249] op_sel_hi:[1,0,0]\n"
    "v_exp_f32 v48, v48\n"
    "v_exp_f32 v49, v49\n"
    "v_exp_f32 v50, v50\n"
    "v_exp_f32 v51, v51\n"
    "v_cvt_pk_bf16_f32 v152, v48, v49\n"
    "v_cvt_pk_bf16_f32 v153, v50, v51\n"
    "v_exp_f32 v44, v44\n"
    "s_nop 0\n"
    "v_mfma_f32_16x16x32_bf16 v[20:23], v[212:215], v[152:155], 0\n"
    "v_exp_f32 v45, v45\n"
    "v_mfma_f32_16x16x32_bf16 v[196:199], v[184:187], v[152:155], 0\n"
    "v_exp_f32 v46, v46\n"
    "v_mfma_f32_16x16x32_bf16 v[200:203], v[188:191], v[152:155], 0\n"
    "v_exp_f32 v47, v47\n"
    "v_mfma_f32_16x16x32_bf16 v[204:207], v[32:35], v[152:155], 0\n"
    "v_cvt_pk_bf16_f32 v156, v44, v45\n"
    "v_cvt_pk_bf16_f32 v157, v46, v47\n"
    "v_mfma_f32_16x16x32_bf16 v[16:19], v[36:39], v[152:155], 0\n"
    "s_waitcnt vmcnt(6)\n"
    "s_waitcnt lgkmcnt(0)\n"
    "s_mov_b32 s90, s64\n"
    "v_mfma_f32_16x16x16_bf16 v[20:23], v[216:217], v[156:157], v[20:23]\n"
    "v_min3_u32 v162, v220, v160, v162\n"
    "v_max_u32 v160, v220, v160\n"
    "v_mfma_f32_16x16x16_bf16 v[196:199], v[192:193], v[156:157], v[196:199]\n"
    "v_mov_b32 v220, v162\n"
    "v_subrev_u32 v221, s43, v221\n"
    "v_mfma_f32_16x16x16_bf16 v[200:203], v[194:195], v[156:157], v[200:203]\n"
    "v_subrev_u32 v161, s43, v161\n"
    "v_subrev_u32 v163, s43, v163\n"
    "v_mfma_f32_16x16x16_bf16 v[204:207], v[40:41], v[156:157], v[204:207]\n"
    "v_max3_i32 v221, v221, v161, v163\n"
    "v_mfma_f32_16x16x16_bf16 v[16:19], v[42:43], v[156:157], v[16:19]\n"
    "s_nop 0\n"
    "v_max_i32_dpp v221, v221, v221 row_ror:8 row_mask:0xf bank_mask:0xf\n"
    "v_rcp_f32 v248, v20\n"
    "s_nop 0\n"
    "v_pk_mul_f32 v[196:197], v[196:197], v[248:249] op_sel_hi:[1,0]\n"
    "v_pk_mul_f32 v[198:199], v[198:199], v[248:249] op_sel_hi:[1,0]\n"
    "v_cvt_pk_bf16_f32 v236, v196, v197\n"
    "v_cvt_pk_bf16_f32 v237, v198, v199\n"
    "v_max_i32_dpp v221, v221, v221 row_ror:4 row_mask:0xf bank_mask:0xf\n"
    "v_pk_mul_f32 v[200:201], v[200:201], v[248:249] op_sel_hi:[1,0]\n"
    "v_pk_mul_f32 v[202:203], v[202:203], v[248:249] op_sel_hi:[1,0]\n"
    "v_cvt_pk_bf16_f32 v238, v200, v201\n"
    "v_cvt_pk_bf16_f32 v239, v202, v203\n"
    "v_max_i32_dpp v221, v221, v221 row_ror:2 row_mask:0xf bank_mask:0xf\n"
    "global_store_dwordx4 %[vg16], v[236:239], s[58:59] offset:0 sc1\n"
    "v_pk_mul_f32 v[204:205], v[204:205], v[248:249] op_sel_hi:[1,0]\n"
    "v_pk_mul_f32 v[206:207], v[206:207], v[248:249] op_sel_hi:[1,0]\n"
    "v_cvt_pk_bf16_f32 v240, v204, v205\n"
    "v_cvt_pk_bf16_f32 v241, v206, v207\n"
    "v_max_i32_dpp v221, v221, v221 row_ror:1 row_mask:0xf bank_mask:0xf\n"
    "v_pk_mul_f32 v[16:17], v[16:17], v[248:249] op_sel_hi:[1,0]\n"
    "v_pk_mul_f32 v[18:19], v[18:19], v[248:249] op_sel_hi:[1,0]\n"
    "v_cvt_pk_bf16_f32 v242, v16, v17\n"
    "v_cvt_pk_bf16_f32 v243, v18, v19\n"
    "global_store_dwordx4 %[vg16], v[240:243], s[70:71] offset:0 sc1\n"
    "s_branch Ax2_%=\n"
    "Ax1_%=:\n"
    "ds_read_b128 v[16:19], %[vw] offset:36864\n"
    "ds_read_b128 v[20:23], %[vw] offset:37888\n"
    "ds_read_b128 v[24:27], %[vw] offset:38912\n"
    "ds_read_b128 v[28:31], %[vw] offset:39936\n"
    "s_add_u32 m0, s86, 0\n"
    "s_nop 0\n"
    "global_load_lds_dwordx4 %[vg16], s[54:55] offset:0 sc1\n"
    "v_add_u32 v245, s84, %[vg16]\n"
    "ds_write_b128 v245, v[192:195] offset:0\n"
    "ds_read_b128 v[32:35], v247 offset:22528\n"
    "ds_read_b128 v[36:39], v247 offset:23552\n"
    "v_subrev_u32 v245, 1024, v245\n"
    "global_load_lds_dwordx4 %[vg16], s[54:55] offset:1024 sc1\n"
    "s_waitcnt lgkmcnt(6)\n"
    "v_mfma_f32_16x16x32_bf16 v[44:47], v[168:171], v[16:19], 0\n"
    "s_add_u32 s40, s40, 1\n"
    "s_and_b32 s92, s40, 3\n"
    "v_mfma_f32_16x16x32_bf16 v[48:51], v[120:123], v[16:19], 0\n"
    "s_add_u32 m0, s86, 8192\n"
    "s_nop 0\n"
    "global_load_lds_dwordx4 %[vg16], s[100:101] offset:0 sc1\n"
    "v_mfma_f32_16x16x32_bf16 v[52:55], v[136:139], v[16:19], 0\n"
    "s_mul_i32 s92, s92, 81920\n"
    "s_add_u32 s92, s92, 1048576\n"
    "s_waitcnt lgkmcnt(5)\n"
    "v_mfma_f32_16x16x32_bf16 v[44:47], v[172:175], v[20:23], v[44:47]\n"
    "s_add_u32 s46, s44, s92\n"
    "s_addc_u32 s47, s45, 0\n"
    "v_mfma_f32_16x16x32_bf16 v[48:51], v[124:127], v[20:23], v[48:51]\n"
    "global_load_lds_dwordx4 %[vg16], s[100:101] offset:1024 sc1\n"
    "v_mfma_f32_16x16x32_bf16 v[52:55], v[140:143], v[20:23], v[52:55]\n"
    "s_waitcnt lgkmcnt(0)\n"
    "s_barrier\n"
    "ds_read_b128 v[40:43], v245 offset:0\n"
    "v_mfma_f32_16x16x32_bf16 v[44:47], v[176:179], v[24:27], v[44:47]\n"
    "s_add_u32 m0, s86, 16384\n"
    "s_nop 0\n"
    "global_load_lds_dwordx4 %[vg16], s[50:51] offset:0 sc1\n"
    "v_mfma_f32_16x16x32_bf16 v[48:51], v[128:131], v[24:27], v[48:51]\n"
    "s_add_u32 s48, s46, 4096\n"
    "s_addc_u32 s49, s47, 0\n"
    "v_mfma_f32_16x16x32_bf16 v[52:55], v[144:147], v[24:27], v[52:55]\n"
    "v_mfma_f32_16x16x32_bf16 v[44:47], v[180:183], v[28:31], v[44:47]\n"
    "v_mfma_f32_16x16x32_bf16 v[48:51], v[132:135], v[28:31], v[48:51]\n"
    "v_mfma_f32_16x16x32_bf16 v[52:55], v[148:151], v[28:31], v[52:55]\n"
    "global_load_lds_dwordx4 %[vg16], s[50:51] offset:1024 sc1\n"
    "v_mov_b32 v234, 0x3e0293ee\n"
    "s_nop 5\n"
    "v_cndmask_b32 v52, v218, v52, s[76:77]\n"
    "v_cndmask_b32 v53, v218, v53, s[78:79]\n"
    "v_cndmask_b32 v54, v218, v54, s[80:81]\n"
    "v_cndmask_b32 v55, v218, v55, s[82:83]\n"
    "v_max3_f32 v248, v44, v45, v46\n"
    "v_max3_f32 v248, v47, v48, v248\n"
    "v_max3_f32 v248, v49, v50, v248\n"
    "v_max3_f32 v248, v51, v52, v248\n"
    "v_max3_f32 v248, v53, v54, v248\n"
    "v_max_f32 v248, v55, v248\n"
    "v_mov_b32 v250, v248\n"
    "s_nop 1\n"
    "v_permlane16_swap_b32 v248, v250\n"
    "v_max_f32 v248, v248, v250\n"
    "v_mov_b32 v250, v248\n"
    "s_nop 1\n"
    "v_permlane32_swap_b32 v248, v250\n"
    "v_max_f32 v248, v248, v250\n"
    "v_mul_f32 v248, v219, v248\n"
    "v_pk_fma_f32 v[44:45], v[44:45], v[234:235], v[248:249] op_sel_hi:[1,0,0]\n"
    "v_pk_fma_f32 v[46:47], v[46:47], v[234:235], v[248:249] op_sel_hi:[1,0,0]\n"
    "v_pk_fma_f32 v[48:49], v[48:49], v[234:235], v[248:249] op_sel_hi:[1,0,0]\n"
    "v_pk_fma_f32 v[50:51], v[50:51], v[234:235], v[248:249] op_sel_hi:[1,0,0]\n"
    "v_pk_fma_f32 v[52:53], v[52:53], v[234:235], v[248:249] op_sel_hi:[1,0,0]\n"
    "v_pk_fma_f32 v[54:55], v[54:55], v[234:235], v[248:249] op_sel_hi:[1,0,0]\n"
    "v_exp_f32 v48, v48\n"
    "v_exp_f32 v49, v49\n"
    "v_exp_f32 v50, v50\n"
    "v_exp_f32 v51, v51\n"
    "v_exp_f32 v52, v52\n"
    "v_exp_f32 v53, v53\n"
    "v_exp_f32 v54, v54\n"
    "v_exp_f32 v55, v55\n"
    "v_cvt_pk_bf16_f32 v152, v48, v49\n"
    "v_cvt_pk_bf16_f32 v153, v50, v51\n"
    "v_cvt_pk_bf16_f32 v154, v52, v53\n"
    "v_cvt_pk_bf16_f32 v155, v54, v55\n"
    "v_exp_f32 v44, v44\n"
    "s_nop 0\n"
    "v_mfma_f32_16x16x32_bf16 v[20:23], v[212:215], v[152:155], 0\n"
    "v_exp_f32 v45, v45\n"
    "v_mfma_f32_16x16x32_bf16 v[196:199], v[184:187], v[152:155], 0\n"
    "v_exp_f32 v46, v46\n"
    "v_mfma_f32_16x16x32_bf16 v[200:203], v[188:191], v[152:155], 0\n"
    "v_exp_f32 v47, v47\n"
    "v_mfma_f32_16x16x32_bf16 v[204:207], v[32:35], v[152:155], 0\n"
    "v_cvt_pk_bf16_f32 v156, v44, v45\n"
    "v_cvt_pk_bf16_f32 v157, v46, v47\n"
    "v_mfma_f32_16x16x32_bf16 v[16:19], v[36:39], v[152:155], 0\n"
    "s_nop 0\n"
    "v_mfma_f32_16x16x16_bf16 v[20:23], v[216:217], v[156:157], v[20:23]\n"
    "v_mfma_f32_16x16x16_bf16 v[196:199], v[192:193], v[156:157], v[196:199]\n"
    "v_mfma_f32_16x16x16_bf16 v[200:203], v[194:195], v[156:157], v[200:203]\n"
    "s_waitcnt lgkmcnt(0)\n"
    "v_mfma_f32_16x16x16_bf16 v[204:207], v[40:41], v[156:157], v[204:207]\n"
    "s_nop 1\n"
    "v_mfma_f32_16x16x16_bf16 v[16:19], v[42:43], v[156:157], v[16:19]\n"
    "s_nop 0\n"
    "v_rcp_f32 v248, v20\n"
    "s_nop 0\n"
    "v_pk_mul_f32 v[196:197], v[196:197], v[248:249] op_sel_hi:[1,0]\n"
    "v_pk_mul_f32 v[198:199], v[198:199], v[248:249] op_sel_hi:[1,0]\n"
    "v_cvt_pk_bf16_f32 v236, v196, v197\n"
    "v_cvt_pk_bf16_f32 v237, v198, v199\n"
    "v_pk_mul_f32 v[200:201], v[200:201], v[248:249] op_sel_hi:[1,0]\n"
    "v_pk_mul_f32 v[202:203], v[202:203], v[248:249] op_sel_hi:[1,0]\n"
    "v_cvt_pk_bf16_f32 v238, v200, v201\n"
    "v_cvt_pk_bf16_f32 v239, v202, v203\n"
    "global_store_dwordx4 %[vg16], v[236:239], s[58:59] offset:0 sc1\n"
    "v_pk_mul_f32 v[204:205], v[204:205], v[248:249] op_sel_hi:[1,0]\n"
    "v_pk_mul_f32 v[206:207], v[206:207], v[248:249] op_sel_hi:[1,0]\n"
    "v_cvt_pk_bf16_f32 v240, v204, v205\n"
    "v_cvt_pk_bf16_f32 v241, v206, v207\n"
    "v_pk_mul_f32 v[16:17], v[16:17], v[248:249] op_sel_hi:[1,0]\n"
    "v_pk_mul_f32 v[18:19], v[18:19], v[248:249] op_sel_hi:[1,0]\n"
    "v_cvt_pk_bf16_f32 v242, v16, v17\n"
    "v_cvt_pk_bf16_f32 v243, v18, v19\n"
    "global_store_dwordx4 %[vg16], v[240:243], s[70:71] offset:0 sc1\n"
    "Ax2_%=:\n"
    "s_mov_b64 exec, s[72:73]\n"
    "global_store_dwordx4 %[vov], v[224:227], s[74:75] offset:0 sc1\n"
    "s_mov_b64 exec, -1\n"
    "s_cmp_lt_u32 s40, 64\n"
    "s_cbranch_scc1 Aloop_%=\n"
    "s_waitcnt vmcnt(0)\n"
    "s_waitcnt lgkmcnt(0)\n"
    "s_barrier\n"
    "v_mov_b32 v228, 64\n"
    "s_cmp_lg_u32 %[w], 0\n"
    "s_cbranch_scc1 Aend_%=\n"
    "s_mov_b64 exec, 1\n"
    "global_store_dword v229, v228, %[ws] offset:0 sc1\n"
    "s_mov_b64 exec, -1\n"
    "s_branch Aend_%=\n"
    "Aretry_%=:\n"
    "v_max3_u32 v252, v16, v17, v18\n"
    "v_max3_u32 v252, v18, v19, v252\n"
    "v_max3_u32 v252, v20, v21, v252\n"
    "v_max3_u32 v252, v22, v23, v252\n"
    "v_max3_u32 v252, v24, v25, v252\n"
    "v_max3_u32 v252, v26, v27, v252\n"
    "v_max3_u32 v252, v28, v29, v252\n"
    "v_max3_u32 v252, v30, v31, v252\n"
    "v_max3_u32 v252, v32, v33, v252\n"
    "v_max3_u32 v252, v34, v35, v252\n"
    "v_max3_u32 v252, v36, v37, v252\n"
    "v_max3_u32 v252, v38, v39, v252\n"
    "v_max3_u32 v252, v40, v41, v252\n"
    "v_max3_u32 v252, v42, v43, v252\n"
    "v_max3_u32 v252, v44, v45, v252\n"
    "v_max3_u32 v252, v46, v47, v252\n"
    "v_max3_u32 v252, v48, v49, v252\n"
    "v_max3_u32 v252, v50, v51, v252\n"
    "v_max3_u32 v252, v52, v53, v252\n"
    "v_max3_u32 v252, v54, v55, v252\n"
    "v_cmp_eq_u32 vcc, 0xff81ff81, v252\n"
    "s_cbranch_vccnz Aretry1_%=\n"
    "s_mov_b64 s[64:65], 0\n"
    "v_cmp_eq_u32 vcc, 0xff81ff81, v16\n"
    "s_or_b64 s[64:65], s[64:65], vcc\n"
    "v_cmp_eq_u32 vcc, 0xff81ff81, v17\n"
    "s_or_b64 s[64:65], s[64:65], vcc\n"
    "v_cmp_eq_u32 vcc, 0xff81ff81, v18\n"
    "s_or_b64 s[64:65], s[64:65], vcc\n"
    "v_cmp_eq_u32 vcc, 0xff81ff81, v19\n"
    "s_or_b64 s[64:65], s[64:65], vcc\n"
    "v_cmp_eq_u32 vcc, 0xff81ff81, v20\n"
    "s_or_b64 s[64:65], s[64:65], vcc\n"
    "v_cmp_eq_u32 vcc, 0xff81ff81, v21\n"
    "s_or_b64 s[64:65], s[64:65], vcc\n"
    "v_cmp_eq_u32 vcc, 0xff81ff81, v22\n"
    "s_or_b64 s[64:65], s[64:65], vcc\n"
    "v_cmp_eq_u32 vcc, 0xff81ff81, v23\n"
    "s_or_b64 s[64:65], s[64:65], vcc\n"
    "v_cmp_eq_u32 vcc, 0xff81ff81, v24\n"
    "s_or_b64 s[64:65], s[64:65], vcc\n"
    "v_cmp_eq_u32 vcc, 0xff81ff81, v25\n"
    "s_or_b64 s[64:65], s[64:65], vcc\n"
    "v_cmp_eq_u32 vcc, 0xff81ff81, v26\n"
    "s_or_b64 s[64:65], s[64:65], vcc\n"
    "v_cmp_eq_u32 vcc, 0xff81ff81, v27\n"
    "s_or_b64 s[64:65], s[64:65], vcc\n"
    "v_cmp_eq_u32 vcc, 0xff81ff81, v28\n"
    "s_or_b64 s[64:65], s[64:65], vcc\n"
    "v_cmp_eq_u32 vcc, 0xff81ff81, v29\n"
    "s_or_b64 s[64:65], s[64:65], vcc\n"
    "v_cmp_eq_u32 vcc, 0xff81ff81, v30\n"
    "s_or_b64 s[64:65], s[64:65], vcc\n"
    "v_cmp_eq_u32 vcc, 0xff81ff81, v31\n"
    "s_or_b64 s[64:65], s[64:65], vcc\n"
    "v_cmp_eq_u32 vcc, 0xff81ff81, v32\n"
    "s_or_b64 s[64:65], s[64:65], vcc\n"
    "v_cmp_eq_u32 vcc, 0xff81ff81, v33\n"
    "s_or_b64 s[64:65], s[64:65], vcc\n"
    "v_cmp_eq_u32 vcc, 0xff81ff81, v34\n"
    "s_or_b64 s[64:65], s[64:65], vcc\n"
    "v_cmp_eq_u32 vcc, 0xff81ff81, v35\n"
    "s_or_b64 s[64:65], s[64:65], vcc\n"
    "v_cmp_eq_u32 vcc, 0xff81ff81, v36\n"
    "s_or_b64 s[64:65], s[64:65], vcc\n"
    "v_cmp_eq_u32 vcc, 0xff81ff81, v37\n"
    "s_or_b64 s[64:65], s[64:65], vcc\n"
    "v_cmp_eq_u32 vcc, 0xff81ff81, v38\n"
    "s_or_b64 s[64:65], s[64:65], vcc\n"
    "v_cmp_eq_u32 vcc, 0xff81ff81, v39\n"
    "s_or_b64 s[64:65], s[64:65], vcc\n"
    "v_cmp_eq_u32 vcc, 0xff81ff81, v40\n"
    "s_or_b64 s[64:65], s[64:65], vcc\n"
    "v_cmp_eq_u32 vcc, 0xff81ff81, v41\n"
    "s_or_b64 s[64:65], s[64:65], vcc\n"
    "v_cmp_eq_u32 vcc, 0xff81ff81, v42\n"
    "s_or_b64 s[64:65], s[64:65], vcc\n"
    "v_cmp_eq_u32 vcc, 0xff81ff81, v43\n"
    "s_or_b64 s[64:65], s[64:65], vcc\n"
    "v_cmp_eq_u32 vcc, 0xff81ff81, v44\n"
    "s_or_b64 s[64:65], s[64:65], vcc\n"
    "v_cmp_eq_u32 vcc, 0xff81ff81, v45\n"
    "s_or_b64 s[64:65], s[64:65], vcc\n"
    "v_cmp_eq_u32 vcc, 0xff81ff81, v46\n"
    "s_or_b64 s[64:65], s[64:65], vcc\n"
    "v_cmp_eq_u32 vcc, 0xff81ff81, v47\n"
    "s_or_b64 s[64:65], s[64:65], vcc\n"
    "v_cmp_eq_u32 vcc, 0xff81ff81, v48\n"
    "s_or_b64 s[64:65], s[64:65], vcc\n"
    "v_cmp_eq_u32 vcc, 0xff81ff81, v49\n"
    "s_or_b64 s[64:65], s[64:65], vcc\n"
    "v_cmp_eq_u32 vcc, 0xff81ff81, v50\n"
    "s_or_b64 s[64:65], s[64:65], vcc\n"
    "v_cmp_eq_u32 vcc, 0xff81ff81, v51\n"
    "s_or_b64 s[64:65], s[64:65], vcc\n"
    "v_cmp_eq_u32 vcc, 0xff81ff81, v52\n"
    "s_or_b64 s[64:65], s[64:65], vcc\n"
    "v_cmp_eq_u32 vcc, 0xff81ff81, v53\n"
    "s_or_b64 s[64:65], s[64:65], vcc\n"
    "v_cmp_eq_u32 vcc, 0xff81ff81, v54\n"
    "s_or_b64 s[64:65], s[64:65], vcc\n"
    "v_cmp_eq_u32 vcc, 0xff81ff81, v55\n"
    "s_or_b64 s[64:65], s[64:65], vcc\n"
    "s_cbranch_scc0 Acont_%=\n"
    "Aretry1_%=:\n"
    "s_waitcnt lgkmcnt(0)\n"
    "s_nop 7\n"
    "s_cmp_lg_u32 %[w], 0\n"
    "s_cbranch_scc1 Aretry3_%=\n"
    "s_cmp_lg_u32 s98, 0\n"
    "s_cbranch_scc1 Aretry2b_%=\n"
    "s_mov_b32 s98, 1\n"
    "s_add_u32 s62, s43, 86\n"
    "s_branch Afw2_%=\n"
    "Aretry2_%=:\n"
    "s_sleep 4\n"
    "Aretry2b_%=:\n"
    "global_load_dwordx2 v[220:221], %[vhv], s[60:61] offset:0 sc1\n"
    "global_load_dwordx2 v[160:161], v230, s[60:61] offset:0 sc1\n"
    "global_load_dwordx2 v[162:163], v231, s[60:61] offset:0 sc1\n"
    "s_add_u32 s42, s42, 1\n"
    "s_cmp_gt_u32 s42, 524288\n"
    "s_cbranch_scc1 Adead_%=\n"
    "s_waitcnt vmcnt(0)\n"
    "v_min3_u32 v162, v220, v160, v162\n"
    "v_max_u32 v160, v220, v160\n"
    "v_mov_b32 v220, v162\n"
    "v_cmp_gt_u32 vcc, s41, v220\n"
    "s_cbranch_vccnz Aretry2_%=\n"
    "v_subrev_u32 v221, s43, v221\n"
    "v_subrev_u32 v161, s43, v161\n"
    "v_subrev_u32 v163, s43, v163\n"
    "v_max3_i32 v221, v221, v161, v163\n"
    "s_nop 1\n"
    "v_max_i32_dpp v221, v221, v221 row_ror:8 row_mask:0xf bank_mask:0xf\n"
    "s_nop 1\n"
    "v_max_i32_dpp v221, v221, v221 row_ror:4 row_mask:0xf bank_mask:0xf\n"
    "s_nop 1\n"
    "v_max_i32_dpp v221, v221, v221 row_ror:2 row_mask:0xf bank_mask:0xf\n"
    "s_nop 1\n"
    "v_max_i32_dpp v221, v221, v221 row_ror:1 row_mask:0xf bank_mask:0xf\n"
    "v_cmp_lt_u32 vcc, s41, v160\n"
    "v_readlane_b32 s62, v221, 0\n"
    "v_readlane_b32 s63, v221, 32\n"
    "v_readlane_b32 s92, v221, 48\n"
    "s_nop 0\n"
    "s_max_i32 s63, s63, s92\n"
    "s_sub_u32 s63, s63, s62\n"
    "s_add_u32 s62, s62, s43\n"
    "s_and_b32 vcc_lo, vcc_lo, 0xffff\n"
    "s_cselect_b32 s92, 2, s63\n"
    "s_cmp_gt_i32 s92, 1\n"
    "s_cbranch_scc1 E1503a_%=\n"
    "E1503b_%=:\n"
    "s_sub_u32 s62, s62, 4\n"
    "Afw2_%=:\n"
    "s_memrealtime s[64:65]\n"
    "Afw2l_%=:\n"
    "s_memrealtime vcc\n"
    "s_waitcnt lgkmcnt(1)\n"
    "s_sub_u32 s63, s62, s64\n"
    "s_cmp_ge_u32 s63, 20000\n"
    "s_cbranch_scc1 Afw2y_%=\n"
    "s_memrealtime s[64:65]\n"
    "s_waitcnt lgkmcnt(1)\n"
    "s_sub_u32 s63, s62, vcc_lo\n"
    "s_cmp_ge_u32 s63, 20000\n"
    "s_cbranch_scc1 Afw2y_%=\n"
    "s_memrealtime vcc\n"
    "s_waitcnt lgkmcnt(1)\n"
    "s_sub_u32 s63, s62, s64\n"
    "s_cmp_ge_u32 s63, 20000\n"
    "s_cbranch_scc1 Afw2y_%=\n"
    "s_memrealtime s[64:65]\n"
    "s_waitcnt lgkmcnt(1)\n"
    "s_sub_u32 s63, s62, vcc_lo\n"
    "s_cmp_ge_u32 s63, 20000\n"
    "s_cbranch_scc1 Afw2y_%=\n"
    "s_memrealtime vcc\n"
    "s_waitcnt lgkmcnt(1)\n"
    "s_sub_u32 s63, s62, s64\n"
    "s_cmp_ge_u32 s63, 20000\n"
    "s_cbranch_scc1 Afw2y_%=\n"
    "s_memrealtime s[64:65]\n"
    "s_waitcnt lgkmcnt(1)\n"
    "s_sub_u32 s63, s62, vcc_lo\n"
    "s_cmp_ge_u32 s63, 20000\n"
    "s_cbranch_scc1 Afw2y_%=\n"
    "s_memrealtime vcc\n"
    "s_waitcnt lgkmcnt(1)\n"
    "s_sub_u32 s63, s62, s64\n"
    "s_cmp_ge_u32 s63, 20000\n"
    "s_cbranch_scc1 Afw2y_%=\n"
    "s_memrealtime s[64:65]\n"
    "s_waitcnt lgkmcnt(1)\n"
    "s_sub_u32 s63, s62, vcc_lo\n"
    "s_cmp_ge_u32 s63, 20000\n"
    "s_cbranch_scc1 Afw2y_%=\n"
    "s_branch Afw2l_%=\n"
    "Afw2y_%=:\n"
    "s_cmp_lt_u32 s63, -3\n"
    "s_cbranch_scc1 Afw2x_%=\n"
    "s_nop 2\n"
    "s_cmp_lt_u32 s63, -2\n"
    "s_cbranch_scc1 Afw2x_%=\n"
    "s_nop 2\n"
    "s_cmp_lt_u32 s63, -1\n"
    "s_cbranch_scc1 Afw2x_%=\n"
    "s_nop 2\n"
    "s_branch Afw2x_%=\n"
    "Afw2x_%=:\n"
    "s_waitcnt lgkmcnt(0)\n"
    "s_sub_u32 s62, s62, s63\n"
    "s_max_u32 s63, s63, -4\n"
    "s_add_u32 s62, s62, s63\n"
    "s_add_u32 s62, s62, 4\n"
    "Aretry3_%=:\n"
    "s_barrier\n"
    "s_mov_b32 m0, s84\n"
    "s_nop 0\n"
    "global_load_lds_dwordx4 %[vmv0], s[46:47] offset:0 sc1\n"
    "s_add_u32 m0, s84, 4096\n"
    "s_nop 0\n"
    "global_load_lds_dwordx4 %[vmv0], s[48:49] offset:0 sc1\n"
    "s_mov_b32 m0, s85\n"
    "s_mov_b32 exec_hi, 0\n"
    "global_load_lds_dwordx4 %[vmv2], s[46:47] offset:0 sc1\n"
    "s_mov_b32 exec_hi, -1\n"
    "s_add_u32 s42, s42, 1\n"
    "s_cmp_gt_u32 s42, 524288\n"
    "s_cbranch_scc1 Adead_%=\n"
    "v_mov_b32 v196, 0\n"
    "v_mov_b32 v197, 0\n"
    "v_mov_b32 v198, 0\n"
    "v_mov_b32 v199, 0\n"
    "v_mov_b32 v200, 0\n"
    "v_mov_b32 v201, 0\n"
    "v_mov_b32 v202, 0\n"
    "v_mov_b32 v203, 0\n"
    "v_mov_b32 v204, 0\n"
    "v_mov_b32 v205, 0\n"
    "v_mov_b32 v206, 0\n"
    "v_mov_b32 v207, 0\n"
    "v_mov_b32 v208, 0\n"
    "v_mov_b32 v209, 0\n"
    "v_mov_b32 v210, 0\n"
    "v_mov_b32 v211, 0\n"
    "s_cmp_lg_u32 %[w], 0\n"
    "s_cbranch_scc1 Aretry4_%=\n"
    "v_or_b32 v222, 1, v222\n"
    "v_writelane_b32 v223, s62, 1\n"
    "s_mov_b64 exec, 3\n"
    "global_store_dwordx2 %[vrd1], v[222:223], s[60:61] offset:0 sc1\n"
    "s_mov_b64 exec, -1\n"
    "s_waitcnt vmcnt(1)\n"
    "s_branch Ago_%=\n"
    "Aretry4_%=:\n"
    "s_waitcnt vmcnt(0)\n"
    "s_branch Ago_%=\n"
    "Alast_%=:\n"
    "s_waitcnt vmcnt(0)\n"
    "s_mov_b64 s[64:65], 0\n"
    "v_cmp_eq_u32 vcc, 0xff81ff81, v192\n"
    "s_or_b64 s[64:65], s[64:65], vcc\n"
    "v_cmp_eq_u32 vcc, 0xff81ff81, v193\n"
    "s_or_b64 s[64:65], s[64:65], vcc\n"
    "v_cmp_eq_u32 vcc, 0xff81ff81, v194\n"
    "s_or_b64 s[64:65], s[64:65], vcc\n"
    "v_cmp_eq_u32 vcc, 0xff81ff81, v195\n"
    "s_or_b64 s[64:65], s[64:65], vcc\n"
    "s_cbranch_scc0 Anoh_%=\n"
    "s_add_u32 s42, s42, 1\n"
    "s_cmp_gt_u32 s42, 524288\n"
    "s_cbranch_scc1 Adead_%=\n"
    "global_load_dwordx2 v[192:193], %[vdg], s[52:53] offset:0 sc1\n"
    "global_load_dwordx2 v[194:195], %[vdg], s[52:53] offset:512 sc1\n"
    "s_branch Alast_%=\n"
    "Afirst_%=:\n"
    "s_memrealtime s[64:65]\n"
    "s_waitcnt lgkmcnt(0)\n"
    "s_mov_b32 s43, s64\n"
    "s_branch Arel_%=\n"
    "Ahp_%=:\n"
    "s_sleep 4\n"
    "global_load_dwordx2 v[220:221], %[vhv], s[60:61] offset:0 sc1\n"
    "global_load_dwordx2 v[160:161], v230, s[60:61] offset:0 sc1\n"
    "global_load_dwordx2 v[162:163], v231, s[60:61] offset:0 sc1\n"
    "s_add_u32 s42, s42, 1\n"
    "s_cmp_gt_u32 s42, 524288\n"
    "s_cbranch_scc1 Adead_%=\n"
    "s_waitcnt vmcnt(0)\n"
    "v_min3_u32 v162, v220, v160, v162\n"
    "v_max_u32 v160, v220, v160\n"
    "v_mov_b32 v220, v162\n"
    "v_cmp_gt_u32 vcc, s41, v220\n"
    "s_cbranch_vccnz Ahp_%=\n"
    "v_subrev_u32 v221, s43, v221\n"
    "v_subrev_u32 v161, s43, v161\n"
    "v_subrev_u32 v163, s43, v163\n"
    "v_max3_i32 v221, v221, v161, v163\n"
    "s_nop 1\n"
    "v_max_i32_dpp v221, v221, v221 row_ror:8 row_mask:0xf bank_mask:0xf\n"
    "s_nop 1\n"
    "v_max_i32_dpp v221, v221, v221 row_ror:4 row_mask:0xf bank_mask:0xf\n"
    "s_nop 1\n"
    "v_max_i32_dpp v221, v221, v221 row_ror:2 row_mask:0xf bank_mask:0xf\n"
    "s_nop 1\n"
    "v_max_i32_dpp v221, v221, v221 row_ror:1 row_mask:0xf bank_mask:0xf\n"
    "s_branch Ahc_%=\n"
    "Avretry_%=:\n"
    "s_cmp_eq_u64 s[64:65], 0\n"
    "s_cbranch_scc1 Avr2_%=\n"
    "s_nop 7\n"
    "v_max_f32 v232, 0, v232\n"
    "v_mov_b32 v233, v232\n"
    "s_nop 1\n"
    "v_permlane16_swap_b32 v232, v233\n"
    "v_max_f32 v232, v232, v233\n"
    "v_sub_f32 v244, 0, v232\n"
    "v_sub_f32 v204, v204, v232\n"
    "v_sub_f32 v205, v205, v232\n"
    "v_sub_f32 v206, v206, v232\n"
    "v_sub_f32 v207, v207, v232\n"
    "v_exp_f32 v244, v244\n"
    "v_exp_f32 v20, v204\n"
    "v_exp_f32 v21, v205\n"
    "v_exp_f32 v22, v206\n"
    "v_exp_f32 v23, v207\n"
    "v_mul_f32 v16, v16, v244\n"
    "v_cvt_pk_bf16_f32 v234, v20, v21\n"
    "v_cvt_pk_bf16_f32 v235, v22, v23\n"
    "v_mul_f32 v236, v236, v244\n"
    "v_mul_f32 v237, v237, v244\n"
    "v_mul_f32 v238, v238, v244\n"
    "v_mul_f32 v239, v239, v244\n"
    "v_mul_f32 v240, v240, v244\n"
    "v_mul_f32 v241, v241, v244\n"
    "v_mul_f32 v242, v242, v244\n"
    "v_mul_f32 v243, v243, v244\n"
    "v_mov_b32 v232, 0\n"
    "s_nop 1\n"
    "s_branch Agen2_%=\n"
    "Avr2_%=:\n"
    "s_mov_b64 s[64:65], 0\n"
    "v_cmp_eq_u32 vcc, 0xff81ff81, v192\n"
    "s_or_b64 s[64:65], s[64:65], vcc\n"
    "v_cmp_eq_u32 vcc, 0xff81ff81, v193\n"
    "s_or_b64 s[64:65], s[64:65], vcc\n"
    "v_cmp_eq_u32 vcc, 0xff81ff81, v194\n"
    "s_or_b64 s[64:65], s[64:65], vcc\n"
    "v_cmp_eq_u32 vcc, 0xff81ff81, v195\n"
    "s_or_b64 s[64:65], s[64:65], vcc\n"
    "s_cbranch_scc0 Avok2_%=\n"
    "s_add_u32 s42, s42, 1\n"
    "s_cmp_gt_u32 s42, 524288\n"
    "s_cbranch_scc1 Adead_%=\n"
    "global_load_dwordx2 v[192:193], %[vdg], s[52:53] offset:0 sc1\n"
    "global_load_dwordx2 v[194:195], %[vdg], s[52:53] offset:512 sc1\n"
    "s_waitcnt vmcnt(0)\n"
    "s_branch Avok_%=\n"
    "E648a_%=:\n"
    "s_add_u32 vcc_lo, s63, 110\n"
    "s_max_i32 vcc_lo, vcc_lo, 225\n"
    "s_cmp_gt_i32 s63, 54\n"
    "s_cselect_b32 s92, vcc_lo, 90\n"
    "s_add_u32 vcc_lo, s63, -22\n"
    "s_max_i32 vcc_lo, vcc_lo, 0\n"
    "s_cmp_eq_u32 s41, 2\n"
    "s_cselect_b32 s92, vcc_lo, s92\n"
    "s_add_u32 s38, s38, s92\n"
    "s_cmp_le_u32 s41, 2\n"
    "s_cbranch_scc1 E648b_%=\n"
    "v_readlane_b32 s92, v221, 16\n"
    "s_nop 0\n"
    "s_add_u32 s92, s92, s43\n"
    "s_sub_u32 s92, s92, s38\n"
    "s_max_i32 s92, s92, 0\n"
    "s_add_u32 s38, s38, s92\n"
    "s_add_u32 s39, s39, 14\n"
    "s_min_u32 s39, s39, 64\n"
    "s_branch E648b_%=\n"
    "E1503a_%=:\n"
    "s_add_u32 vcc_lo, s63, 110\n"
    "s_max_i32 vcc_lo, vcc_lo, 225\n"
    "s_cmp_gt_i32 s63, 54\n"
    "s_cselect_b32 s92, vcc_lo, 90\n"
    "s_add_u32 vcc_lo, s63, -22\n"
    "s_max_i32 vcc_lo, vcc_lo, 0\n"
    "s_cmp_eq_u32 s41, 2\n"
    "s_cselect_b32 s92, vcc_lo, s92\n"
    "s_add_u32 s62, s62, s92\n"
    "s_cmp_le_u32 s41, 2\n"
    "s_cbranch_scc1 E1503b_%=\n"
    "v_readlane_b32 s92, v221, 16\n"
    "s_nop 0\n"
    "s_add_u32 s92, s92, s43\n"
    "s_sub_u32 s92, s92, s62\n"
    "s_max_i32 s92, s92, 0\n"
    "s_add_u32 s62, s62, s92\n"
    "s_branch E1503b_%=\n"
    "Adead_%=:\n"
    "v_mov_b32 v254, 512\n"
    "v_mov_b32 v255, 2098432\n"
    "global_store_dword v255, v254, %[ws] offset:0 sc1\n"
    "s_waitcnt vmcnt(0)\n"
    "s_endpgm\n"
    "Aend_%=:\n"
    "s_endpgm\n"
    "s_mov_b32 m0, s99\n"
    : [vw] "+v"(vw), [vrd0] "+v"(vrd0), [vrd1] "+v"(vrd1), [vdg] "+v"(vdg)
    : [vlm] "v"(vlm), [viw] "v"(viw), [vir] "v"(vir), [vmv0] "v"(vmv0), [vmv2] "v"(vmv2), [vov] "v"(vov), [vg16] "v"(vg16),
      [vflv] "v"(vflv), [vidx] "v"(vidx), [vhv] "v"((C_HINT << 14) + (lane < 32 ? G * 8 + (lane & 15) * 64 : 1024 + lane * 32)), [ws] "s"(p.ws),
      [wq] "s"((const char*)p.wq + col), [wk] "s"((const char*)p.wk + col), [h] "s"(h), [w] "s"(w), [da] "s"(p.dA), [db] "s"(p.dB), [lb] "s"(lb)
    : "memory", "vcc", "scc", "v16", "v17", "v18", "v19", "v20", "v21", "v22", "v23", "v24", "v25", "v26", "v27", "v28", "v29", "v30", "v31", "v32", "v33", "v34", "v35", "v36", "v37", "v38", "v39", "v40", "v41", "v42", "v43", "v44", "v45", "v46", "v47", "v48", "v49", "v50", "v51", "v52", "v53", "v54", "v55", "v56", "v57", "v58", "v59", "v60", "v61", "v62", "v63", "v64", "v65", "v66", "v67", "v68", "v69", "v70", "v71", "v72", "v73", "v74", "v75", "v76", "v77", "v78", "v79", "v80", "v81", "v82", "v83", "v84", "v85", "v86", "v87", "v88", "v89", "v90", "v91", "v92", "v93", "v94", "v95", "v96", "v97", "v98", "v99", "v100", "v101", "v102", "v103", "v104", "v105", "v106", "v107", "v108", "v109", "v110", "v111", "v112", "v113", "v114", "v115", "v116", "v117", "v118", "v119", "v120", "v121", "v122", "v123", "v124", "v125", "v126", "v127", "v128", "v129", "v130", "v131", "v132", "v133", "v134", "v135", "v136", "v137", "v138", "v139", "v140", "v141", "v142", "v143", "v144", "v145", "v146", "v147", "v148", "v149", "v150", "v151", "v152", "v153", "v154", "v155", "v156", "v157", "v158", "v159", "v160", "v161", "v162", "v163", "v164", "v165", "v166", "v167", "v168", "v169", "v170", "v171", "v172", "v173", "v174", "v175", "v176", "v177", "v178", "v179", "v180", "v181", "v182", "v183", "v184", "v185", "v186", "v187", "v188", "v189", "v190", "v191", "v192", "v193", "v194", "v195", "v196", "v197", "v198", "v199", "v200", "v201", "v202", "v203", "v204", "v205", "v206", "v207", "v208", "v209", "v210", "v211", "v212", "v213", "v214", "v215", "v216", "v217", "v218", "v219", "v220", "v221", "v222", "v223", "v224", "v225", "v226", "v227", "v228", "v229", "v230", "v231", "v232", "v233", "v234", "v235", "v236", "v237", "v238", "v239", "v240", "v241", "v242", "v243", "v244", "v245", "v246", "v247", "v248", "v249", "v250", "v251", "v252", "v253", "v254", "v255", "a0", "a1", "a2", "a3", "a4", "a5", "a6", "a7", "a8", "a9", "a10", "a11", "a12", "a13", "a14", "a15", "a16", "a17", "a18", "a19", "a20", "a21", "a22", "a23", "a24", "a25", "a26", "a27", "a28", "a29", "a30", "a31", "a32", "a33", "a34", "a35", "a36", "a37", "a38", "a39", "a40", "a41", "a42", "a43", "a44", "a45", "a46", "a47", "a48", "a49", "a50", "a51", "a52", "a53", "a54", "a55", "a56", "a57", "a58", "a59", "a60", "a61", "a62", "a63", "a64", "a65", "a66", "a67", "a68", "a69", "a70", "a71", "a72", "a73", "a74", "a75", "a76", "a77", "a78", "a79", "a80", "a81", "a82", "a83", "a84", "a85", "a86", "a87", "a88", "a89", "a90", "a91", "a92", "a93", "a94", "a95", "a96", "a97", "a98", "a99", "a100", "a101", "a102", "a103", "a104", "a105", "a106", "a107", "a108", "a109", "a110", "a111", "a112", "a113", "a114", "a115", "a116", "a117", "a118", "a119", "a120", "a121", "a122", "a123", "a124", "a125", "a126", "a127", "a128", "a129", "a130", "a131", "a132", "a133", "a134", "a135", "a136", "a137", "a138", "a139", "a140", "a141", "a142", "a143", "a144", "a145", "a146", "a147", "a148", "a149", "a150", "a151", "a152", "a153", "a154", "a155", "a156", "a157", "a158", "a159", "a160", "a161", "a162", "a163", "a164", "a165", "a166", "a167", "a168", "a169", "a170", "a171", "a172", "a173", "a174", "a175", "a176", "a177", "a178", "a179", "a180", "a181", "a182", "a183", "a184", "a185", "a186", "a187", "a188", "a189", "a190", "a191", "a192", "a193", "a194", "a195", "a196", "a197", "a198", "a199", "a200", "a201", "a202", "a203", "a204", "a205", "a206", "a207", "a208", "a209", "a210", "a211", "a212", "a213", "a214", "a215", "a216", "a217", "a218", "a219", "a220", "a221", "a222", "a223", "a224", "a225", "a226", "a227", "a228", "a229", "a230", "a231", "a232", "a233", "a234", "a235", "a236", "a237", "a238", "a239", "a240", "a241", "a242", "a243", "a244", "a245", "a246", "a247", "a248", "a249", "a250", "a251", "a252", "a253", "a254", "a255", "s34", "s35", "s36", "s37", "s38", "s39", "s40", "s41", "s42", "s43", "s44", "s45", "s46", "s47", "s48", "s49", "s50", "s51", "s52", "s53", "s54", "s55", "s56", "s57", "s58", "s59", "s60", "s61", "s62", "s63", "s64", "s65", "s66", "s67", "s68", "s69", "s70", "s71", "s72", "s73", "s74", "s75", "s76", "s77", "s78", "s79", "s80", "s81", "s82", "s83", "s84", "s85", "s86", "s87", "s88", "s89", "s90", "s91", "s92", "s93", "s94", "s95", "s96", "s97", "s98", "s99", "s100", "s101");
}

DI void role_b(const Params& p, int c, int x, int vh, lptr L, int lane, int w) {
  const u32 lb = __builtin_amdgcn_readfirstlane((u32)(size_t)L);
  const int G = lane >> 4, q = (lane >> 2) & 3, sch = (lane & 3) ^ ((G & 1) << 1), tok = lane & 7, ph = (lane >> 3) & 1;
  const u32 vw = (u32)(16 * G + 8 * (sch >> 1) + 4 * (q >> 1) + (q & 1)) * 1280 + (sch & 1) * 16;
  const u32 vwb = csrc128(lane, w, 8192);
  const u32 rd = lb + w * 32768 + G * 256 + q * 64 + (lane & 3) * 8;
  const u32 vrd0 = rd + (G & 1) * 32, vrd1 = rd + ((G & 1) ^ 1) * 32;
  const int g = x >> 1;
  const u32 mo = c * 256 + g * 128, bi = g * 40 + c;
  const u32 vmv = (mo < 8192 ? (w << 14) + mo : (4 << 14) + w * 2048 + mo - 8192) + (((lane >> 3) & 3) * 2 + (lane & 1)) * 16 + ((lane & 7) >> 1) * 4;
  const u32 vg16 = lane * 16, vhv = (C_HINT << 14) + (lane < 32 ? G * 8 + (lane & 15) * 64 : 1024 + lane * 32), vidx = (q & 1) ? 0xEEEEu : 0x4444u, vidx2 = ph ? 0xEEEEu : 0x4444u;
  const u32 vrw = lb + (lane >> 1) * 48 + w * 8 + (lane & 1) * 4, vrr = lb + (lane & 31) * 48, vlm = lb + 4096 + lane * 16;
  const u32 vmv0 = (x << 14) + w * 1024 + G * 256 + (tok >> 2) * 128 + ((tok & 3) * 2 + ph) * 16;
  const u32 vmv2 = (4 << 14) + x * 2048 + (w >> 1) * 1024 + (2 * (w & 1) + (G & 1)) * 256 + (tok >> 2) * 128 + ((tok & 3) * 2 + ph) * 16;
  asm volatile(
    "s_mov_b32 s99, m0\n"
    "s_mov_b32 s80, 2097152\n"
    "s_load_dwordx2 s[80:81], %[ws], s80\n"
    "s_mov_b32 s43, 0\n"
    "v_mov_b32 v68, 0xff81ff81\n"
    "v_mov_b32 v69, 0xff81ff81\n"
    "v_lshrrev_b32 v70, 1, %[vg16]\n"
    "s_mov_b64 s[44:45], %[ws]\n"
    "s_mov_b32 exec_hi, 0\n"
    "s_add_u32 s46, s44, 1130496\n"
    "s_addc_u32 s47, s45, 0\n"
    "global_store_dword %[vmv], v68, s[46:47] offset:0 sc1\n"
    "s_add_u32 s46, s44, 1138688\n"
    "s_addc_u32 s47, s45, 0\n"
    "global_store_dword %[vmv], v68, s[46:47] offset:0 sc1\n"
    "s_add_u32 s46, s44, 1212416\n"
    "s_addc_u32 s47, s45, 0\n"
    "global_store_dword %[vmv], v68, s[46:47] offset:0 sc1\n"
    "s_add_u32 s46, s44, 1220608\n"
    "s_addc_u32 s47, s45, 0\n"
    "global_store_dword %[vmv], v68, s[46:47] offset:0 sc1\n"
    "s_mov_b32 exec_hi, -1\n"
    "s_lshr_b32 s41, %[vh], 2\n"
    "s_lshl_b32 s41, s41, 14\n"
    "s_and_b32 s42, %[vh], 3\n"
    "s_lshl_b32 s42, s42, 11\n"
    "s_add_u32 s67, s41, s42\n"
    "s_lshl_b32 s42, %[w], 9\n"
    "s_add_u32 s67, s67, s42\n"
    "s_add_u32 s67, s67, 1392640\n"
    "s_cmp_lt_i32 %[vh], 0\n"
    "s_cbranch_scc1 Bnov_%=\n"
    "s_add_u32 s48, s44, s67\n"
    "s_addc_u32 s49, s45, 0\n"
    "s_add_u32 s46, s48, 0\n"
    "s_addc_u32 s47, s49, 0\n"
    "global_store_dwordx2 v70, v[68:69], s[46:47] offset:0 sc1\n"
    "s_add_u32 s46, s48, 8192\n"
    "s_addc_u32 s47, s49, 0\n"
    "global_store_dwordx2 v70, v[68:69], s[46:47] offset:0 sc1\n"
    "s_add_u32 s46, s48, 262144\n"
    "s_addc_u32 s47, s49, 0\n"
    "global_store_dwordx2 v70, v[68:69], s[46:47] offset:0 sc1\n"
    "s_add_u32 s46, s48, 270336\n"
    "s_addc_u32 s47, s49, 0\n"
    "global_store_dwordx2 v70, v[68:69], s[46:47] offset:0 sc1\n"
    "s_lshl_b32 s60, %[w], 10\n"
    "s_add_u32 s60, s60, %[lb]\n"
    "v_add_u32 v73, 65536, %[vrdv]\n"
    "s_mov_b64 s[44:45], %[wv]\n"
    "s_add_u32 m0, s60, 0\n"
    "s_nop 0\n"
    "global_load_lds_dwordx4 %[vwb], s[44:45] offset:0 nt\n"
    "s_add_u32 s44, s44, 262144\n"
    "s_addc_u32 s45, s45, 0\n"
    "s_add_u32 m0, s60, 4096\n"
    "s_nop 0\n"
    "global_load_lds_dwordx4 %[vwb], s[44:45] offset:0 nt\n"
    "s_add_u32 s44, s44, 262144\n"
    "s_addc_u32 s45, s45, 0\n"
    "s_add_u32 m0, s60, 8192\n"
    "s_nop 0\n"
    "global_load_lds_dwordx4 %[vwb], s[44:45] offset:0 nt\n"
    "s_add_u32 s44, s44, 262144\n"
    "s_addc_u32 s45, s45, 0\n"
    "s_add_u32 m0, s60, 12288\n"
    "s_nop 0\n"
    "global_load_lds_dwordx4 %[vwb], s[44:45] offset:0 nt\n"
    "s_add_u32 s44, s44, 262144\n"
    "s_addc_u32 s45, s45, 0\n"
    "s_add_u32 m0, s60, 16384\n"
    "s_nop 0\n"
    "global_load_lds_dwordx4 %[vwb], s[44:45] offset:0 nt\n"
    "s_add_u32 s44, s44, 262144\n"
    "s_addc_u32 s45, s45, 0\n"
    "s_add_u32 m0, s60, 20480\n"
    "s_nop 0\n"
    "global_load_lds_dwordx4 %[vwb], s[44:45] offset:0 nt\n"
    "s_add_u32 s44, s44, 262144\n"
    "s_addc_u32 s45, s45, 0\n"
    "s_add_u32 m0, s60, 24576\n"
    "s_nop 0\n"
    "global_load_lds_dwordx4 %[vwb], s[44:45] offset:0 nt\n"
    "s_add_u32 s44, s44, 262144\n"
    "s_addc_u32 s45, s45, 0\n"
    "s_add_u32 m0, s60, 28672\n"
    "s_nop 0\n"
    "global_load_lds_dwordx4 %[vwb], s[44:45] offset:0 nt\n"
    "s_add_u32 s44, s44, 262144\n"
    "s_addc_u32 s45, s45, 0\n"
    "s_add_u32 m0, s60, 32768\n"
    "s_nop 0\n"
    "global_load_lds_dwordx4 %[vwb], s[44:45] offset:0 nt\n"
    "s_add_u32 s44, s44, 262144\n"
    "s_addc_u32 s45, s45, 0\n"
    "s_add_u32 m0, s60, 36864\n"
    "s_nop 0\n"
    "global_load_lds_dwordx4 %[vwb], s[44:45] offset:0 nt\n"
    "s_add_u32 s44, s44, 262144\n"
    "s_addc_u32 s45, s45, 0\n"
    "s_add_u32 m0, s60, 40960\n"
    "s_nop 0\n"
    "global_load_lds_dwordx4 %[vwb], s[44:45] offset:0 nt\n"
    "s_add_u32 s44, s44, 262144\n"
    "s_addc_u32 s45, s45, 0\n"
    "s_add_u32 m0, s60, 45056\n"
    "s_nop 0\n"
    "global_load_lds_dwordx4 %[vwb], s[44:45] offset:0 nt\n"
    "s_add_u32 s44, s44, 262144\n"
    "s_addc_u32 s45, s45, 0\n"
    "s_add_u32 m0, s60, 49152\n"
    "s_nop 0\n"
    "global_load_lds_dwordx4 %[vwb], s[44:45] offset:0 nt\n"
    "s_add_u32 s44, s44, 262144\n"
    "s_addc_u32 s45, s45, 0\n"
    "s_add_u32 m0, s60, 53248\n"
    "s_nop 0\n"
    "global_load_lds_dwordx4 %[vwb], s[44:45] offset:0 nt\n"
    "s_add_u32 s44, s44, 262144\n"
    "s_addc_u32 s45, s45, 0\n"
    "s_add_u32 m0, s60, 57344\n"
    "s_nop 0\n"
    "global_load_lds_dwordx4 %[vwb], s[44:45] offset:0 nt\n"
    "s_add_u32 s44, s44, 262144\n"
    "s_addc_u32 s45, s45, 0\n"
    "s_add_u32 m0, s60, 61440\n"
    "s_nop 0\n"
    "global_load_lds_dwordx4 %[vwb], s[44:45] offset:0 nt\n"
    "s_add_u32 s44, s44, 262144\n"
    "s_addc_u32 s45, s45, 0\n"
    "s_add_u32 m0, s60, 65536\n"
    "s_nop 0\n"
    "global_load_lds_dwordx4 %[vwb], s[44:45] offset:0 nt\n"
    "s_add_u32 s44, s44, 262144\n"
    "s_addc_u32 s45, s45, 0\n"
    "s_add_u32 m0, s60, 69632\n"
    "s_nop 0\n"
    "global_load_lds_dwordx4 %[vwb], s[44:45] offset:0 nt\n"
    "s_add_u32 s44, s44, 262144\n"
    "s_addc_u32 s45, s45, 0\n"
    "s_add_u32 m0, s60, 73728\n"
    "s_nop 0\n"
    "global_load_lds_dwordx4 %[vwb], s[44:45] offset:0 nt\n"
    "s_add_u32 s44, s44, 262144\n"
    "s_addc_u32 s45, s45, 0\n"
    "s_add_u32 m0, s60, 77824\n"
    "s_nop 0\n"
    "global_load_lds_dwordx4 %[vwb], s[44:45] offset:0 nt\n"
    "s_add_u32 s44, s44, 262144\n"
    "s_addc_u32 s45, s45, 0\n"
    "s_waitcnt vmcnt(10)\n"
    "s_waitcnt lgkmcnt(0)\n"
    "s_barrier\n"
    "ds_read_b64_tr_b16 a[128:129], %[vrdv] offset:0\n"
    "ds_read_b64_tr_b16 a[130:131], %[vrdv] offset:512\n"
    "ds_read_b64_tr_b16 a[132:133], %[vrdv] offset:4096\n"
    "ds_read_b64_tr_b16 a[134:135], %[vrdv] offset:4608\n"
    "ds_read_b64_tr_b16 a[136:137], %[vrdv] offset:8192\n"
    "ds_read_b64_tr_b16 a[138:139], %[vrdv] offset:8704\n"
    "ds_read_b64_tr_b16 a[140:141], %[vrdv] offset:12288\n"
    "ds_read_b64_tr_b16 a[142:143], %[vrdv] offset:12800\n"
    "ds_read_b64_tr_b16 a[144:145], %[vrdv] offset:16384\n"
    "ds_read_b64_tr_b16 a[146:147], %[vrdv] offset:16896\n"
    "ds_read_b64_tr_b16 a[148:149], %[vrdv] offset:20480\n"
    "ds_read_b64_tr_b16 a[150:151], %[vrdv] offset:20992\n"
    "ds_read_b64_tr_b16 a[152:153], %[vrdv] offset:24576\n"
    "ds_read_b64_tr_b16 a[154:155], %[vrdv] offset:25088\n"
    "ds_read_b64_tr_b16 a[156:157], %[vrdv] offset:28672\n"
    "ds_read_b64_tr_b16 a[158:159], %[vrdv] offset:29184\n"
    "ds_read_b64_tr_b16 a[160:161], %[vrdv] offset:32768\n"
    "ds_read_b64_tr_b16 a[162:163], %[vrdv] offset:33280\n"
    "ds_read_b64_tr_b16 a[164:165], %[vrdv] offset:36864\n"
    "ds_read_b64_tr_b16 a[166:167], %[vrdv] offset:37376\n"
    "s_waitcnt vmcnt(0)\n"
    "s_waitcnt lgkmcnt(0)\n"
    "s_barrier\n"
    "ds_read_b64_tr_b16 a[168:169], %[vrdv] offset:40960\n"
    "ds_read_b64_tr_b16 a[170:171], %[vrdv] offset:41472\n"
    "ds_read_b64_tr_b16 a[172:173], %[vrdv] offset:45056\n"
    "ds_read_b64_tr_b16 a[174:175], %[vrdv] offset:45568\n"
    "ds_read_b64_tr_b16 a[176:177], %[vrdv] offset:49152\n"
    "ds_read_b64_tr_b16 a[178:179], %[vrdv] offset:49664\n"
    "ds_read_b64_tr_b16 a[180:181], %[vrdv] offset:53248\n"
    "ds_read_b64_tr_b16 a[182:183], %[vrdv] offset:53760\n"
    "ds_read_b64_tr_b16 a[184:185], %[vrdv] offset:57344\n"
    "ds_read_b64_tr_b16 a[186:187], %[vrdv] offset:57856\n"
    "ds_read_b64_tr_b16 a[188:189], %[vrdv] offset:61440\n"
    "ds_read_b64_tr_b16 a[190:191], %[vrdv] offset:61952\n"
    "ds_read_b64_tr_b16 a[192:193], v73 offset:0\n"
    "ds_read_b64_tr_b16 a[194:195], v73 offset:512\n"
    "ds_read_b64_tr_b16 a[196:197], v73 offset:4096\n"
    "ds_read_b64_tr_b16 a[198:199], v73 offset:4608\n"
    "ds_read_b64_tr_b16 a[200:201], v73 offset:8192\n"
    "ds_read_b64_tr_b16 a[202:203], v73 offset:8704\n"
    "ds_read_b64_tr_b16 a[204:205], v73 offset:12288\n"
    "ds_read_b64_tr_b16 a[206:207], v73 offset:12800\n"
    "s_waitcnt lgkmcnt(0)\n"
    "s_barrier\n"
    "Bnov_%=:\n"
    "s_waitcnt lgkmcnt(0)\n"
    "s_add_u32 s80, s80, 1\n"
    "s_and_b32 s81, s81, -8\n"
    "v_lshlrev_b32 v80, 3, %[vg16]\n"
    "v_min_u32 v80, 640, v80\n"
    "v_add_u32 v80, 2146560, v80\n"
    "s_branch Binb_%=\n"
    "Bin_%=:\n"
    "s_sleep 4\n"
    "s_add_u32 s43, s43, 1\n"
    "s_cmp_gt_u32 s43, 524288\n"
    "s_cbranch_scc1 Bdead_%=\n"
    "Binb_%=:\n"
    "global_load_dwordx2 v[76:77], v80, %[ws] offset:0 sc1\n"
    "s_waitcnt vmcnt(0)\n"
    "v_mov_b32 v78, v77\n"
    "v_and_b32 v77, -8, v77\n"
    "v_cmp_ne_u64 vcc, s[80:81], v[76:77]\n"
    "s_cbranch_vccnz Bin_%=\n"
    "s_mov_b64 s[44:45], %[ws]\n"
    "s_add_u32 s50, s44, 1048576\n"
    "s_addc_u32 s51, s45, 0\n"
    "s_add_u32 s70, s50, 4096\n"
    "s_addc_u32 s71, s51, 0\n"
    "v_add_u32 v116, 1024, %[vhv]\n"
    "v_mov_b32 v117, %[vhv]\n"
    "s_mov_b32 exec_lo, 0\n"
    "v_and_b32 v117, 0xfffffdff, %[vhv]\n"
    "v_add_u32 v117, 2048, v117\n"
    "s_mov_b32 exec_lo, -1\n"
    "v_mov_b32 v118, %[bi]\n"
    "s_lshl_b32 s61, %[g], 2\n"
    "s_add_u32 s61, s61, %[w]\n"
    "s_lshl_b32 s61, s61, 14\n"
    "s_lshl_b32 s42, %[w], 9\n"
    "s_add_u32 s69, s42, %[lb]\n"
    "s_add_u32 s69, s69, 12288\n"
    "s_lshl_b32 s42, %[w], 10\n"
    "s_add_u32 s68, s42, %[lb]\n"
    "s_add_u32 s68, s68, 4096\n"
    "s_mov_b32 s87, 800\n"
    "s_mov_b32 s88, 568\n"
    "s_mov_b32 s77, 12\n"
    "s_add_u32 s89, s87, %[da]\n"
    "s_add_u32 s89, s89, s77\n"
    "s_lshr_b32 s89, s89, 3\n"
    "s_add_u32 s95, s88, %[db]\n"
    "s_add_u32 s95, s95, s77\n"
    "s_lshr_b32 s95, s95, 3\n"
    "s_add_u32 s95, s95, s89\n"
    "s_mov_b32 s91, 0\n"
    "s_mov_b32 s76, 0\n"
    "s_mov_b32 s62, -1\n"
    "s_mov_b32 s63, 0\n"
    "s_mov_b32 s40, 0\n"
    "s_mov_b32 s43, 0\n"
    "s_waitcnt lgkmcnt(0)\n"
    "s_memrealtime s[56:57]\n"
    "s_waitcnt lgkmcnt(0)\n"
    "s_mov_b32 s66, s56\n"
    "s_barrier\n"
    "Bloop_%=:\n"
    "s_cmp_lt_i32 %[vh], 0\n"
    "s_cbranch_scc1 Bskipv_%=\n"
    "s_cmp_lg_u32 %[w], 0\n"
    "s_cbranch_scc1 Bvrel_%=\n"
    "s_mov_b32 s82, s91\n"
    "s_mov_b32 s91, 0\n"
    "s_mov_b32 s83, s66\n"
    "s_sub_u32 s66, s66, 4\n"
    "Bvw_%=:\n"
    "s_memrealtime s[56:57]\n"
    "Bvwl_%=:\n"
    "s_memrealtime vcc\n"
    "s_waitcnt lgkmcnt(1)\n"
    "s_sub_u32 s59, s66, s56\n"
    "s_cmp_ge_u32 s59, 20000\n"
    "s_cbranch_scc1 Bvwy_%=\n"
    "s_memrealtime s[56:57]\n"
    "s_waitcnt lgkmcnt(1)\n"
    "s_sub_u32 s59, s66, vcc_lo\n"
    "s_cmp_ge_u32 s59, 20000\n"
    "s_cbranch_scc1 Bvwy_%=\n"
    "s_memrealtime vcc\n"
    "s_waitcnt lgkmcnt(1)\n"
    "s_sub_u32 s59, s66, s56\n"
    "s_cmp_ge_u32 s59, 20000\n"
    "s_cbranch_scc1 Bvwy_%=\n"
    "s_memrealtime s[56:57]\n"
    "s_waitcnt lgkmcnt(1)\n"
    "s_sub_u32 s59, s66, vcc_lo\n"
    "s_cmp_ge_u32 s59, 20000\n"
    "s_cbranch_scc1 Bvwy_%=\n"
    "s_memrealtime vcc\n"
    "s_waitcnt lgkmcnt(1)\n"
    "s_sub_u32 s59, s66, s56\n"
    "s_cmp_ge_u32 s59, 20000\n"
    "s_cbranch_scc1 Bvwy_%=\n"
    "s_memrealtime s[56:57]\n"
    "s_waitcnt lgkmcnt(1)\n"
    "s_sub_u32 s59, s66, vcc_lo\n"
    "s_cmp_ge_u32 s59, 20000\n"
    "s_cbranch_scc1 Bvwy_%=\n"
    "s_memrealtime vcc\n"
    "s_waitcnt lgkmcnt(1)\n"
    "s_sub_u32 s59, s66, s56\n"
    "s_cmp_ge_u32 s59, 20000\n"
    "s_cbranch_scc1 Bvwy_%=\n"
    "s_memrealtime s[56:57]\n"
    "s_waitcnt lgkmcnt(1)\n"
    "s_sub_u32 s59, s66, vcc_lo\n"
    "s_cmp_ge_u32 s59, 20000\n"
    "s_cbranch_scc1 Bvwy_%=\n"
    "s_branch Bvwl_%=\n"
    "Bvwy_%=:\n"
    "s_cmp_lt_u32 s59, -3\n"
    "s_cbranch_scc1 Bvwx_%=\n"
    "s_nop 2\n"
    "s_cmp_lt_u32 s59, -2\n"
    "s_cbranch_scc1 Bvwx_%=\n"
    "s_nop 2\n"
    "s_cmp_lt_u32 s59, -1\n"
    "s_cbranch_scc1 Bvwx_%=\n"
    "s_nop 2\n"
    "s_branch Bvwx_%=\n"
    "Bvwx_%=:\n"
    "s_waitcnt lgkmcnt(0)\n"
    "s_sub_u32 s66, s66, s59\n"
    "s_max_u32 s59, s59, -4\n"
    "s_add_u32 s66, s66, s59\n"
    "s_add_u32 s66, s66, 4\n"
    "Bvrel_%=:\n"
    "s_barrier\n"
    "s_mov_b32 m0, s68\n"
    "s_nop 0\n"
    "global_load_lds_dwordx4 %[vmv0], s[50:51] offset:0 sc1\n"
    "s_add_u32 m0, s68, 4096\n"
    "s_nop 0\n"
    "global_load_lds_dwordx4 %[vmv0], s[70:71] offset:0 sc1\n"
    "s_mov_b32 m0, s69\n"
    "s_mov_b32 exec_hi, 0\n"
    "global_load_lds_dwordx4 %[vmv2], s[50:51] offset:0 sc1\n"
    "s_mov_b32 exec_hi, -1\n"
    "s_and_b32 s41, s40, 3\n"
    "s_xor_b32 s41, s41, 2\n"
    "s_mul_i32 s41, s41, 81920\n"
    "s_add_u32 s41, s41, 1048576\n"
    "s_add_u32 s52, s44, s41\n"
    "s_addc_u32 s53, s45, 0\n"
    "s_and_b32 s41, s40, 1\n"
    "s_lshl_b32 s41, s41, 18\n"
    "s_add_u32 s42, s41, s67\n"
    "s_add_u32 s72, s44, s42\n"
    "s_addc_u32 s73, s45, 0\n"
    "s_xor_b32 s41, s41, 262144\n"
    "s_add_u32 s42, s41, s67\n"
    "s_add_u32 s74, s44, s42\n"
    "s_addc_u32 s75, s45, 0\n"
    "s_and_b32 s41, s40, 1\n"
    "s_lshl_b32 s41, s41, 4\n"
    "s_add_u32 s54, s44, s41\n"
    "s_addc_u32 s55, s45, 0\n"
    "s_xor_b32 s41, s41, 16\n"
    "s_add_u32 s78, s44, s41\n"
    "s_addc_u32 s79, s45, 0\n"
    "s_add_u32 s42, s40, 1\n"
    "s_lshl_b32 s85, s42, 1\n"
    "v_mov_b32 v56, 0\n"
    "v_mov_b32 v57, 0\n"
    "v_mov_b32 v58, 0\n"
    "v_mov_b32 v59, 0\n"
    "s_sleep 1\n"
    "global_store_dwordx2 v70, v[68:69], s[74:75] offset:0 sc1\n"
    "s_mov_b32 exec_hi, 0\n"
    "global_store_dword %[vmv], v68, s[52:53] offset:0 sc1\n"
    "s_mov_b32 exec_hi, -1\n"
    "s_waitcnt vmcnt(2)\n"
    "s_barrier\n"
    "ds_read_b128 v[16:19], %[vlm] offset:0\n"
    "ds_read_b128 v[20:23], %[vlm] offset:1024\n"
    "ds_read_b128 v[24:27], %[vlm] offset:2048\n"
    "ds_read_b128 v[28:31], %[vlm] offset:3072\n"
    "s_waitcnt lgkmcnt(3)\n"
    "v_smfmac_f32_16x16x64_bf16 v[56:59], v[16:19], a[128:135], %[vidx2]\n"
    "s_waitcnt lgkmcnt(2)\n"
    "ds_read_b128 v[32:35], %[vlm] offset:4096\n"
    "v_smfmac_f32_16x16x64_bf16 v[56:59], v[20:23], a[136:143], %[vidx2]\n"
    "s_waitcnt lgkmcnt(2)\n"
    "ds_read_b128 v[36:39], %[vlm] offset:5120\n"
    "v_smfmac_f32_16x16x64_bf16 v[56:59], v[24:27], a[144:151], %[vidx2]\n"
    "s_waitcnt lgkmcnt(2)\n"
    "ds_read_b128 v[40:43], %[vlm] offset:6144\n"
    "v_smfmac_f32_16x16x64_bf16 v[56:59], v[28:31], a[152:159], %[vidx2]\n"
    "s_waitcnt lgkmcnt(2)\n"
    "ds_read_b128 v[44:47], %[vlm] offset:7168\n"
    "v_smfmac_f32_16x16x64_bf16 v[56:59], v[32:35], a[160:167], %[vidx2]\n"
    "s_waitcnt lgkmcnt(2)\n"
    "ds_read_b128 v[48:51], %[vlm] offset:8192\n"
    "v_smfmac_f32_16x16x64_bf16 v[56:59], v[36:39], a[168:175], %[vidx2]\n"
    "s_waitcnt lgkmcnt(2)\n"
    "ds_read_b128 v[52:55], %[vlm] offset:9216\n"
    "v_smfmac_f32_16x16x64_bf16 v[56:59], v[40:43], a[176:183], %[vidx2]\n"
    "s_waitcnt lgkmcnt(2)\n"
    "v_smfmac_f32_16x16x64_bf16 v[56:59], v[44:47], a[184:191], %[vidx2]\n"
    "s_waitcnt lgkmcnt(1)\n"
    "v_smfmac_f32_16x16x64_bf16 v[56:59], v[48:51], a[192:199], %[vidx2]\n"
    "s_waitcnt lgkmcnt(0)\n"
    "v_smfmac_f32_16x16x64_bf16 v[56:59], v[52:55], a[200:207], %[vidx2]\n"
    "s_nop 7\n"
    "v_cmp_u_f32 s[56:57], v56, v57\n"
    "v_cmp_u_f32 vcc, v58, v59\n"
    "v_cvt_pk_bf16_f32 v74, v56, v57\n"
    "v_cvt_pk_bf16_f32 v75, v58, v59\n"
    "s_or_b64 vcc, vcc, s[56:57]\n"
    "s_cbranch_scc1 Bvslow_%=\n"
    "Bvok_%=:\n"
    "global_store_dwordx2 v70, v[74:75], s[72:73] offset:0 sc1\n"
    "Bopart_%=:\n"
    "s_cmp_eq_u32 s40, 0\n"
    "s_cbranch_scc1 Bwo_%=\n"
    "Bwo2_%=:\n"
    "s_cmp_lg_u32 %[w], 0\n"
    "s_cbranch_scc1 Bnr2_%=\n"
    "global_load_dwordx2 v[110:111], %[vhv], s[54:55] offset:0 sc1\n"
    "global_load_dwordx2 v[112:113], v116, s[54:55] offset:0 sc1\n"
    "global_load_dwordx2 v[114:115], v117, s[54:55] offset:0 sc1\n"
    "Bnr2_%=:\n"
    "s_cmp_eq_u32 s40, 63\n"
    "s_cbranch_scc1 Bfin_%=\n"
    "s_and_b32 s41, s40, 3\n"
    "s_lshl_b32 s41, s41, 18\n"
    "s_add_u32 s41, s41, s61\n"
    "s_add_u32 s46, s44, s41\n"
    "s_addc_u32 s47, s45, 0\n"
    "s_add_u32 s48, s46, 4096\n"
    "s_addc_u32 s49, s47, 0\n"
    "s_and_b32 s41, s42, 3\n"
    "s_mul_i32 s41, s41, 81920\n"
    "s_add_u32 s41, s41, 1048576\n"
    "s_add_u32 s50, s44, s41\n"
    "s_addc_u32 s51, s45, 0\n"
    "s_add_u32 s70, s50, 4096\n"
    "s_addc_u32 s71, s51, 0\n"
    "v_mov_b64 v[56:57], 0\n"
    "v_mov_b64 v[58:59], 0\n"
    "v_mov_b64 v[60:61], 0\n"
    "v_mov_b64 v[62:63], 0\n"
    "s_cmp_eq_u32 %[w], 1\n"
    "s_cbranch_scc1 Bawake_%=\n"
    "s_cmp_lg_u32 %[w], 0\n"
    "s_cbranch_scc1 Bgo_%=\n"
    "s_waitcnt vmcnt(0)\n"
    "s_branch Bpc_%=\n"
    "Bpoll_%=:\n"
    "s_sleep 4\n"
    "global_load_dwordx2 v[110:111], %[vhv], s[54:55] offset:0 sc1\n"
    "global_load_dwordx2 v[112:113], v116, s[54:55] offset:0 sc1\n"
    "global_load_dwordx2 v[114:115], v117, s[54:55] offset:0 sc1\n"
    "s_add_u32 s43, s43, 1\n"
    "s_cmp_gt_u32 s43, 2097152\n"
    "s_cbranch_scc1 Bdead_%=\n"
    "s_waitcnt vmcnt(0)\n"
    "Bpc_%=:\n"
    "v_min3_u32 v114, v110, v112, v114\n"
    "v_max_u32 v112, v110, v112\n"
    "v_mov_b32 v110, v114\n"
    "v_cmp_gt_u32 vcc, s85, v110\n"
    "s_cbranch_vccnz Bpoll_%=\n"
    "v_subrev_u32 v111, s66, v111\n"
    "v_subrev_u32 v113, s66, v113\n"
    "v_subrev_u32 v115, s66, v115\n"
    "v_max3_i32 v111, v111, v113, v115\n"
    "s_nop 1\n"
    "v_max_i32_dpp v111, v111, v111 row_ror:8 row_mask:0xf bank_mask:0xf\n"
    "s_nop 1\n"
    "v_max_i32_dpp v111, v111, v111 row_ror:4 row_mask:0xf bank_mask:0xf\n"
    "s_nop 1\n"
    "v_max_i32_dpp v111, v111, v111 row_ror:2 row_mask:0xf bank_mask:0xf\n"
    "s_nop 1\n"
    "v_max_i32_dpp v111, v111, v111 row_ror:1 row_mask:0xf bank_mask:0xf\n"
    "v_cmp_lt_u32 vcc, s85, v112\n"
    "v_readlane_b32 s98, v111, 0\n"
    "v_readlane_b32 s59, v111, 32\n"
    "v_readlane_b32 s41, v111, 48\n"
    "v_readlane_b32 s96, v111, 16\n"
    "s_max_i32 s59, s59, s41\n"
    "s_add_u32 s97, s59, s66\n"
    "s_add_u32 s96, s96, s66\n"
    "s_sub_u32 s59, s59, s98\n"
    "s_add_u32 s98, s98, s66\n"
    "s_and_b32 vcc_lo, vcc_lo, 0xffff\n"
    "s_cselect_b32 s41, 2, s59\n"
    "s_cmp_gt_i32 s41, 1\n"
    "s_cbranch_scc1 E479a_%=\n"
    "E479b_%=:\n"
    "s_add_u32 s58, s98, s89\n"
    ".p2align 5\n"
    "s_nop 0\n"
    "s_sub_u32 s58, s58, 4\n"
    "Bfw_%=:\n"
    "s_memrealtime s[56:57]\n"
    "Bfwl_%=:\n"
    "s_memrealtime vcc\n"
    "s_waitcnt lgkmcnt(1)\n"
    "s_sub_u32 s59, s58, s56\n"
    "s_cmp_ge_u32 s59, 20000\n"
    "s_cbranch_scc1 Bfwy_%=\n"
    "s_memrealtime s[56:57]\n"
    "s_waitcnt lgkmcnt(1)\n"
    "s_sub_u32 s59, s58, vcc_lo\n"
    "s_cmp_ge_u32 s59, 20000\n"
    "s_cbranch_scc1 Bfwy_%=\n"
    "s_memrealtime vcc\n"
    "s_waitcnt lgkmcnt(1)\n"
    "s_sub_u32 s59, s58, s56\n"
    "s_cmp_ge_u32 s59, 20000\n"
    "s_cbranch_scc1 Bfwy_%=\n"
    "s_memrealtime s[56:57]\n"
    "s_waitcnt lgkmcnt(1)\n"
    "s_sub_u32 s59, s58, vcc_lo\n"
    "s_cmp_ge_u32 s59, 20000\n"
    "s_cbranch_scc1 Bfwy_%=\n"
    "s_memrealtime vcc\n"
    "s_waitcnt lgkmcnt(1)\n"
    "s_sub_u32 s59, s58, s56\n"
    "s_cmp_ge_u32 s59, 20000\n"
    "s_cbranch_scc1 Bfwy_%=\n"
    "s_memrealtime s[56:57]\n"
    "s_waitcnt lgkmcnt(1)\n"
    "s_sub_u32 s59, s58, vcc_lo\n"
    "s_cmp_ge_u32 s59, 20000\n"
    "s_cbranch_scc1 Bfwy_%=\n"
    "s_memrealtime vcc\n"
    "s_waitcnt lgkmcnt(1)\n"
    "s_sub_u32 s59, s58, s56\n"
    "s_cmp_ge_u32 s59, 20000\n"
    "s_cbranch_scc1 Bfwy_%=\n"
    "s_memrealtime s[56:57]\n"
    "s_waitcnt lgkmcnt(1)\n"
    "s_sub_u32 s59, s58, vcc_lo\n"
    "s_cmp_ge_u32 s59, 20000\n"
    "s_cbranch_scc1 Bfwy_%=\n"
    "s_branch Bfwl_%=\n"
    "Bfwy_%=:\n"
    "s_cmp_lt_u32 s59, -3\n"
    "s_cbranch_scc1 Bfwx_%=\n"
    "s_nop 2\n"
    "s_cmp_lt_u32 s59, -2\n"
    "s_cbranch_scc1 Bfwx_%=\n"
    "s_nop 2\n"
    "s_cmp_lt_u32 s59, -1\n"
    "s_cbranch_scc1 Bfwx_%=\n"
    "s_nop 2\n"
    "s_branch Bfwx_%=\n"
    "Bfwx_%=:\n"
    "s_waitcnt lgkmcnt(0)\n"
    "s_sub_u32 s58, s58, s59\n"
    "s_max_u32 s59, s59, -4\n"
    "s_add_u32 s58, s58, s59\n"
    "s_add_u32 s58, s58, 4\n"
    "Bgo_%=:\n"
    "s_barrier\n"
    "global_load_dwordx4 v[16:19], %[vg16], s[46:47] offset:0 sc1\n"
    "s_mov_b32 s90, s95\n"
    "s_add_u32 s66, s98, s90\n"
    "s_add_u32 s66, s66, %[dp]\n"
    "s_mov_b64 s[92:93], -1\n"
    "s_add_u32 s41, s85, 2\n"
    "v_mov_b32 v66, s41\n"
    "s_sub_u32 s97, s97, s94\n"
    "s_nop 3\n"
    "global_load_dwordx4 v[20:23], %[vg16], s[46:47] offset:1024 sc1\n"
    "s_sub_u32 s96, s96, s86\n"
    "s_lshl_b32 s96, s96, 3\n"
    "s_sub_u32 s96, s96, s87\n"
    "s_cmp_lt_i32 s96, -160\n"
    "s_cselect_b32 s96, 0, s96\n"
    "s_cmp_le_u32 s85, 4\n"
    "s_cselect_b32 s96, 0, s96\n"
    "s_nop 3\n"
    "global_load_dwordx4 v[24:27], %[vg16], s[46:47] offset:2048 sc1\n"
    "s_min_i32 s96, s96, 32\n"
    "s_max_i32 s96, s96, -48\n"
    "s_ashr_i32 s96, s96, 2\n"
    "s_add_u32 s87, s87, s96\n"
    "s_sub_u32 s97, s97, s86\n"
    "s_lshl_b32 s97, s97, 3\n"
    "s_sub_u32 s97, s97, s88\n"
    "s_nop 3\n"
    "global_load_dwordx4 v[28:31], %[vg16], s[46:47] offset:3072 sc1\n"
    "s_cmp_le_u32 s85, 4\n"
    "s_cselect_b32 s97, 0, s97\n"
    "s_min_i32 s97, s97, 32\n"
    "s_max_i32 s97, s97, -48\n"
    "s_ashr_i32 s97, s97, 2\n"
    "s_add_u32 s88, s88, s97\n"
    "s_mov_b32 s86, s98\n"
    "s_nop 3\n"
    "global_load_dwordx4 v[32:35], %[vg16], s[48:49] offset:0 sc1\n"
    "s_mov_b32 s94, s89\n"
    "s_sub_u32 s77, s77, 1\n"
    "s_max_i32 s77, s77, 0\n"
    "s_add_u32 s89, s87, %[da]\n"
    "s_add_u32 s89, s89, s77\n"
    "s_lshr_b32 s89, s89, 3\n"
    "s_add_u32 s95, s88, %[db]\n"
    "s_nop 3\n"
    "global_load_dwordx4 v[36:39], %[vg16], s[48:49] offset:1024 sc1\n"
    "s_add_u32 s95, s95, s77\n"
    "s_lshr_b32 s95, s95, 3\n"
    "s_add_u32 s95, s95, s89\n"
    "s_nop 7\n"
    "global_load_dwordx4 v[40:43], %[vg16], s[48:49] offset:2048 sc1\n"
    "s_nop 10\n"
    "global_load_dwordx4 v[44:47], %[vg16], s[48:49] offset:3072 sc1\n"
    "s_waitcnt vmcnt(7)\n"
    "v_smfmac_f32_16x16x64_bf16 v[56:59], v[16:19], a[0:7], %[vidx]\n"
    "v_max3_u32 v64, v16, v17, v18\n"
    "v_smfmac_f32_16x16x64_bf16 v[60:63], v[16:19], a[8:15], %[vidx]\n"
    "v_max_u32 v64, v19, v64\n"
    "v_cmp_le_u32 vcc, v68, v64\n"
    "s_cbranch_vccnz Bbad0_%=\n"
    "Bnext0_%=:\n"
    "s_waitcnt vmcnt(6)\n"
    "v_smfmac_f32_16x16x64_bf16 v[56:59], v[20:23], a[16:23], %[vidx]\n"
    "v_max3_u32 v64, v20, v21, v22\n"
    "v_smfmac_f32_16x16x64_bf16 v[60:63], v[20:23], a[24:31], %[vidx]\n"
    "v_max_u32 v64, v23, v64\n"
    "v_cmp_le_u32 vcc, v68, v64\n"
    "s_cbranch_vccnz Bbad1_%=\n"
    "Bnext1_%=:\n"
    "s_waitcnt vmcnt(5)\n"
    "v_smfmac_f32_16x16x64_bf16 v[56:59], v[24:27], a[32:39], %[vidx]\n"
    "v_max3_u32 v64, v24, v25, v26\n"
    "v_smfmac_f32_16x16x64_bf16 v[60:63], v[24:27], a[40:47], %[vidx]\n"
    "v_max_u32 v64, v27, v64\n"
    "v_cmp_le_u32 vcc, v68, v64\n"
    "s_cbranch_vccnz Bbad2_%=\n"
    "Bnext2_%=:\n"
    "s_waitcnt vmcnt(4)\n"
    "v_smfmac_f32_16x16x64_bf16 v[56:59], v[28:31], a[48:55], %[vidx]\n"
    "v_smfmac_f32_16x16x64_bf16 v[60:63], v[28:31], a[56:63], %[vidx]\n"
    "v_mfma_f32_16x16x32_bf16 v[96:99], v[48:51], v[48:51], v[96:99]\n"
    "s_waitcnt vmcnt(3)\n"
    "v_smfmac_f32_16x16x64_bf16 v[56:59], v[32:35], a[64:71], %[vidx]\n"
    "v_smfmac_f32_16x16x64_bf16 v[60:63], v[32:35], a[72:79], %[vidx]\n"
    "v_mfma_f32_16x16x32_bf16 v[96:99], v[48:51], v[48:51], v[96:99]\n"
    "s_waitcnt vmcnt(2)\n"
    "v_smfmac_f32_16x16x64_bf16 v[56:59], v[36:39], a[80:87], %[vidx]\n"
    "v_smfmac_f32_16x16x64_bf16 v[60:63], v[36:39], a[88:95], %[vidx]\n"
    "v_mfma_f32_16x16x32_bf16 v[96:99], v[48:51], v[48:51], v[96:99]\n"
    "s_waitcnt vmcnt(1)\n"
    "v_smfmac_f32_16x16x64_bf16 v[56:59], v[40:43], a[96:103], %[vidx]\n"
    "v_smfmac_f32_16x16x64_bf16 v[60:63], v[40:43], a[104:111], %[vidx]\n"
    "v_mfma_f32_16x16x32_bf16 v[96:99], v[48:51], v[48:51], v[96:99]\n"
    "s_waitcnt vmcnt(0)\n"
    "v_smfmac_f32_16x16x64_bf16 v[56:59], v[44:47], a[112:119], %[vidx]\n"
    "v_smfmac_f32_16x16x64_bf16 v[60:63], v[44:47], a[120:127], %[vidx]\n"
    "Bok_%=:\n"
    "s_nop 7\n"
    "v_permlane32_swap_b32 v60, v58\n"
    "v_permlane32_swap_b32 v61, v59\n"
    "v_add_f32 v56, v56, v58\n"
    "v_add_f32 v57, v57, v59\n"
    "s_nop 0\n"
    "v_add_f32_dpp v56, v62, v60 quad_perm:[0,1,2,3] row_mask:0xc bank_mask:0xf\n"
    "v_add_f32_dpp v57, v63, v61 quad_perm:[0,1,2,3] row_mask:0xc bank_mask:0xf\n"
    "s_nop 1\n"
    "v_permlane16_swap_b32 v56, v57\n"
    "v_add_f32 v56, v56, v57\n"
    "ds_write_b32 %[vrw], v56 offset:0\n"
    "v_cmp_u_f32 vcc, v56, v56\n"
    "v_mfma_f32_16x16x32_bf16 v[96:99], v[48:51], v[48:51], v[96:99]\n"
    "s_and_b64 vcc, vcc, s[92:93]\n"
    "s_cbranch_scc1 Bnan_%=\n"
    "v_mfma_f32_16x16x32_bf16 v[96:99], v[48:51], v[48:51], v[96:99]\n"
    "s_cmp_lg_u32 %[w], 0\n"
    "s_nop 1\n"
    "v_mfma_f32_16x16x32_bf16 v[96:99], v[48:51], v[48:51], v[96:99]\n"
    "s_waitcnt lgkmcnt(0)\n"
    "s_barrier\n"
    "ds_read_b128 v[74:77], %[vrr] offset:0\n"
    "ds_read_b128 v[78:81], %[vrr] offset:16\n"
    "v_mov_b32 v93, v92\n"
    "s_mov_b32 exec_hi, 0\n"
    "s_nop 0\n"
    "v_mov_b32 v93, v92\n"
    "s_nop 1\n"
    "v_mov_b32 v93, v92\n"
    "s_nop 1\n"
    "v_mov_b32 v93, v92\n"
    "s_waitcnt lgkmcnt(1)\n"
    "v_pk_add_f32 v[74:75], v[74:75], v[76:77]\n"
    "s_waitcnt lgkmcnt(0)\n"
    "v_pk_add_f32 v[78:79], v[78:79], v[80:81]\n"
    "v_pk_add_f32 v[74:75], v[74:75], v[78:79]\n"
    "v_cvt_pk_bf16_f32 v74, v74, v75\n"
    "s_memrealtime s[56:57]\n"
    "global_store_dword %[vmv], v74, s[50:51] offset:0 sc1\n"
    "s_mov_b32 exec_hi, -1\n"
    "s_cbranch_scc1 Bnext_%=\n"
    "s_waitcnt lgkmcnt(0)\n"
    "v_mov_b32 v67, s56\n"
    "s_sub_u32 s41, s56, s66\n"
    "s_cmp_gt_i32 s41, 4\n"
    "s_cbranch_scc1 Blate_%=\n"
    "s_mov_b64 exec, 1\n"
    "global_store_dwordx2 v118, v[66:67], s[78:79] offset:0 sc1\n"
    "s_mov_b64 exec, -1\n"
    "Bnext_%=:\n"
    "s_add_u32 s40, s40, 1\n"
    "s_branch Bloop_%=\n"
    "Bwo_%=:\n"
    "s_cmp_lt_i32 %[vh], 0\n"
    "s_cbranch_scc1 Bwo0_%=\n"
    "s_add_u32 s72, s72, 8192\n"
    "s_addc_u32 s73, s73, 0\n"
    "global_store_dwordx2 v70, v[74:75], s[72:73] offset:0 sc1\n"
    "Bwo0_%=:\n"
    "s_waitcnt vmcnt(4)\n"
    "s_waitcnt lgkmcnt(0)\n"
    "s_barrier\n"
    "s_cmp_lg_u32 %[w], 0\n"
    "s_cbranch_scc1 Bwo1_%=\n"
    "s_add_u32 s46, s54, 8192\n"
    "s_addc_u32 s47, s55, 0\n"
    "v_mov_b32 v66, 1\n"
    "v_mov_b32 v67, s66\n"
    "s_mov_b64 exec, 1\n"
    "global_store_dwordx2 v118, v[66:67], s[54:55] offset:0 sc1\n"
    "global_store_dwordx2 v118, v[66:67], s[46:47] offset:0 sc1\n"
    "s_mov_b64 exec, -1\n"
    "Bwo1_%=:\n"
    "s_lshl_b32 s60, %[w], 15\n"
    "s_add_u32 s60, s60, %[lb]\n"
    "v_add_u32 v72, 1536, %[vw]\n"
    "s_mov_b64 s[46:47], %[wo]\n"
    "s_lshr_b32 s41, %[bi], 5\n"
    "s_add_u32 s41, s41, 0\n"
    "s_and_b32 s41, s41, 3\n"
    "s_lshl_b32 s84, s41, 13\n"
    "s_add_u32 s84, s84, s60\n"
    "s_mul_i32 s41, s41, 327680\n"
    "s_add_u32 s48, s46, s41\n"
    "s_addc_u32 s49, s47, 0\n"
    "s_add_u32 m0, s84, 0\n"
    "s_nop 0\n"
    "global_load_lds_dwordx4 %[vw], s[48:49] offset:0\n"
    "global_load_lds_dwordx4 v72, s[48:49] offset:1024\n"
    "s_add_u32 s48, s48, 81920\n"
    "s_addc_u32 s49, s49, 0\n"
    "s_add_u32 m0, s84, 2048\n"
    "s_nop 0\n"
    "global_load_lds_dwordx4 %[vw], s[48:49] offset:0\n"
    "global_load_lds_dwordx4 v72, s[48:49] offset:1024\n"
    "s_add_u32 s48, s48, 81920\n"
    "s_addc_u32 s49, s49, 0\n"
    "s_add_u32 m0, s84, 4096\n"
    "s_nop 0\n"
    "global_load_lds_dwordx4 %[vw], s[48:49] offset:0\n"
    "global_load_lds_dwordx4 v72, s[48:49] offset:1024\n"
    "s_add_u32 s48, s48, 81920\n"
    "s_addc_u32 s49, s49, 0\n"
    "s_add_u32 m0, s84, 6144\n"
    "s_nop 0\n"
    "global_load_lds_dwordx4 %[vw], s[48:49] offset:0\n"
    "global_load_lds_dwordx4 v72, s[48:49] offset:1024\n"
    "s_add_u32 s48, s48, 81920\n"
    "s_addc_u32 s49, s49, 0\n"
    "s_lshr_b32 s41, %[bi], 5\n"
    "s_add_u32 s41, s41, 1\n"
    "s_and_b32 s41, s41, 3\n"
    "s_lshl_b32 s84, s41, 13\n"
    "s_add_u32 s84, s84, s60\n"
    "s_mul_i32 s41, s41, 327680\n"
    "s_add_u32 s48, s46, s41\n"
    "s_addc_u32 s49, s47, 0\n"
    "s_add_u32 m0, s84, 0\n"
    "s_nop 0\n"
    "global_load_lds_dwordx4 %[vw], s[48:49] offset:0\n"
    "global_load_lds_dwordx4 v72, s[48:49] offset:1024\n"
    "s_add_u32 s48, s48, 81920\n"
    "s_addc_u32 s49, s49, 0\n"
    "s_add_u32 m0, s84, 2048\n"
    "s_nop 0\n"
    "global_load_lds_dwordx4 %[vw], s[48:49] offset:0\n"
    "global_load_lds_dwordx4 v72, s[48:49] offset:1024\n"
    "s_add_u32 s48, s48, 81920\n"
    "s_addc_u32 s49, s49, 0\n"
    "s_add_u32 m0, s84, 4096\n"
    "s_nop 0\n"
    "global_load_lds_dwordx4 %[vw], s[48:49] offset:0\n"
    "global_load_lds_dwordx4 v72, s[48:49] offset:1024\n"
    "s_add_u32 s48, s48, 81920\n"
    "s_addc_u32 s49, s49, 0\n"
    "s_add_u32 m0, s84, 6144\n"
    "s_nop 0\n"
    "global_load_lds_dwordx4 %[vw], s[48:49] offset:0\n"
    "global_load_lds_dwordx4 v72, s[48:49] offset:1024\n"
    "s_add_u32 s48, s48, 81920\n"
    "s_addc_u32 s49, s49, 0\n"
    "s_lshr_b32 s41, %[bi], 5\n"
    "s_add_u32 s41, s41, 2\n"
    "s_and_b32 s41, s41, 3\n"
    "s_lshl_b32 s84, s41, 13\n"
    "s_add_u32 s84, s84, s60\n"
    "s_mul_i32 s41, s41, 327680\n"
    "s_add_u32 s48, s46, s41\n"
    "s_addc_u32 s49, s47, 0\n"
    "s_add_u32 m0, s84, 0\n"
    "s_nop 0\n"
    "global_load_lds_dwordx4 %[vw], s[48:49] offset:0\n"
    "global_load_lds_dwordx4 v72, s[48:49] offset:1024\n"
    "s_add_u32 s48, s48, 81920\n"
    "s_addc_u32 s49, s49, 0\n"
    "s_add_u32 m0, s84, 2048\n"
    "s_nop 0\n"
    "global_load_lds_dwordx4 %[vw], s[48:49] offset:0\n"
    "global_load_lds_dwordx4 v72, s[48:49] offset:1024\n"
    "s_add_u32 s48, s48, 81920\n"
    "s_addc_u32 s49, s49, 0\n"
    "s_add_u32 m0, s84, 4096\n"
    "s_nop 0\n"
    "global_load_lds_dwordx4 %[vw], s[48:49] offset:0\n"
    "global_load_lds_dwordx4 v72, s[48:49] offset:1024\n"
    "s_add_u32 s48, s48, 81920\n"
    "s_addc_u32 s49, s49, 0\n"
    "s_add_u32 m0, s84, 6144\n"
    "s_nop 0\n"
    "global_load_lds_dwordx4 %[vw], s[48:49] offset:0\n"
    "global_load_lds_dwordx4 v72, s[48:49] offset:1024\n"
    "s_add_u32 s48, s48, 81920\n"
    "s_addc_u32 s49, s49, 0\n"
    "s_lshr_b32 s41, %[bi], 5\n"
    "s_add_u32 s41, s41, 3\n"
    "s_and_b32 s41, s41, 3\n"
    "s_lshl_b32 s84, s41, 13\n"
    "s_add_u32 s84, s84, s60\n"
    "s_mul_i32 s41, s41, 327680\n"
    "s_add_u32 s48, s46, s41\n"
    "s_addc_u32 s49, s47, 0\n"
    "s_add_u32 m0, s84, 0\n"
    "s_nop 0\n"
    "global_load_lds_dwordx4 %[vw], s[48:49] offset:0\n"
    "global_load_lds_dwordx4 v72, s[48:49] offset:1024\n"
    "s_add_u32 s48, s48, 81920\n"
    "s_addc_u32 s49, s49, 0\n"
    "s_add_u32 m0, s84, 2048\n"
    "s_nop 0\n"
    "global_load_lds_dwordx4 %[vw], s[48:49] offset:0\n"
    "global_load_lds_dwordx4 v72, s[48:49] offset:1024\n"
    "s_add_u32 s48, s48, 81920\n"
    "s_addc_u32 s49, s49, 0\n"
    "s_add_u32 m0, s84, 4096\n"
    "s_nop 0\n"
    "global_load_lds_dwordx4 %[vw], s[48:49] offset:0\n"
    "global_load_lds_dwordx4 v72, s[48:49] offset:1024\n"
    "s_add_u32 s48, s48, 81920\n"
    "s_addc_u32 s49, s49, 0\n"
    "s_add_u32 m0, s84, 6144\n"
    "s_nop 0\n"
    "global_load_lds_dwordx4 %[vw], s[48:49] offset:0\n"
    "global_load_lds_dwordx4 v72, s[48:49] offset:1024\n"
    "s_add_u32 s48, s48, 81920\n"
    "s_addc_u32 s49, s49, 0\n"
    "s_waitcnt vmcnt(0)\n"
    "ds_read_b64_tr_b16 a[0:1], %[vrd0] offset:0\n"
    "ds_read_b64_tr_b16 a[2:3], %[vrd0] offset:1024\n"
    "ds_read_b64_tr_b16 a[8:9], %[vrd1] offset:0\n"
    "ds_read_b64_tr_b16 a[10:11], %[vrd1] offset:1024\n"
    "ds_read_b64_tr_b16 a[4:5], %[vrd0] offset:2048\n"
    "ds_read_b64_tr_b16 a[6:7], %[vrd0] offset:3072\n"
    "ds_read_b64_tr_b16 a[12:13], %[vrd1] offset:2048\n"
    "ds_read_b64_tr_b16 a[14:15], %[vrd1] offset:3072\n"
    "ds_read_b64_tr_b16 a[16:17], %[vrd0] offset:4096\n"
    "ds_read_b64_tr_b16 a[18:19], %[vrd0] offset:5120\n"
    "ds_read_b64_tr_b16 a[24:25], %[vrd1] offset:4096\n"
    "ds_read_b64_tr_b16 a[26:27], %[vrd1] offset:5120\n"
    "ds_read_b64_tr_b16 a[20:21], %[vrd0] offset:6144\n"
    "ds_read_b64_tr_b16 a[22:23], %[vrd0] offset:7168\n"
    "ds_read_b64_tr_b16 a[28:29], %[vrd1] offset:6144\n"
    "ds_read_b64_tr_b16 a[30:31], %[vrd1] offset:7168\n"
    "ds_read_b64_tr_b16 a[32:33], %[vrd0] offset:8192\n"
    "ds_read_b64_tr_b16 a[34:35], %[vrd0] offset:9216\n"
    "ds_read_b64_tr_b16 a[40:41], %[vrd1] offset:8192\n"
    "ds_read_b64_tr_b16 a[42:43], %[vrd1] offset:9216\n"
    "ds_read_b64_tr_b16 a[36:37], %[vrd0] offset:10240\n"
    "ds_read_b64_tr_b16 a[38:39], %[vrd0] offset:11264\n"
    "ds_read_b64_tr_b16 a[44:45], %[vrd1] offset:10240\n"
    "ds_read_b64_tr_b16 a[46:47], %[vrd1] offset:11264\n"
    "ds_read_b64_tr_b16 a[48:49], %[vrd0] offset:12288\n"
    "ds_read_b64_tr_b16 a[50:51], %[vrd0] offset:13312\n"
    "ds_read_b64_tr_b16 a[56:57], %[vrd1] offset:12288\n"
    "ds_read_b64_tr_b16 a[58:59], %[vrd1] offset:13312\n"
    "ds_read_b64_tr_b16 a[52:53], %[vrd0] offset:14336\n"
    "ds_read_b64_tr_b16 a[54:55], %[vrd0] offset:15360\n"
    "ds_read_b64_tr_b16 a[60:61], %[vrd1] offset:14336\n"
    "ds_read_b64_tr_b16 a[62:63], %[vrd1] offset:15360\n"
    "ds_read_b64_tr_b16 a[64:65], %[vrd0] offset:16384\n"
    "ds_read_b64_tr_b16 a[66:67], %[vrd0] offset:17408\n"
    "ds_read_b64_tr_b16 a[72:73], %[vrd1] offset:16384\n"
    "ds_read_b64_tr_b16 a[74:75], %[vrd1] offset:17408\n"
    "ds_read_b64_tr_b16 a[68:69], %[vrd0] offset:18432\n"
    "ds_read_b64_tr_b16 a[70:71], %[vrd0] offset:19456\n"
    "ds_read_b64_tr_b16 a[76:77], %[vrd1] offset:18432\n"
    "ds_read_b64_tr_b16 a[78:79], %[vrd1] offset:19456\n"
    "ds_read_b64_tr_b16 a[80:81], %[vrd0] offset:20480\n"
    "ds_read_b64_tr_b16 a[82:83], %[vrd0] offset:21504\n"
    "ds_read_b64_tr_b16 a[88:89], %[vrd1] offset:20480\n"
    "ds_read_b64_tr_b16 a[90:91], %[vrd1] offset:21504\n"
    "ds_read_b64_tr_b16 a[84:85], %[vrd0] offset:22528\n"
    "ds_read_b64_tr_b16 a[86:87], %[vrd0] offset:23552\n"
    "ds_read_b64_tr_b16 a[92:93], %[vrd1] offset:22528\n"
    "ds_read_b64_tr_b16 a[94:95], %[vrd1] offset:23552\n"
    "ds_read_b64_tr_b16 a[96:97], %[vrd0] offset:24576\n"
    "ds_read_b64_tr_b16 a[98:99], %[vrd0] offset:25600\n"
    "ds_read_b64_tr_b16 a[104:105], %[vrd1] offset:24576\n"
    "ds_read_b64_tr_b16 a[106:107], %[vrd1] offset:25600\n"
    "ds_read_b64_tr_b16 a[100:101], %[vrd0] offset:26624\n"
    "ds_read_b64_tr_b16 a[102:103], %[vrd0] offset:27648\n"
    "ds_read_b64_tr_b16 a[108:109], %[vrd1] offset:26624\n"
    "ds_read_b64_tr_b16 a[110:111], %[vrd1] offset:27648\n"
    "ds_read_b64_tr_b16 a[112:113], %[vrd0] offset:28672\n"
    "ds_read_b64_tr_b16 a[114:115], %[vrd0] offset:29696\n"
    "ds_read_b64_tr_b16 a[120:121], %[vrd1] offset:28672\n"
    "ds_read_b64_tr_b16 a[122:123], %[vrd1] offset:29696\n"
    "ds_read_b64_tr_b16 a[116:117], %[vrd0] offset:30720\n"
    "ds_read_b64_tr_b16 a[118:119], %[vrd0] offset:31744\n"
    "ds_read_b64_tr_b16 a[124:125], %[vrd1] offset:30720\n"
    "ds_read_b64_tr_b16 a[126:127], %[vrd1] offset:31744\n"
    "v_lshlrev_b32 v80, 3, %[vg16]\n"
    "v_min_u32 v80, 128, v80\n"
    "v_add_u32 v80, 2146304, v80\n"
    "s_branch Bi2b_%=\n"
    "Bi2_%=:\n"
    "s_sleep 4\n"
    "s_add_u32 s43, s43, 1\n"
    "s_cmp_gt_u32 s43, 524288\n"
    "s_cbranch_scc1 Bdead_%=\n"
    "Bi2b_%=:\n"
    "global_load_dwordx2 v[76:77], v80, %[ws] offset:0 sc1\n"
    "s_waitcnt vmcnt(0)\n"
    "v_mov_b32 v78, v77\n"
    "v_and_b32 v77, -8, v77\n"
    "v_cmp_ne_u64 vcc, s[80:81], v[76:77]\n"
    "s_cbranch_vccnz Bi2_%=\n"
    "v_readlane_b32 s41, v78, 1\n"
    "s_nop 4\n"
    "s_and_b32 s41, s41, 1\n"
    "s_lshl_b32 s41, s41, 13\n"
    "s_add_u32 s44, s44, s41\n"
    "s_addc_u32 s45, s45, 0\n"
    "s_add_u32 s54, s54, s41\n"
    "s_addc_u32 s55, s55, 0\n"
    "s_add_u32 s78, s78, s41\n"
    "s_addc_u32 s79, s79, 0\n"
    "s_waitcnt lgkmcnt(0)\n"
    "s_barrier\n"
    "s_cmp_lg_u32 %[w], 1\n"
    "s_cbranch_scc1 Bwo3_%=\n"
    "s_memrealtime s[56:57]\n"
    "s_waitcnt lgkmcnt(0)\n"
    "v_mov_b32 v66, 2\n"
    "v_mov_b32 v67, s56\n"
    "s_mov_b64 exec, 1\n"
    "global_store_dwordx2 v118, v[66:67], s[54:55] offset:0 sc1\n"
    "s_mov_b64 exec, -1\n"
    "Bwo3_%=:\n"
    "s_branch Bwo2_%=\n"
    "Blate_%=:\n"
    "global_load_dwordx2 v[110:111], %[vhv], s[54:55] offset:0 sc1\n"
    "global_load_dwordx2 v[112:113], v116, s[54:55] offset:0 sc1\n"
    "global_load_dwordx2 v[114:115], v117, s[54:55] offset:0 sc1\n"
    "s_mov_b64 exec, 1\n"
    "global_store_dwordx2 v118, v[66:67], s[78:79] offset:0 sc1\n"
    "s_mov_b64 exec, -1\n"
    "s_waitcnt vmcnt(1)\n"
    "v_min3_u32 v114, v110, v112, v114\n"
    "v_max_u32 v112, v110, v112\n"
    "v_mov_b32 v110, v114\n"
    "v_subrev_u32 v111, s66, v111\n"
    "v_subrev_u32 v113, s66, v113\n"
    "v_subrev_u32 v115, s66, v115\n"
    "v_max3_i32 v111, v111, v113, v115\n"
    "s_nop 1\n"
    "v_max_i32_dpp v111, v111, v111 row_ror:8 row_mask:0xf bank_mask:0xf\n"
    "s_nop 1\n"
    "v_max_i32_dpp v111, v111, v111 row_ror:4 row_mask:0xf bank_mask:0xf\n"
    "s_nop 1\n"
    "v_max_i32_dpp v111, v111, v111 row_ror:2 row_mask:0xf bank_mask:0xf\n"
    "s_nop 1\n"
    "v_max_i32_dpp v111, v111, v111 row_ror:1 row_mask:0xf bank_mask:0xf\n"
    "v_cmp_lt_u32 vcc, s85, v112\n"
    "v_readlane_b32 s58, v111, 0\n"
    "v_readlane_b32 s59, v111, 32\n"
    "v_readlane_b32 s41, v111, 48\n"
    "s_nop 0\n"
    "s_max_i32 s59, s59, s41\n"
    "s_sub_u32 s59, s59, s58\n"
    "s_add_u32 s58, s58, s66\n"
    "s_and_b32 vcc_lo, vcc_lo, 0xffff\n"
    "s_cselect_b32 s41, 2, s59\n"
    "s_cmp_gt_i32 s41, 1\n"
    "s_cbranch_scc1 E991a_%=\n"
    "E991b_%=:\n"
    "s_mov_b32 s86, s58\n"
    "s_add_u32 s66, s58, s90\n"
    "s_add_u32 s66, s66, %[dp]\n"
    "s_sub_u32 s41, s56, s66\n"
    "s_cmp_gt_i32 s41, 4\n"
    "s_cselect_b32 s41, 88, 0\n"
    "s_add_u32 s66, s66, s41\n"
    "s_sub_u32 s91, 0, s41\n"
    "s_branch Bnext_%=\n"
    "Bawake_%=:\n"
    "v_mov_b32 v93, v92\n"
    "s_nop 1\n"
    "v_mov_b32 v93, v92\n"
    "s_nop 1\n"
    "v_mov_b32 v93, v92\n"
    "s_nop 1\n"
    "v_mov_b32 v93, v92\n"
    "s_nop 1\n"
    "v_mov_b32 v93, v92\n"
    "s_nop 1\n"
    "v_mov_b32 v93, v92\n"
    "s_nop 1\n"
    "v_mov_b32 v93, v92\n"
    "s_nop 1\n"
    "v_mov_b32 v93, v92\n"
    "s_nop 1\n"
    "v_mov_b32 v93, v92\n"
    "s_nop 1\n"
    "v_mov_b32 v93, v92\n"
    "s_nop 1\n"
    "v_mov_b32 v93, v92\n"
    "s_nop 1\n"
    "v_mov_b32 v93, v92\n"
    "s_nop 1\n"
    "v_mov_b32 v93, v92\n"
    "s_nop 1\n"
    "v_mov_b32 v93, v92\n"
    "s_nop 1\n"
    "v_mov_b32 v93, v92\n"
    "s_nop 1\n"
    "v_mov_b32 v93, v92\n"
    "s_nop 1\n"
    "v_mov_b32 v93, v92\n"
    "s_nop 1\n"
    "v_mov_b32 v93, v92\n"
    "s_nop 1\n"
    "v_mov_b32 v93, v92\n"
    "s_nop 1\n"
    "v_mov_b32 v93, v92\n"
    "s_nop 1\n"
    "v_mov_b32 v93, v92\n"
    "s_nop 1\n"
    "v_mov_b32 v93, v92\n"
    "s_nop 1\n"
    "v_mov_b32 v93, v92\n"
    "s_nop 1\n"
    "v_mov_b32 v93, v92\n"
    "s_nop 1\n"
    "v_mov_b32 v93, v92\n"
    "s_nop 1\n"
    "v_mov_b32 v93, v92\n"
    "s_nop 1\n"
    "v_mov_b32 v93, v92\n"
    "s_nop 1\n"
    "v_mov_b32 v93, v92\n"
    "s_nop 1\n"
    "v_mov_b32 v93, v92\n"
    "s_nop 1\n"
    "v_mov_b32 v93, v92\n"
    "s_nop 1\n"
    "v_mov_b32 v93, v92\n"
    "s_nop 1\n"
    "v_mov_b32 v93, v92\n"
    "s_nop 1\n"
    "v_mov_b32 v93, v92\n"
    "s_nop 1\n"
    "v_mov_b32 v93, v92\n"
    "s_nop 1\n"
    "v_mov_b32 v93, v92\n"
    "s_nop 1\n"
    "v_mov_b32 v93, v92\n"
    "s_nop 1\n"
    "v_mov_b32 v93, v92\n"
    "s_nop 1\n"
    "v_mov_b32 v93, v92\n"
    "s_nop 1\n"
    "v_mov_b32 v93, v92\n"
    "s_nop 1\n"
    "v_mov_b32 v93, v92\n"
    "s_nop 1\n"
    "s_branch Bgo_%=\n"
    "Bskipv_%=:\n"
    "s_and_b32 s41, s40, 3\n"
    "s_xor_b32 s41, s41, 2\n"
    "s_mul_i32 s41, s41, 81920\n"
    "s_add_u32 s41, s41, 1048576\n"
    "s_add_u32 s52, s44, s41\n"
    "s_addc_u32 s53, s45, 0\n"
    "s_and_b32 s41, s40, 1\n"
    "s_lshl_b32 s41, s41, 4\n"
    "s_add_u32 s54, s44, s41\n"
    "s_addc_u32 s55, s45, 0\n"
    "s_xor_b32 s41, s41, 16\n"
    "s_add_u32 s78, s44, s41\n"
    "s_addc_u32 s79, s45, 0\n"
    "s_add_u32 s42, s40, 1\n"
    "s_lshl_b32 s85, s42, 1\n"
    "s_sleep 8\n"
    "s_mov_b32 exec_hi, 0\n"
    "global_store_dword %[vmv], v68, s[52:53] offset:0 sc1\n"
    "s_mov_b32 exec_hi, -1\n"
    "s_sleep 12\n"
    "s_branch Bopart_%=\n"
    "Bbad0_%=:\n"
    "s_mov_b64 s[56:57], 0\n"
    "v_cmp_eq_u32 vcc, v68, v16\n"
    "s_or_b64 s[56:57], s[56:57], vcc\n"
    "v_cmp_eq_u32 vcc, v68, v17\n"
    "s_or_b64 s[56:57], s[56:57], vcc\n"
    "v_cmp_eq_u32 vcc, v68, v18\n"
    "s_or_b64 s[56:57], s[56:57], vcc\n"
    "v_cmp_eq_u32 vcc, v68, v19\n"
    "s_or_b64 s[56:57], s[56:57], vcc\n"
    "s_cbranch_scc0 Bnext0_%=\n"
    "global_load_dwordx4 v[16:19], %[vg16], s[46:47] offset:0 sc1\n"
    "s_branch Bnext0_%=\n"
    "Bbad1_%=:\n"
    "s_mov_b64 s[56:57], 0\n"
    "v_cmp_eq_u32 vcc, v68, v20\n"
    "s_or_b64 s[56:57], s[56:57], vcc\n"
    "v_cmp_eq_u32 vcc, v68, v21\n"
    "s_or_b64 s[56:57], s[56:57], vcc\n"
    "v_cmp_eq_u32 vcc, v68, v22\n"
    "s_or_b64 s[56:57], s[56:57], vcc\n"
    "v_cmp_eq_u32 vcc, v68, v23\n"
    "s_or_b64 s[56:57], s[56:57], vcc\n"
    "s_cbranch_scc0 Bnext1_%=\n"
    "global_load_dwordx4 v[20:23], %[vg16], s[46:47] offset:1024 sc1\n"
    "s_branch Bnext1_%=\n"
    "Bbad2_%=:\n"
    "s_mov_b64 s[56:57], 0\n"
    "v_cmp_eq_u32 vcc, v68, v24\n"
    "s_or_b64 s[56:57], s[56:57], vcc\n"
    "v_cmp_eq_u32 vcc, v68, v25\n"
    "s_or_b64 s[56:57], s[56:57], vcc\n"
    "v_cmp_eq_u32 vcc, v68, v26\n"
    "s_or_b64 s[56:57], s[56:57], vcc\n"
    "v_cmp_eq_u32 vcc, v68, v27\n"
    "s_or_b64 s[56:57], s[56:57], vcc\n"
    "s_cbranch_scc0 Bnext2_%=\n"
    "global_load_dwordx4 v[24:27], %[vg16], s[46:47] offset:2048 sc1\n"
    "s_branch Bnext2_%=\n"
    "Bnan_%=:\n"
    "s_mov_b32 s84, 0xff\n"
    "s_mov_b64 s[92:93], 0\n"
    "s_mov_b32 s76, 1\n"
    "Bslow_%=:\n"
    "s_waitcnt vmcnt(0)\n"
    "s_bitcmp0_b32 s84, 0\n"
    "s_cbranch_scc1 Bsk0_%=\n"
    "v_max3_u32 v64, v16, v17, v18\n"
    "v_max_u32 v64, v19, v64\n"
    "v_cmp_le_u32 vcc, v68, v64\n"
    "s_cbranch_vccz Bsv0_%=\n"
    "v_cmp_eq_u32 vcc, v68, v64\n"
    "s_cbranch_vccnz Bsv0n_%=\n"
    "s_mov_b64 s[56:57], 0\n"
    "v_cmp_eq_u32 vcc, v68, v16\n"
    "s_or_b64 s[56:57], s[56:57], vcc\n"
    "v_cmp_eq_u32 vcc, v68, v17\n"
    "s_or_b64 s[56:57], s[56:57], vcc\n"
    "v_cmp_eq_u32 vcc, v68, v18\n"
    "s_or_b64 s[56:57], s[56:57], vcc\n"
    "v_cmp_eq_u32 vcc, v68, v19\n"
    "s_or_b64 s[56:57], s[56:57], vcc\n"
    "s_cbranch_scc0 Bsv0_%=\n"
    "Bsv0n_%=:\n"
    "s_branch Bsk0_%=\n"
    "Bsv0_%=:\n"
    "s_bitset0_b32 s84, 0\n"
    "Bsk0_%=:\n"
    "s_bitcmp0_b32 s84, 1\n"
    "s_cbranch_scc1 Bsk1_%=\n"
    "v_max3_u32 v64, v20, v21, v22\n"
    "v_max_u32 v64, v23, v64\n"
    "v_cmp_le_u32 vcc, v68, v64\n"
    "s_cbranch_vccz Bsv1_%=\n"
    "v_cmp_eq_u32 vcc, v68, v64\n"
    "s_cbranch_vccnz Bsv1n_%=\n"
    "s_mov_b64 s[56:57], 0\n"
    "v_cmp_eq_u32 vcc, v68, v20\n"
    "s_or_b64 s[56:57], s[56:57], vcc\n"
    "v_cmp_eq_u32 vcc, v68, v21\n"
    "s_or_b64 s[56:57], s[56:57], vcc\n"
    "v_cmp_eq_u32 vcc, v68, v22\n"
    "s_or_b64 s[56:57], s[56:57], vcc\n"
    "v_cmp_eq_u32 vcc, v68, v23\n"
    "s_or_b64 s[56:57], s[56:57], vcc\n"
    "s_cbranch_scc0 Bsv1_%=\n"
    "Bsv1n_%=:\n"
    "s_branch Bsk1_%=\n"
    "Bsv1_%=:\n"
    "s_bitset0_b32 s84, 1\n"
    "Bsk1_%=:\n"
    "s_bitcmp0_b32 s84, 2\n"
    "s_cbranch_scc1 Bsk2_%=\n"
    "v_max3_u32 v64, v24, v25, v26\n"
    "v_max_u32 v64, v27, v64\n"
    "v_cmp_le_u32 vcc, v68, v64\n"
    "s_cbranch_vccz Bsv2_%=\n"
    "v_cmp_eq_u32 vcc, v68, v64\n"
    "s_cbranch_vccnz Bsv2n_%=\n"
    "s_mov_b64 s[56:57], 0\n"
    "v_cmp_eq_u32 vcc, v68, v24\n"
    "s_or_b64 s[56:57], s[56:57], vcc\n"
    "v_cmp_eq_u32 vcc, v68, v25\n"
    "s_or_b64 s[56:57], s[56:57], vcc\n"
    "v_cmp_eq_u32 vcc, v68, v26\n"
    "s_or_b64 s[56:57], s[56:57], vcc\n"
    "v_cmp_eq_u32 vcc, v68, v27\n"
    "s_or_b64 s[56:57], s[56:57], vcc\n"
    "s_cbranch_scc0 Bsv2_%=\n"
    "Bsv2n_%=:\n"
    "s_branch Bsk2_%=\n"
    "Bsv2_%=:\n"
    "s_bitset0_b32 s84, 2\n"
    "Bsk2_%=:\n"
    "s_bitcmp0_b32 s84, 3\n"
    "s_cbranch_scc1 Bsk3_%=\n"
    "v_max3_u32 v64, v28, v29, v30\n"
    "v_max_u32 v64, v31, v64\n"
    "v_cmp_le_u32 vcc, v68, v64\n"
    "s_cbranch_vccz Bsv3_%=\n"
    "v_cmp_eq_u32 vcc, v68, v64\n"
    "s_cbranch_vccnz Bsv3n_%=\n"
    "s_mov_b64 s[56:57], 0\n"
    "v_cmp_eq_u32 vcc, v68, v28\n"
    "s_or_b64 s[56:57], s[56:57], vcc\n"
    "v_cmp_eq_u32 vcc, v68, v29\n"
    "s_or_b64 s[56:57], s[56:57], vcc\n"
    "v_cmp_eq_u32 vcc, v68, v30\n"
    "s_or_b64 s[56:57], s[56:57], vcc\n"
    "v_cmp_eq_u32 vcc, v68, v31\n"
    "s_or_b64 s[56:57], s[56:57], vcc\n"
    "s_cbranch_scc0 Bsv3_%=\n"
    "Bsv3n_%=:\n"
    "s_branch Bsk3_%=\n"
    "Bsv3_%=:\n"
    "s_bitset0_b32 s84, 3\n"
    "Bsk3_%=:\n"
    "s_bitcmp0_b32 s84, 4\n"
    "s_cbranch_scc1 Bsk4_%=\n"
    "v_max3_u32 v64, v32, v33, v34\n"
    "v_max_u32 v64, v35, v64\n"
    "v_cmp_le_u32 vcc, v68, v64\n"
    "s_cbranch_vccz Bsv4_%=\n"
    "v_cmp_eq_u32 vcc, v68, v64\n"
    "s_cbranch_vccnz Bsv4n_%=\n"
    "s_mov_b64 s[56:57], 0\n"
    "v_cmp_eq_u32 vcc, v68, v32\n"
    "s_or_b64 s[56:57], s[56:57], vcc\n"
    "v_cmp_eq_u32 vcc, v68, v33\n"
    "s_or_b64 s[56:57], s[56:57], vcc\n"
    "v_cmp_eq_u32 vcc, v68, v34\n"
    "s_or_b64 s[56:57], s[56:57], vcc\n"
    "v_cmp_eq_u32 vcc, v68, v35\n"
    "s_or_b64 s[56:57], s[56:57], vcc\n"
    "s_cbranch_scc0 Bsv4_%=\n"
    "Bsv4n_%=:\n"
    "s_branch Bsk4_%=\n"
    "Bsv4_%=:\n"
    "s_bitset0_b32 s84, 4\n"
    "Bsk4_%=:\n"
    "s_bitcmp0_b32 s84, 5\n"
    "s_cbranch_scc1 Bsk5_%=\n"
    "v_max3_u32 v64, v36, v37, v38\n"
    "v_max_u32 v64, v39, v64\n"
    "v_cmp_le_u32 vcc, v68, v64\n"
    "s_cbranch_vccz Bsv5_%=\n"
    "v_cmp_eq_u32 vcc, v68, v64\n"
    "s_cbranch_vccnz Bsv5n_%=\n"
    "s_mov_b64 s[56:57], 0\n"
    "v_cmp_eq_u32 vcc, v68, v36\n"
    "s_or_b64 s[56:57], s[56:57], vcc\n"
    "v_cmp_eq_u32 vcc, v68, v37\n"
    "s_or_b64 s[56:57], s[56:57], vcc\n"
    "v_cmp_eq_u32 vcc, v68, v38\n"
    "s_or_b64 s[56:57], s[56:57], vcc\n"
    "v_cmp_eq_u32 vcc, v68, v39\n"
    "s_or_b64 s[56:57], s[56:57], vcc\n"
    "s_cbranch_scc0 Bsv5_%=\n"
    "Bsv5n_%=:\n"
    "s_branch Bsk5_%=\n"
    "Bsv5_%=:\n"
    "s_bitset0_b32 s84, 5\n"
    "Bsk5_%=:\n"
    "s_bitcmp0_b32 s84, 6\n"
    "s_cbranch_scc1 Bsk6_%=\n"
    "v_max3_u32 v64, v40, v41, v42\n"
    "v_max_u32 v64, v43, v64\n"
    "v_cmp_le_u32 vcc, v68, v64\n"
    "s_cbranch_vccz Bsv6_%=\n"
    "v_cmp_eq_u32 vcc, v68, v64\n"
    "s_cbranch_vccnz Bsv6n_%=\n"
    "s_mov_b64 s[56:57], 0\n"
    "v_cmp_eq_u32 vcc, v68, v40\n"
    "s_or_b64 s[56:57], s[56:57], vcc\n"
    "v_cmp_eq_u32 vcc, v68, v41\n"
    "s_or_b64 s[56:57], s[56:57], vcc\n"
    "v_cmp_eq_u32 vcc, v68, v42\n"
    "s_or_b64 s[56:57], s[56:57], vcc\n"
    "v_cmp_eq_u32 vcc, v68, v43\n"
    "s_or_b64 s[56:57], s[56:57], vcc\n"
    "s_cbranch_scc0 Bsv6_%=\n"
    "Bsv6n_%=:\n"
    "s_branch Bsk6_%=\n"
    "Bsv6_%=:\n"
    "s_bitset0_b32 s84, 6\n"
    "Bsk6_%=:\n"
    "s_bitcmp0_b32 s84, 7\n"
    "s_cbranch_scc1 Bsk7_%=\n"
    "v_max3_u32 v64, v44, v45, v46\n"
    "v_max_u32 v64, v47, v64\n"
    "v_cmp_le_u32 vcc, v68, v64\n"
    "s_cbranch_vccz Bsv7_%=\n"
    "v_cmp_eq_u32 vcc, v68, v64\n"
    "s_cbranch_vccnz Bsv7n_%=\n"
    "s_mov_b64 s[56:57], 0\n"
    "v_cmp_eq_u32 vcc, v68, v44\n"
    "s_or_b64 s[56:57], s[56:57], vcc\n"
    "v_cmp_eq_u32 vcc, v68, v45\n"
    "s_or_b64 s[56:57], s[56:57], vcc\n"
    "v_cmp_eq_u32 vcc, v68, v46\n"
    "s_or_b64 s[56:57], s[56:57], vcc\n"
    "v_cmp_eq_u32 vcc, v68, v47\n"
    "s_or_b64 s[56:57], s[56:57], vcc\n"
    "s_cbranch_scc0 Bsv7_%=\n"
    "Bsv7n_%=:\n"
    "s_branch Bsk7_%=\n"
    "Bsv7_%=:\n"
    "s_bitset0_b32 s84, 7\n"
    "Bsk7_%=:\n"
    "s_cmp_eq_u32 s84, 0\n"
    "s_cbranch_scc1 Bredo_%=\n"
    "s_cmp_lg_u32 s76, 0\n"
    "s_mov_b32 s76, 0\n"
    "s_cbranch_scc1 Bsall_%=\n"
    "s_bitcmp0_b32 s84, 0\n"
    "s_cbranch_scc1 Bsp0_%=\n"
    "Bsw0_%=:\n"
    "s_add_u32 s43, s43, 1\n"
    "s_cmp_gt_u32 s43, 2097152\n"
    "s_cbranch_scc1 Bdead_%=\n"
    "global_load_dwordx4 v[16:19], %[vg16], s[46:47] offset:0 sc1\n"
    "s_waitcnt vmcnt(0)\n"
    "v_max3_u32 v64, v16, v17, v18\n"
    "v_max_u32 v64, v19, v64\n"
    "v_cmp_le_u32 vcc, v68, v64\n"
    "s_cbranch_vccz Bsx0_%=\n"
    "v_cmp_eq_u32 vcc, v68, v64\n"
    "s_cbranch_vccnz Bsx0n_%=\n"
    "s_mov_b64 s[56:57], 0\n"
    "v_cmp_eq_u32 vcc, v68, v16\n"
    "s_or_b64 s[56:57], s[56:57], vcc\n"
    "v_cmp_eq_u32 vcc, v68, v17\n"
    "s_or_b64 s[56:57], s[56:57], vcc\n"
    "v_cmp_eq_u32 vcc, v68, v18\n"
    "s_or_b64 s[56:57], s[56:57], vcc\n"
    "v_cmp_eq_u32 vcc, v68, v19\n"
    "s_or_b64 s[56:57], s[56:57], vcc\n"
    "s_cbranch_scc0 Bsx0_%=\n"
    "Bsx0n_%=:\n"
    "s_sleep 1\n"
    "s_branch Bsw0_%=\n"
    "Bsx0_%=:\n"
    "s_bitset0_b32 s84, 0\n"
    "s_branch Bsall_%=\n"
    "Bsp0_%=:\n"
    "s_bitcmp0_b32 s84, 1\n"
    "s_cbranch_scc1 Bsp1_%=\n"
    "Bsw1_%=:\n"
    "s_add_u32 s43, s43, 1\n"
    "s_cmp_gt_u32 s43, 2097152\n"
    "s_cbranch_scc1 Bdead_%=\n"
    "global_load_dwordx4 v[20:23], %[vg16], s[46:47] offset:1024 sc1\n"
    "s_waitcnt vmcnt(0)\n"
    "v_max3_u32 v64, v20, v21, v22\n"
    "v_max_u32 v64, v23, v64\n"
    "v_cmp_le_u32 vcc, v68, v64\n"
    "s_cbranch_vccz Bsx1_%=\n"
    "v_cmp_eq_u32 vcc, v68, v64\n"
    "s_cbranch_vccnz Bsx1n_%=\n"
    "s_mov_b64 s[56:57], 0\n"
    "v_cmp_eq_u32 vcc, v68, v20\n"
    "s_or_b64 s[56:57], s[56:57], vcc\n"
    "v_cmp_eq_u32 vcc, v68, v21\n"
    "s_or_b64 s[56:57], s[56:57], vcc\n"
    "v_cmp_eq_u32 vcc, v68, v22\n"
    "s_or_b64 s[56:57], s[56:57], vcc\n"
    "v_cmp_eq_u32 vcc, v68, v23\n"
    "s_or_b64 s[56:57], s[56:57], vcc\n"
    "s_cbranch_scc0 Bsx1_%=\n"
    "Bsx1n_%=:\n"
    "s_sleep 1\n"
    "s_branch Bsw1_%=\n"
    "Bsx1_%=:\n"
    "s_bitset0_b32 s84, 1\n"
    "s_branch Bsall_%=\n"
    "Bsp1_%=:\n"
    "s_bitcmp0_b32 s84, 2\n"
    "s_cbranch_scc1 Bsp2_%=\n"
    "Bsw2_%=:\n"
    "s_add_u32 s43, s43, 1\n"
    "s_cmp_gt_u32 s43, 2097152\n"
    "s_cbranch_scc1 Bdead_%=\n"
    "global_load_dwordx4 v[24:27], %[vg16], s[46:47] offset:2048 sc1\n"
    "s_waitcnt vmcnt(0)\n"
    "v_max3_u32 v64, v24, v25, v26\n"
    "v_max_u32 v64, v27, v64\n"
    "v_cmp_le_u32 vcc, v68, v64\n"
    "s_cbranch_vccz Bsx2_%=\n"
    "v_cmp_eq_u32 vcc, v68, v64\n"
    "s_cbranch_vccnz Bsx2n_%=\n"
    "s_mov_b64 s[56:57], 0\n"
    "v_cmp_eq_u32 vcc, v68, v24\n"
    "s_or_b64 s[56:57], s[56:57], vcc\n"
    "v_cmp_eq_u32 vcc, v68, v25\n"
    "s_or_b64 s[56:57], s[56:57], vcc\n"
    "v_cmp_eq_u32 vcc, v68, v26\n"
    "s_or_b64 s[56:57], s[56:57], vcc\n"
    "v_cmp_eq_u32 vcc, v68, v27\n"
    "s_or_b64 s[56:57], s[56:57], vcc\n"
    "s_cbranch_scc0 Bsx2_%=\n"
    "Bsx2n_%=:\n"
    "s_sleep 1\n"
    "s_branch Bsw2_%=\n"
    "Bsx2_%=:\n"
    "s_bitset0_b32 s84, 2\n"
    "s_branch Bsall_%=\n"
    "Bsp2_%=:\n"
    "s_bitcmp0_b32 s84, 3\n"
    "s_cbranch_scc1 Bsp3_%=\n"
    "Bsw3_%=:\n"
    "s_add_u32 s43, s43, 1\n"
    "s_cmp_gt_u32 s43, 2097152\n"
    "s_cbranch_scc1 Bdead_%=\n"
    "global_load_dwordx4 v[28:31], %[vg16], s[46:47] offset:3072 sc1\n"
    "s_waitcnt vmcnt(0)\n"
    "v_max3_u32 v64, v28, v29, v30\n"
    "v_max_u32 v64, v31, v64\n"
    "v_cmp_le_u32 vcc, v68, v64\n"
    "s_cbranch_vccz Bsx3_%=\n"
    "v_cmp_eq_u32 vcc, v68, v64\n"
    "s_cbranch_vccnz Bsx3n_%=\n"
    "s_mov_b64 s[56:57], 0\n"
    "v_cmp_eq_u32 vcc, v68, v28\n"
    "s_or_b64 s[56:57], s[56:57], vcc\n"
    "v_cmp_eq_u32 vcc, v68, v29\n"
    "s_or_b64 s[56:57], s[56:57], vcc\n"
    "v_cmp_eq_u32 vcc, v68, v30\n"
    "s_or_b64 s[56:57], s[56:57], vcc\n"
    "v_cmp_eq_u32 vcc, v68, v31\n"
    "s_or_b64 s[56:57], s[56:57], vcc\n"
    "s_cbranch_scc0 Bsx3_%=\n"
    "Bsx3n_%=:\n"
    "s_sleep 1\n"
    "s_branch Bsw3_%=\n"
    "Bsx3_%=:\n"
    "s_bitset0_b32 s84, 3\n"
    "s_branch Bsall_%=\n"
    "Bsp3_%=:\n"
    "s_bitcmp0_b32 s84, 4\n"
    "s_cbranch_scc1 Bsp4_%=\n"
    "Bsw4_%=:\n"
    "s_add_u32 s43, s43, 1\n"
    "s_cmp_gt_u32 s43, 2097152\n"
    "s_cbranch_scc1 Bdead_%=\n"
    "global_load_dwordx4 v[32:35], %[vg16], s[48:49] offset:0 sc1\n"
    "s_waitcnt vmcnt(0)\n"
    "v_max3_u32 v64, v32, v33, v34\n"
    "v_max_u32 v64, v35, v64\n"
    "v_cmp_le_u32 vcc, v68, v64\n"
    "s_cbranch_vccz Bsx4_%=\n"
    "v_cmp_eq_u32 vcc, v68, v64\n"
    "s_cbranch_vccnz Bsx4n_%=\n"
    "s_mov_b64 s[56:57], 0\n"
    "v_cmp_eq_u32 vcc, v68, v32\n"
    "s_or_b64 s[56:57], s[56:57], vcc\n"
    "v_cmp_eq_u32 vcc, v68, v33\n"
    "s_or_b64 s[56:57], s[56:57], vcc\n"
    "v_cmp_eq_u32 vcc, v68, v34\n"
    "s_or_b64 s[56:57], s[56:57], vcc\n"
    "v_cmp_eq_u32 vcc, v68, v35\n"
    "s_or_b64 s[56:57], s[56:57], vcc\n"
    "s_cbranch_scc0 Bsx4_%=\n"
    "Bsx4n_%=:\n"
    "s_sleep 1\n"
    "s_branch Bsw4_%=\n"
    "Bsx4_%=:\n"
    "s_bitset0_b32 s84, 4\n"
    "s_branch Bsall_%=\n"
    "Bsp4_%=:\n"
    "s_bitcmp0_b32 s84, 5\n"
    "s_cbranch_scc1 Bsp5_%=\n"
    "Bsw5_%=:\n"
    "s_add_u32 s43, s43, 1\n"
    "s_cmp_gt_u32 s43, 2097152\n"
    "s_cbranch_scc1 Bdead_%=\n"
    "global_load_dwordx4 v[36:39], %[vg16], s[48:49] offset:1024 sc1\n"
    "s_waitcnt vmcnt(0)\n"
    "v_max3_u32 v64, v36, v37, v38\n"
    "v_max_u32 v64, v39, v64\n"
    "v_cmp_le_u32 vcc, v68, v64\n"
    "s_cbranch_vccz Bsx5_%=\n"
    "v_cmp_eq_u32 vcc, v68, v64\n"
    "s_cbranch_vccnz Bsx5n_%=\n"
    "s_mov_b64 s[56:57], 0\n"
    "v_cmp_eq_u32 vcc, v68, v36\n"
    "s_or_b64 s[56:57], s[56:57], vcc\n"
    "v_cmp_eq_u32 vcc, v68, v37\n"
    "s_or_b64 s[56:57], s[56:57], vcc\n"
    "v_cmp_eq_u32 vcc, v68, v38\n"
    "s_or_b64 s[56:57], s[56:57], vcc\n"
    "v_cmp_eq_u32 vcc, v68, v39\n"
    "s_or_b64 s[56:57], s[56:57], vcc\n"
    "s_cbranch_scc0 Bsx5_%=\n"
    "Bsx5n_%=:\n"
    "s_sleep 1\n"
    "s_branch Bsw5_%=\n"
    "Bsx5_%=:\n"
    "s_bitset0_b32 s84, 5\n"
    "s_branch Bsall_%=\n"
    "Bsp5_%=:\n"
    "s_bitcmp0_b32 s84, 6\n"
    "s_cbranch_scc1 Bsp6_%=\n"
    "Bsw6_%=:\n"
    "s_add_u32 s43, s43, 1\n"
    "s_cmp_gt_u32 s43, 2097152\n"
    "s_cbranch_scc1 Bdead_%=\n"
    "global_load_dwordx4 v[40:43], %[vg16], s[48:49] offset:2048 sc1\n"
    "s_waitcnt vmcnt(0)\n"
    "v_max3_u32 v64, v40, v41, v42\n"
    "v_max_u32 v64, v43, v64\n"
    "v_cmp_le_u32 vcc, v68, v64\n"
    "s_cbranch_vccz Bsx6_%=\n"
    "v_cmp_eq_u32 vcc, v68, v64\n"
    "s_cbranch_vccnz Bsx6n_%=\n"
    "s_mov_b64 s[56:57], 0\n"
    "v_cmp_eq_u32 vcc, v68, v40\n"
    "s_or_b64 s[56:57], s[56:57], vcc\n"
    "v_cmp_eq_u32 vcc, v68, v41\n"
    "s_or_b64 s[56:57], s[56:57], vcc\n"
    "v_cmp_eq_u32 vcc, v68, v42\n"
    "s_or_b64 s[56:57], s[56:57], vcc\n"
    "v_cmp_eq_u32 vcc, v68, v43\n"
    "s_or_b64 s[56:57], s[56:57], vcc\n"
    "s_cbranch_scc0 Bsx6_%=\n"
    "Bsx6n_%=:\n"
    "s_sleep 1\n"
    "s_branch Bsw6_%=\n"
    "Bsx6_%=:\n"
    "s_bitset0_b32 s84, 6\n"
    "s_branch Bsall_%=\n"
    "Bsp6_%=:\n"
    "s_bitcmp0_b32 s84, 7\n"
    "s_cbranch_scc1 Bsp7_%=\n"
    "Bsw7_%=:\n"
    "s_add_u32 s43, s43, 1\n"
    "s_cmp_gt_u32 s43, 2097152\n"
    "s_cbranch_scc1 Bdead_%=\n"
    "global_load_dwordx4 v[44:47], %[vg16], s[48:49] offset:3072 sc1\n"
    "s_waitcnt vmcnt(0)\n"
    "v_max3_u32 v64, v44, v45, v46\n"
    "v_max_u32 v64, v47, v64\n"
    "v_cmp_le_u32 vcc, v68, v64\n"
    "s_cbranch_vccz Bsx7_%=\n"
    "v_cmp_eq_u32 vcc, v68, v64\n"
    "s_cbranch_vccnz Bsx7n_%=\n"
    "s_mov_b64 s[56:57], 0\n"
    "v_cmp_eq_u32 vcc, v68, v44\n"
    "s_or_b64 s[56:57], s[56:57], vcc\n"
    "v_cmp_eq_u32 vcc, v68, v45\n"
    "s_or_b64 s[56:57], s[56:57], vcc\n"
    "v_cmp_eq_u32 vcc, v68, v46\n"
    "s_or_b64 s[56:57], s[56:57], vcc\n"
    "v_cmp_eq_u32 vcc, v68, v47\n"
    "s_or_b64 s[56:57], s[56:57], vcc\n"
    "s_cbranch_scc0 Bsx7_%=\n"
    "Bsx7n_%=:\n"
    "s_sleep 1\n"
    "s_branch Bsw7_%=\n"
    "Bsx7_%=:\n"
    "s_bitset0_b32 s84, 7\n"
    "s_branch Bsall_%=\n"
    "Bsp7_%=:\n"
    "Bsall_%=:\n"
    "s_bitcmp0_b32 s84, 0\n"
    "s_cbranch_scc1 Bsq0_%=\n"
    "global_load_dwordx4 v[16:19], %[vg16], s[46:47] offset:0 sc1\n"
    "Bsq0_%=:\n"
    "s_bitcmp0_b32 s84, 1\n"
    "s_cbranch_scc1 Bsq1_%=\n"
    "global_load_dwordx4 v[20:23], %[vg16], s[46:47] offset:1024 sc1\n"
    "Bsq1_%=:\n"
    "s_bitcmp0_b32 s84, 2\n"
    "s_cbranch_scc1 Bsq2_%=\n"
    "global_load_dwordx4 v[24:27], %[vg16], s[46:47] offset:2048 sc1\n"
    "Bsq2_%=:\n"
    "s_bitcmp0_b32 s84, 3\n"
    "s_cbranch_scc1 Bsq3_%=\n"
    "global_load_dwordx4 v[28:31], %[vg16], s[46:47] offset:3072 sc1\n"
    "Bsq3_%=:\n"
    "s_bitcmp0_b32 s84, 4\n"
    "s_cbranch_scc1 Bsq4_%=\n"
    "global_load_dwordx4 v[32:35], %[vg16], s[48:49] offset:0 sc1\n"
    "Bsq4_%=:\n"
    "s_bitcmp0_b32 s84, 5\n"
    "s_cbranch_scc1 Bsq5_%=\n"
    "global_load_dwordx4 v[36:39], %[vg16], s[48:49] offset:1024 sc1\n"
    "Bsq5_%=:\n"
    "s_bitcmp0_b32 s84, 6\n"
    "s_cbranch_scc1 Bsq6_%=\n"
    "global_load_dwordx4 v[40:43], %[vg16], s[48:49] offset:2048 sc1\n"
    "Bsq6_%=:\n"
    "s_bitcmp0_b32 s84, 7\n"
    "s_cbranch_scc1 Bsq7_%=\n"
    "global_load_dwordx4 v[44:47], %[vg16], s[48:49] offset:3072 sc1\n"
    "Bsq7_%=:\n"
    "s_branch Bslow_%=\n"
    "Bredo_%=:\n"
    "v_mov_b64 v[56:57], 0\n"
    "v_mov_b64 v[58:59], 0\n"
    "v_mov_b64 v[60:61], 0\n"
    "v_mov_b64 v[62:63], 0\n"
    "v_smfmac_f32_16x16x64_bf16 v[56:59], v[16:19], a[0:7], %[vidx]\n"
    "s_nop 0\n"
    "v_smfmac_f32_16x16x64_bf16 v[60:63], v[16:19], a[8:15], %[vidx]\n"
    "v_smfmac_f32_16x16x64_bf16 v[56:59], v[20:23], a[16:23], %[vidx]\n"
    "v_smfmac_f32_16x16x64_bf16 v[60:63], v[20:23], a[24:31], %[vidx]\n"
    "v_smfmac_f32_16x16x64_bf16 v[56:59], v[24:27], a[32:39], %[vidx]\n"
    "v_smfmac_f32_16x16x64_bf16 v[60:63], v[24:27], a[40:47], %[vidx]\n"
    "v_smfmac_f32_16x16x64_bf16 v[56:59], v[28:31], a[48:55], %[vidx]\n"
    "v_smfmac_f32_16x16x64_bf16 v[60:63], v[28:31], a[56:63], %[vidx]\n"
    "v_smfmac_f32_16x16x64_bf16 v[56:59], v[32:35], a[64:71], %[vidx]\n"
    "v_smfmac_f32_16x16x64_bf16 v[60:63], v[32:35], a[72:79], %[vidx]\n"
    "v_smfmac_f32_16x16x64_bf16 v[56:59], v[36:39], a[80:87], %[vidx]\n"
    "v_smfmac_f32_16x16x64_bf16 v[60:63], v[36:39], a[88:95], %[vidx]\n"
    "v_smfmac_f32_16x16x64_bf16 v[56:59], v[40:43], a[96:103], %[vidx]\n"
    "v_smfmac_f32_16x16x64_bf16 v[60:63], v[40:43], a[104:111], %[vidx]\n"
    "v_smfmac_f32_16x16x64_bf16 v[56:59], v[44:47], a[112:119], %[vidx]\n"
    "v_smfmac_f32_16x16x64_bf16 v[60:63], v[44:47], a[120:127], %[vidx]\n"
    "s_nop 7\n"
    "s_branch Bok_%=\n"
    "Bvslow_%=:\n"
    "v_max3_u32 v64, v16, v17, v18\n"
    "v_max3_u32 v64, v18, v19, v64\n"
    "v_max3_u32 v64, v20, v21, v64\n"
    "v_max3_u32 v64, v22, v23, v64\n"
    "v_max3_u32 v64, v24, v25, v64\n"
    "v_max3_u32 v64, v26, v27, v64\n"
    "v_max3_u32 v64, v28, v29, v64\n"
    "v_max3_u32 v64, v30, v31, v64\n"
    "v_max3_u32 v64, v32, v33, v64\n"
    "v_max3_u32 v64, v34, v35, v64\n"
    "v_max3_u32 v64, v36, v37, v64\n"
    "v_max3_u32 v64, v38, v39, v64\n"
    "v_max3_u32 v64, v40, v41, v64\n"
    "v_max3_u32 v64, v42, v43, v64\n"
    "v_max3_u32 v64, v44, v45, v64\n"
    "v_max3_u32 v64, v46, v47, v64\n"
    "v_max3_u32 v64, v48, v49, v64\n"
    "v_max3_u32 v64, v50, v51, v64\n"
    "v_max3_u32 v64, v52, v53, v64\n"
    "v_max3_u32 v64, v54, v55, v64\n"
    "v_cmp_eq_u32 vcc, 0xff81ff81, v64\n"
    "s_cbranch_vccnz Bvslow1_%=\n"
    "s_mov_b64 s[56:57], 0\n"
    "v_cmp_eq_u32 vcc, 0xff81ff81, v16\n"
    "s_or_b64 s[56:57], s[56:57], vcc\n"
    "v_cmp_eq_u32 vcc, 0xff81ff81, v17\n"
    "s_or_b64 s[56:57], s[56:57], vcc\n"
    "v_cmp_eq_u32 vcc, 0xff81ff81, v18\n"
    "s_or_b64 s[56:57], s[56:57], vcc\n"
    "v_cmp_eq_u32 vcc, 0xff81ff81, v19\n"
    "s_or_b64 s[56:57], s[56:57], vcc\n"
    "v_cmp_eq_u32 vcc, 0xff81ff81, v20\n"
    "s_or_b64 s[56:57], s[56:57], vcc\n"
    "v_cmp_eq_u32 vcc, 0xff81ff81, v21\n"
    "s_or_b64 s[56:57], s[56:57], vcc\n"
    "v_cmp_eq_u32 vcc, 0xff81ff81, v22\n"
    "s_or_b64 s[56:57], s[56:57], vcc\n"
    "v_cmp_eq_u32 vcc, 0xff81ff81, v23\n"
    "s_or_b64 s[56:57], s[56:57], vcc\n"
    "v_cmp_eq_u32 vcc, 0xff81ff81, v24\n"
    "s_or_b64 s[56:57], s[56:57], vcc\n"
    "v_cmp_eq_u32 vcc, 0xff81ff81, v25\n"
    "s_or_b64 s[56:57], s[56:57], vcc\n"
    "v_cmp_eq_u32 vcc, 0xff81ff81, v26\n"
    "s_or_b64 s[56:57], s[56:57], vcc\n"
    "v_cmp_eq_u32 vcc, 0xff81ff81, v27\n"
    "s_or_b64 s[56:57], s[56:57], vcc\n"
    "v_cmp_eq_u32 vcc, 0xff81ff81, v28\n"
    "s_or_b64 s[56:57], s[56:57], vcc\n"
    "v_cmp_eq_u32 vcc, 0xff81ff81, v29\n"
    "s_or_b64 s[56:57], s[56:57], vcc\n"
    "v_cmp_eq_u32 vcc, 0xff81ff81, v30\n"
    "s_or_b64 s[56:57], s[56:57], vcc\n"
    "v_cmp_eq_u32 vcc, 0xff81ff81, v31\n"
    "s_or_b64 s[56:57], s[56:57], vcc\n"
    "v_cmp_eq_u32 vcc, 0xff81ff81, v32\n"
    "s_or_b64 s[56:57], s[56:57], vcc\n"
    "v_cmp_eq_u32 vcc, 0xff81ff81, v33\n"
    "s_or_b64 s[56:57], s[56:57], vcc\n"
    "v_cmp_eq_u32 vcc, 0xff81ff81, v34\n"
    "s_or_b64 s[56:57], s[56:57], vcc\n"
    "v_cmp_eq_u32 vcc, 0xff81ff81, v35\n"
    "s_or_b64 s[56:57], s[56:57], vcc\n"
    "v_cmp_eq_u32 vcc, 0xff81ff81, v36\n"
    "s_or_b64 s[56:57], s[56:57], vcc\n"
    "v_cmp_eq_u32 vcc, 0xff81ff81, v37\n"
    "s_or_b64 s[56:57], s[56:57], vcc\n"
    "v_cmp_eq_u32 vcc, 0xff81ff81, v38\n"
    "s_or_b64 s[56:57], s[56:57], vcc\n"
    "v_cmp_eq_u32 vcc, 0xff81ff81, v39\n"
    "s_or_b64 s[56:57], s[56:57], vcc\n"
    "v_cmp_eq_u32 vcc, 0xff81ff81, v40\n"
    "s_or_b64 s[56:57], s[56:57], vcc\n"
    "v_cmp_eq_u32 vcc, 0xff81ff81, v41\n"
    "s_or_b64 s[56:57], s[56:57], vcc\n"
    "v_cmp_eq_u32 vcc, 0xff81ff81, v42\n"
    "s_or_b64 s[56:57], s[56:57], vcc\n"
    "v_cmp_eq_u32 vcc, 0xff81ff81, v43\n"
    "s_or_b64 s[56:57], s[56:57], vcc\n"
    "v_cmp_eq_u32 vcc, 0xff81ff81, v44\n"
    "s_or_b64 s[56:57], s[56:57], vcc\n"
    "v_cmp_eq_u32 vcc, 0xff81ff81, v45\n"
    "s_or_b64 s[56:57], s[56:57], vcc\n"
    "v_cmp_eq_u32 vcc, 0xff81ff81, v46\n"
    "s_or_b64 s[56:57], s[56:57], vcc\n"
    "v_cmp_eq_u32 vcc, 0xff81ff81, v47\n"
    "s_or_b64 s[56:57], s[56:57], vcc\n"
    "v_cmp_eq_u32 vcc, 0xff81ff81, v48\n"
    "s_or_b64 s[56:57], s[56:57], vcc\n"
    "v_cmp_eq_u32 vcc, 0xff81ff81, v49\n"
    "s_or_b64 s[56:57], s[56:57], vcc\n"
    "v_cmp_eq_u32 vcc, 0xff81ff81, v50\n"
    "s_or_b64 s[56:57], s[56:57], vcc\n"
    "v_cmp_eq_u32 vcc, 0xff81ff81, v51\n"
    "s_or_b64 s[56:57], s[56:57], vcc\n"
    "v_cmp_eq_u32 vcc, 0xff81ff81, v52\n"
    "s_or_b64 s[56:57], s[56:57], vcc\n"
    "v_cmp_eq_u32 vcc, 0xff81ff81, v53\n"
    "s_or_b64 s[56:57], s[56:57], vcc\n"
    "v_cmp_eq_u32 vcc, 0xff81ff81, v54\n"
    "s_or_b64 s[56:57], s[56:57], vcc\n"
    "v_cmp_eq_u32 vcc, 0xff81ff81, v55\n"
    "s_or_b64 s[56:57], s[56:57], vcc\n"
    "s_cbranch_scc0 Bvok_%=\n"
    "Bvslow1_%=:\n"
    "s_add_u32 s43, s43, 1\n"
    "s_cmp_gt_u32 s43, 2097152\n"
    "s_cbranch_scc1 Bdead_%=\n"
    "s_cmp_lg_u32 %[w], 0\n"
    "s_cbranch_scc1 Bvrel_%=\n"
    "s_cmp_lg_u32 s82, 0\n"
    "s_cbranch_scc1 Bvs3_%=\n"
    "s_mov_b32 s82, 1\n"
    "s_add_u32 s66, s83, 86\n"
    "s_branch Bvw_%=\n"
    "Bvs2_%=:\n"
    "s_sleep 4\n"
    "Bvs3_%=:\n"
    "global_load_dwordx2 v[110:111], %[vhv], s[54:55] offset:0 sc1\n"
    "global_load_dwordx2 v[112:113], v116, s[54:55] offset:0 sc1\n"
    "global_load_dwordx2 v[114:115], v117, s[54:55] offset:0 sc1\n"
    "s_add_u32 s43, s43, 1\n"
    "s_cmp_gt_u32 s43, 2097152\n"
    "s_cbranch_scc1 Bdead_%=\n"
    "s_waitcnt vmcnt(0)\n"
    "v_min3_u32 v114, v110, v112, v114\n"
    "v_max_u32 v112, v110, v112\n"
    "v_mov_b32 v110, v114\n"
    "v_cmp_gt_u32 vcc, s85, v110\n"
    "s_cbranch_vccnz Bvs2_%=\n"
    "v_subrev_u32 v111, s66, v111\n"
    "v_subrev_u32 v113, s66, v113\n"
    "v_subrev_u32 v115, s66, v115\n"
    "v_max3_i32 v111, v111, v113, v115\n"
    "s_nop 1\n"
    "v_max_i32_dpp v111, v111, v111 row_ror:8 row_mask:0xf bank_mask:0xf\n"
    "s_nop 1\n"
    "v_max_i32_dpp v111, v111, v111 row_ror:4 row_mask:0xf bank_mask:0xf\n"
    "s_nop 1\n"
    "v_max_i32_dpp v111, v111, v111 row_ror:2 row_mask:0xf bank_mask:0xf\n"
    "s_nop 1\n"
    "v_max_i32_dpp v111, v111, v111 row_ror:1 row_mask:0xf bank_mask:0xf\n"
    "v_cmp_lt_u32 vcc, s85, v112\n"
    "v_readlane_b32 s58, v111, 0\n"
    "v_readlane_b32 s59, v111, 32\n"
    "v_readlane_b32 s41, v111, 48\n"
    "s_nop 0\n"
    "s_max_i32 s59, s59, s41\n"
    "s_sub_u32 s59, s59, s58\n"
    "s_add_u32 s58, s58, s66\n"
    "s_and_b32 vcc_lo, vcc_lo, 0xffff\n"
    "s_cselect_b32 s41, 2, s59\n"
    "s_cmp_gt_i32 s41, 1\n"
    "s_cbranch_scc1 E1800a_%=\n"
    "E1800b_%=:\n"
    "s_sub_u32 s66, s58, 4\n"
    "s_branch Bvw_%=\n"
    "E479a_%=:\n"
    "s_add_u32 vcc_lo, s59, 110\n"
    "s_max_i32 vcc_lo, vcc_lo, 225\n"
    "s_cmp_gt_i32 s59, 54\n"
    "s_cselect_b32 s41, vcc_lo, 90\n"
    "s_add_u32 vcc_lo, s59, -22\n"
    "s_max_i32 vcc_lo, vcc_lo, 0\n"
    "s_cmp_eq_u32 s85, 2\n"
    "s_cselect_b32 s41, vcc_lo, s41\n"
    "s_add_u32 s98, s98, s41\n"
    "s_cmp_le_u32 s85, 2\n"
    "s_cbranch_scc1 E479b_%=\n"
    "v_readlane_b32 s41, v111, 16\n"
    "s_nop 0\n"
    "s_add_u32 s41, s41, s66\n"
    "s_sub_u32 s41, s41, s98\n"
    "s_max_i32 s41, s41, 0\n"
    "s_add_u32 s98, s98, s41\n"
    "s_add_u32 s77, s77, 14\n"
    "s_min_u32 s77, s77, 64\n"
    "s_branch E479b_%=\n"
    "E991a_%=:\n"
    "s_add_u32 vcc_lo, s59, 110\n"
    "s_max_i32 vcc_lo, vcc_lo, 225\n"
    "s_cmp_gt_i32 s59, 54\n"
    "s_cselect_b32 s41, vcc_lo, 90\n"
    "s_add_u32 vcc_lo, s59, -22\n"
    "s_max_i32 vcc_lo, vcc_lo, 0\n"
    "s_cmp_eq_u32 s85, 2\n"
    "s_cselect_b32 s41, vcc_lo, s41\n"
    "s_add_u32 s58, s58, s41\n"
    "s_cmp_le_u32 s85, 2\n"
    "s_cbranch_scc1 E991b_%=\n"
    "v_readlane_b32 s41, v111, 16\n"
    "s_nop 0\n"
    "s_add_u32 s41, s41, s66\n"
    "s_sub_u32 s41, s41, s58\n"
    "s_max_i32 s41, s41, 0\n"
    "s_add_u32 s58, s58, s41\n"
    "s_branch E991b_%=\n"
    "E1800a_%=:\n"
    "s_add_u32 vcc_lo, s59, 110\n"
    "s_max_i32 vcc_lo, vcc_lo, 225\n"
    "s_cmp_gt_i32 s59, 54\n"
    "s_cselect_b32 s41, vcc_lo, 90\n"
    "s_add_u32 vcc_lo, s59, -22\n"
    "s_max_i32 vcc_lo, vcc_lo, 0\n"
    "s_cmp_eq_u32 s85, 2\n"
    "s_cselect_b32 s41, vcc_lo, s41\n"
    "s_add_u32 s58, s58, s41\n"
    "s_cmp_le_u32 s85, 2\n"
    "s_cbranch_scc1 E1800b_%=\n"
    "v_readlane_b32 s41, v111, 16\n"
    "s_nop 0\n"
    "s_add_u32 s41, s41, s66\n"
    "s_sub_u32 s41, s41, s58\n"
    "s_max_i32 s41, s41, 0\n"
    "s_add_u32 s58, s58, s41\n"
    "s_branch E1800b_%=\n"
    "Bdead_%=:\n"
    "v_mov_b32 v74, 768\n"
    "v_mov_b32 v75, 2098432\n"
    "global_store_dword v75, v74, %[ws] offset:0 sc1\n"
    "s_waitcnt vmcnt(0)\n"
    "s_endpgm\n"
    "Bfin_%=:\n"
    "s_waitcnt vmcnt(0)\n"
    "s_endpgm\n"
    "s_mov_b32 m0, s99\n"
    :
    : [vw] "v"(vw), [vrd0] "v"(vrd0), [vrd1] "v"(vrd1), [vg16] "v"(vg16), [vhv] "v"(vhv), [vmv] "v"(vmv), [vidx] "v"(vidx), [vrw] "v"(vrw), [vrr] "v"(vrr),
      [vwb] "v"(vwb), [vrdv] "v"(lb + crd128(lane, w)), [vmv0] "v"(vmv0), [vmv2] "v"(vmv2), [vidx2] "v"(vidx2), [vlm] "v"(vlm),
      [ws] "s"(p.ws), [wo] "s"((const char*)p.wo + ((size_t)w * 1024 * DM + c * 16) * 2),
      [wv] "s"((const char*)p.wv + (size_t)max(vh, 0) * 128), [g] "s"(x & 2), [w] "s"(w),
      [da] "s"(p.dA), [db] "s"(p.dB), [dp] "s"(p.dP), [lb] "s"(lb), [vh] "s"(vh), [bi] "s"((C_HINT << 14) + 2048 + bi * 32)
    : "memory", "vcc", "scc", "v16", "v17", "v18", "v19", "v20", "v21", "v22", "v23", "v24", "v25", "v26", "v27", "v28", "v29", "v30", "v31", "v32", "v33", "v34", "v35", "v36", "v37", "v38", "v39", "v40", "v41", "v42", "v43", "v44", "v45", "v46", "v47", "v48", "v49", "v50", "v51", "v52", "v53", "v54", "v55", "v56", "v57", "v58", "v59", "v60", "v61", "v62", "v63", "v64", "v65", "v66", "v67", "v68", "v69", "v70", "v71", "v72", "v73", "v74", "v75", "v76", "v77", "v78", "v79", "v80", "v81", "v82", "v83", "v84", "v85", "v86", "v87", "v88", "v89", "v90", "v91", "v92", "v93", "v94", "v95", "v96", "v97", "v98", "v99", "v100", "v101", "v102", "v103", "v104", "v105", "v106", "v107", "v108", "v109", "v110", "v111", "v112", "v113", "v114", "v115", "v116", "v117", "v118", "v119", "v120", "v121", "v122", "v123", "v124", "v125", "v126", "v127", "v128", "v129", "v130", "v131", "v132", "v133", "v134", "v135", "v136", "v137", "v138", "v139", "v140", "v141", "v142", "v143", "v144", "v145", "v146", "v147", "v148", "v149", "v150", "v151", "v152", "v153", "v154", "v155", "v156", "v157", "v158", "v159", "v160", "v161", "v162", "v163", "v164", "v165", "v166", "v167", "v168", "v169", "v170", "v171", "v172", "v173", "v174", "v175", "v176", "v177", "v178", "v179", "v180", "v181", "v182", "v183", "v184", "v185", "v186", "v187", "v188", "v189", "v190", "v191", "v192", "v193", "v194", "v195", "v196", "v197", "v198", "v199", "v200", "v201", "v202", "v203", "v204", "v205", "v206", "v207", "v208", "v209", "v210", "v211", "v212", "v213", "v214", "v215", "v216", "v217", "v218", "v219", "v220", "v221", "v222", "v223", "v224", "v225", "v226", "v227", "v228", "v229", "v230", "v231", "v232", "v233", "v234", "v235", "v236", "v237", "v238", "v239", "v240", "v241", "v242", "v243", "v244", "v245", "v246", "v247", "v248", "v249", "v250", "v251", "v252", "v253", "v254", "v255", "a0", "a1", "a2", "a3", "a4", "a5", "a6", "a7", "a8", "a9", "a10", "a11", "a12", "a13", "a14", "a15", "a16", "a17", "a18", "a19", "a20", "a21", "a22", "a23", "a24", "a25", "a26", "a27", "a28", "a29", "a30", "a31", "a32", "a33", "a34", "a35", "a36", "a37", "a38", "a39", "a40", "a41", "a42", "a43", "a44", "a45", "a46", "a47", "a48", "a49", "a50", "a51", "a52", "a53", "a54", "a55", "a56", "a57", "a58", "a59", "a60", "a61", "a62", "a63", "a64", "a65", "a66", "a67", "a68", "a69", "a70", "a71", "a72", "a73", "a74", "a75", "a76", "a77", "a78", "a79", "a80", "a81", "a82", "a83", "a84", "a85", "a86", "a87", "a88", "a89", "a90", "a91", "a92", "a93", "a94", "a95", "a96", "a97", "a98", "a99", "a100", "a101", "a102", "a103", "a104", "a105", "a106", "a107", "a108", "a109", "a110", "a111", "a112", "a113", "a114", "a115", "a116", "a117", "a118", "a119", "a120", "a121", "a122", "a123", "a124", "a125", "a126", "a127", "a128", "a129", "a130", "a131", "a132", "a133", "a134", "a135", "a136", "a137", "a138", "a139", "a140", "a141", "a142", "a143", "a144", "a145", "a146", "a147", "a148", "a149", "a150", "a151", "a152", "a153", "a154", "a155", "a156", "a157", "a158", "a159", "a160", "a161", "a162", "a163", "a164", "a165", "a166", "a167", "a168", "a169", "a170", "a171", "a172", "a173", "a174", "a175", "a176", "a177", "a178", "a179", "a180", "a181", "a182", "a183", "a184", "a185", "a186", "a187", "a188", "a189", "a190", "a191", "a192", "a193", "a194", "a195", "a196", "a197", "a198", "a199", "a200", "a201", "a202", "a203", "a204", "a205", "a206", "a207", "a208", "a209", "a210", "a211", "a212", "a213", "a214", "a215", "a216", "a217", "a218", "a219", "a220", "a221", "a222", "a223", "a224", "a225", "a226", "a227", "a228", "a229", "a230", "a231", "a232", "a233", "a234", "a235", "a236", "a237", "a238", "a239", "a240", "a241", "a242", "a243", "a244", "a245", "a246", "a247", "a248", "a249", "a250", "a251", "a252", "a253", "a254", "a255", "s40", "s41", "s42", "s43", "s44", "s45", "s46", "s47", "s48", "s49", "s50", "s51", "s52", "s53", "s54", "s55", "s56", "s57", "s58", "s59", "s60", "s61", "s62", "s63", "s64", "s65", "s66", "s67", "s68", "s69", "s70", "s71", "s72", "s73", "s74", "s75", "s76", "s77", "s78", "s79", "s80", "s81", "s82", "s83", "s84", "s85", "s86", "s87", "s88", "s89", "s90", "s91", "s92", "s93", "s94", "s95", "s96", "s97", "s98", "s99");
}

#define LN_OPS \
      [vxi] "v"((lane & 15) * (DM * 2) + (w * 4 + (lane >> 4)) * 16), [vxo] "v"((w * 4 + (lane >> 4)) * 256 + (lane & 15) * 16), \
      [vrw] "v"(lb + w * 64 + (lane & 15) * 4), [vrr] "v"(lb + (lane & 15) * 4), [lx] "s"((const char*)p.x + (size_t)max(li, 0) * 16 * DM * 2), \
      [lo] "s"(p.ws + O_XN + (size_t)max(li, 0) * 20480), [iw] "s"(p.ws + F_INIT + (8 + max(li, 0)) * 128)

template <bool ISV>
DI void role_qkv(const Params& p, int id, int li, lptr L, int lane, int w) {
  const u32 lb = __builtin_amdgcn_readfirstlane((u32)(size_t)L);
  const int m = id >> 5;
  const u16* wsrc = m == 0 ? p.wq : (m == 1 ? p.wk : p.wv);
#define QKV_OPS \
    : \
    : [vw] "v"(csrc256(lane, w, 8192)), [vrd0] "v"(lb + 40960 + crd256(lane, 2 * w)), [vrd1] "v"(lb + 40960 + crd256(lane, 2 * w + 1)), [vxv] "v"(w * 10240 + lane * 16), [vlr] "v"(lb + lane * 16), \
      [vovo] "v"((ISV ? w * 2048 : w * 1024) + lane * 16), [ws] "s"(p.ws), [wp] "s"((const char*)wsrc + (size_t)(id & 31) * 256), \
      [og] "s"(p.ws + O_QF + ((size_t)m << 25) + (size_t)(id & 31) * 16384), [fl] "s"(p.ws + F_QKV + id * 128), [w] "s"(w), [lb] "s"(lb), [li] "s"(li), \
      LN_OPS \
    : "memory", "vcc", "scc", "v16", "v17", "v18", "v19", "v20", "v21", "v22", "v23", "v24", "v25", "v26", "v27", "v28", "v29", "v30", "v31", "v32", "v33", "v34", "v35", "v36", "v37", "v38", "v39", "v40", "v41", "v42", "v43", "v44", "v45", "v46", "v47", "v48", "v49", "v50", "v51", "v52", "v53", "v54", "v55", "v56", "v57", "v58", "v59", "v60", "v61", "v62", "v63", "v64", "v65", "v66", "v67", "v68", "v69", "v70", "v71", "v72", "v73", "v74", "v75", "v76", "v77", "v78", "v79", "v80", "v81", "v82", "v83", "v84", "v85", "v86", "v87", "v88", "v89", "v90", "v91", "v92", "v93", "v94", "v95", "v96", "v97", "v98", "v99", "v100", "v101", "v102", "v103", "v104", "v105", "v106", "v107", "v108", "v109", "v110", "v111", "v112", "v113", "v114", "v115", "v116", "v117", "v118", "v119", "v120", "v121", "v122", "v123", "v124", "v125", "v126", "v127", "v128", "v129", "v130", "v131", "v132", "v133", "v134", "v135", "v136", "v137", "v138", "v139", "v140", "v141", "v142", "v143", "v144", "v145", "v146", "v147", "v148", "v149", "v150", "v151", "v152", "v153", "v154", "v155", "v156", "v157", "v158", "v159", "v160", "v161", "v162", "v163", "v164", "v165", "v166", "v167", "v168", "v169", "v170", "v171", "v172", "v173", "v174", "v175", "v176", "v177", "v178", "v179", "v180", "v181", "v182", "v183", "v184", "v185", "v186", "v187", "v188", "v189", "v190", "v191", "v192", "v193", "v194", "v195", "v196", "v197", "v198", "v199", "v200", "v201", "v202", "v203", "v204", "v205", "v206", "v207", "v208", "v209", "v210", "v211", "v212", "v213", "v214", "v215", "v216", "v217", "v218", "v219", "v220", "v221", "v222", "v223", "v224", "v225", "v226", "v227", "v228", "v229", "v230", "v231", "v232", "v233", "v234", "v235", "v236", "v237", "v238", "v239", "v240", "v241", "v242", "v243", "v244", "v245", "v246", "v247", "v248", "v249", "v250", "v251", "v252", "v253", "v254", "v255", "a0", "a1", "a2", "a3", "a4", "a5", "a6", "a7", "a8", "a9", "a10", "a11", "a12", "a13", "a14", "a15", "a16", "a17", "a18", "a19", "a20", "a21", "a22", "a23", "a24", "a25", "a26", "a27", "a28", "a29", "a30", "a31", "a32", "a33", "a34", "a35", "a36", "a37", "a38", "a39", "a40", "a41", "a42", "a43", "a44", "a45", "a46", "a47", "a48", "a49", "a50", "a51", "a52", "a53", "a54", "a55", "a56", "a57", "a58", "a59", "a60", "a61", "a62", "a63", "a64", "a65", "a66", "a67", "a68", "a69", "a70", "a71", "a72", "a73", "a74", "a75", "a76", "a77", "a78", "a79", "a80", "a81", "a82", "a83", "a84", "a85", "a86", "a87", "a88", "a89", "a90", "a91", "a92", "a93", "a94", "a95", "a96", "a97", "a98", "a99", "a100", "a101", "a102", "a103", "a104", "a105", "a106", "a107", "a108", "a109", "a110", "a111", "a112", "a113", "a114", "a115", "a116", "a117", "a118", "a119", "a120", "a121", "a122", "a123", "a124", "a125", "a126", "a127", "a128", "a129", "a130", "a131", "a132", "a133", "a134", "a135", "a136", "a137", "a138", "a139", "a140", "a141", "a142", "a143", "a144", "a145", "a146", "a147", "a148", "a149", "a150", "a151", "a152", "a153", "a154", "a155", "a156", "a157", "a158", "a159", "a160", "a161", "a162", "a163", "a164", "a165", "a166", "a167", "a168", "a169", "a170", "a171", "a172", "a173", "a174", "a175", "a176", "a177", "a178", "a179", "a180", "a181", "a182", "a183", "a184", "a185", "a186", "a187", "a188", "a189", "a190", "a191", "a192", "a193", "a194", "a195", "a196", "a197", "a198", "a199", "a200", "a201", "a202", "a203", "a204", "a205", "a206", "a207", "a208", "a209", "a210", "a211", "a212", "a213", "a214", "a215", "a216", "a217", "a218", "a219", "a220", "a221", "a222", "a223", "a224", "a225", "a226", "a227", "a228", "a229", "a230", "a231", "a232", "a233", "a234", "a235", "a236", "a237", "a238", "a239", "a240", "a241", "a242", "a243", "a244", "a245", "a246", "a247", "a248", "a249", "a250", "a251", "a252", "a253", "a254", "a255", "s40", "s41", "s42", "s43", "s44", "s45", "s46", "s47", "s48", "s49", "s50", "s51", "s52", "s53", "s54", "s55", "s56", "s57", "s58", "s59", "s60", "s61", "s62", "s63", "s64", "s65", "s66", "s67", "s99"
  if (ISV) {
    asm volatile(
    "s_mov_b32 s99, m0\n"
    "s_mul_i32 s52, %[w], 10240\n"
    "s_add_u32 s52, s52, %[lb]\n"
    "s_mov_b64 s[46:47], %[ws]\n"
    "s_add_u32 s46, s46, 3145728\n"
    "s_addc_u32 s47, s47, 0\n"
    "s_lshl_b32 s60, %[w], 11\n"
    "s_add_u32 s60, s60, %[lb]\n"
    "s_add_u32 s60, s60, 40960\n"
    "v_add_u32 v90, 31744, %[vw]\n"
    "s_cmp_lt_i32 %[li], 0\n"
    "s_cbranch_scc1 Vl0_%=\n"
    "global_load_dwordx4 v[100:103], %[vxi], %[lx] offset:0\n"
    "global_load_dwordx4 v[104:107], %[vxi], %[lx] offset:256\n"
    "global_load_dwordx4 v[108:111], %[vxi], %[lx] offset:512\n"
    "global_load_dwordx4 v[112:115], %[vxi], %[lx] offset:768\n"
    "global_load_dwordx4 v[116:119], %[vxi], %[lx] offset:1024\n"
    "Vl0_%=:\n"
    "s_mov_b32 s64, 2097152\n"
    "s_load_dwordx2 s[64:65], %[ws], s64\n"
    "v_add_u32 v94, 65536, %[vrd0]\n"
    "v_add_u32 v95, 65536, %[vrd1]\n"
    "s_mov_b64 s[44:45], %[wp]\n"
    "s_add_u32 m0, s60, 0\n"
    "s_nop 0\n"
    "global_load_lds_dwordx4 %[vw], s[44:45] offset:0 nt\n"
    "global_load_lds_dwordx4 v90, s[44:45] offset:1024 nt\n"
    "s_add_u32 s44, s44, 262144\n"
    "s_addc_u32 s45, s45, 0\n"
    "s_add_u32 m0, s60, 8192\n"
    "s_nop 0\n"
    "global_load_lds_dwordx4 %[vw], s[44:45] offset:0 nt\n"
    "global_load_lds_dwordx4 v90, s[44:45] offset:1024 nt\n"
    "s_add_u32 s44, s44, 262144\n"
    "s_addc_u32 s45, s45, 0\n"
    "s_add_u32 m0, s60, 16384\n"
    "s_nop 0\n"
    "global_load_lds_dwordx4 %[vw], s[44:45] offset:0 nt\n"
    "global_load_lds_dwordx4 v90, s[44:45] offset:1024 nt\n"
    "s_add_u32 s44, s44, 262144\n"
    "s_addc_u32 s45, s45, 0\n"
    "s_add_u32 m0, s60, 24576\n"
    "s_nop 0\n"
    "global_load_lds_dwordx4 %[vw], s[44:45] offset:0 nt\n"
    "global_load_lds_dwordx4 v90, s[44:45] offset:1024 nt\n"
    "s_add_u32 s44, s44, 262144\n"
    "s_addc_u32 s45, s45, 0\n"
    "s_add_u32 m0, s60, 32768\n"
    "s_nop 0\n"
    "global_load_lds_dwordx4 %[vw], s[44:45] offset:0 nt\n"
    "global_load_lds_dwordx4 v90, s[44:45] offset:1024 nt\n"
    "s_add_u32 s44, s44, 262144\n"
    "s_addc_u32 s45, s45, 0\n"
    "s_add_u32 m0, s60, 40960\n"
    "s_nop 0\n"
    "global_load_lds_dwordx4 %[vw], s[44:45] offset:0 nt\n"
    "global_load_lds_dwordx4 v90, s[44:45] offset:1024 nt\n"
    "s_add_u32 s44, s44, 262144\n"
    "s_addc_u32 s45, s45, 0\n"
    "s_add_u32 m0, s60, 49152\n"
    "s_nop 0\n"
    "global_load_lds_dwordx4 %[vw], s[44:45] offset:0 nt\n"
    "global_load_lds_dwordx4 v90, s[44:45] offset:1024 nt\n"
    "s_add_u32 s44, s44, 262144\n"
    "s_addc_u32 s45, s45, 0\n"
    "s_add_u32 m0, s60, 57344\n"
    "s_nop 0\n"
    "global_load_lds_dwordx4 %[vw], s[44:45] offset:0 nt\n"
    "global_load_lds_dwordx4 v90, s[44:45] offset:1024 nt\n"
    "s_add_u32 s44, s44, 262144\n"
    "s_addc_u32 s45, s45, 0\n"
    "s_add_u32 m0, s60, 65536\n"
    "s_nop 0\n"
    "global_load_lds_dwordx4 %[vw], s[44:45] offset:0 nt\n"
    "global_load_lds_dwordx4 v90, s[44:45] offset:1024 nt\n"
    "s_add_u32 s44, s44, 262144\n"
    "s_addc_u32 s45, s45, 0\n"
    "s_add_u32 m0, s60, 73728\n"
    "s_nop 0\n"
    "global_load_lds_dwordx4 %[vw], s[44:45] offset:0 nt\n"
    "global_load_lds_dwordx4 v90, s[44:45] offset:1024 nt\n"
    "s_add_u32 s44, s44, 262144\n"
    "s_addc_u32 s45, s45, 0\n"
    "s_add_u32 m0, s60, 81920\n"
    "s_nop 0\n"
    "global_load_lds_dwordx4 %[vw], s[44:45] offset:0 nt\n"
    "global_load_lds_dwordx4 v90, s[44:45] offset:1024 nt\n"
    "s_add_u32 s44, s44, 262144\n"
    "s_addc_u32 s45, s45, 0\n"
    "s_add_u32 m0, s60, 90112\n"
    "s_nop 0\n"
    "global_load_lds_dwordx4 %[vw], s[44:45] offset:0 nt\n"
    "global_load_lds_dwordx4 v90, s[44:45] offset:1024 nt\n"
    "s_add_u32 s44, s44, 262144\n"
    "s_addc_u32 s45, s45, 0\n"
    "s_waitcnt lgkmcnt(0)\n"
    "s_add_u32 s64, s64, 1\n"
    "s_and_b32 s65, s65, -8\n"
    "s_cmp_lt_i32 %[li], 0\n"
    "s_cbranch_scc1 Vl1_%=\n"
    "s_waitcnt vmcnt(24)\n"
    "v_lshlrev_b32 v120, 16, v100\n"
    "v_and_b32 v121, 0xffff0000, v100\n"
    "v_lshlrev_b32 v122, 16, v101\n"
    "v_and_b32 v123, 0xffff0000, v101\n"
    "v_lshlrev_b32 v124, 16, v102\n"
    "v_and_b32 v125, 0xffff0000, v102\n"
    "v_lshlrev_b32 v126, 16, v103\n"
    "v_and_b32 v127, 0xffff0000, v103\n"
    "v_lshlrev_b32 v128, 16, v104\n"
    "v_and_b32 v129, 0xffff0000, v104\n"
    "v_lshlrev_b32 v130, 16, v105\n"
    "v_and_b32 v131, 0xffff0000, v105\n"
    "v_lshlrev_b32 v132, 16, v106\n"
    "v_and_b32 v133, 0xffff0000, v106\n"
    "v_lshlrev_b32 v134, 16, v107\n"
    "v_and_b32 v135, 0xffff0000, v107\n"
    "v_lshlrev_b32 v136, 16, v108\n"
    "v_and_b32 v137, 0xffff0000, v108\n"
    "v_lshlrev_b32 v138, 16, v109\n"
    "v_and_b32 v139, 0xffff0000, v109\n"
    "v_lshlrev_b32 v140, 16, v110\n"
    "v_and_b32 v141, 0xffff0000, v110\n"
    "v_lshlrev_b32 v142, 16, v111\n"
    "v_and_b32 v143, 0xffff0000, v111\n"
    "v_lshlrev_b32 v144, 16, v112\n"
    "v_and_b32 v145, 0xffff0000, v112\n"
    "v_lshlrev_b32 v146, 16, v113\n"
    "v_and_b32 v147, 0xffff0000, v113\n"
    "v_lshlrev_b32 v148, 16, v114\n"
    "v_and_b32 v149, 0xffff0000, v114\n"
    "v_lshlrev_b32 v150, 16, v115\n"
    "v_and_b32 v151, 0xffff0000, v115\n"
    "v_lshlrev_b32 v152, 16, v116\n"
    "v_and_b32 v153, 0xffff0000, v116\n"
    "v_lshlrev_b32 v154, 16, v117\n"
    "v_and_b32 v155, 0xffff0000, v117\n"
    "v_lshlrev_b32 v156, 16, v118\n"
    "v_and_b32 v157, 0xffff0000, v118\n"
    "v_lshlrev_b32 v158, 16, v119\n"
    "v_and_b32 v159, 0xffff0000, v119\n"
    "v_pk_add_f32 v[162:163], v[120:121], v[122:123]\n"
    "v_pk_add_f32 v[162:163], v[162:163], v[124:125]\n"
    "v_pk_add_f32 v[162:163], v[162:163], v[126:127]\n"
    "v_pk_add_f32 v[162:163], v[162:163], v[128:129]\n"
    "v_pk_add_f32 v[162:163], v[162:163], v[130:131]\n"
    "v_pk_add_f32 v[162:163], v[162:163], v[132:133]\n"
    "v_pk_add_f32 v[162:163], v[162:163], v[134:135]\n"
    "v_pk_add_f32 v[162:163], v[162:163], v[136:137]\n"
    "v_pk_add_f32 v[162:163], v[162:163], v[138:139]\n"
    "v_pk_add_f32 v[162:163], v[162:163], v[140:141]\n"
    "v_pk_add_f32 v[162:163], v[162:163], v[142:143]\n"
    "v_pk_add_f32 v[162:163], v[162:163], v[144:145]\n"
    "v_pk_add_f32 v[162:163], v[162:163], v[146:147]\n"
    "v_pk_add_f32 v[162:163], v[162:163], v[148:149]\n"
    "v_pk_add_f32 v[162:163], v[162:163], v[150:151]\n"
    "v_pk_add_f32 v[162:163], v[162:163], v[152:153]\n"
    "v_pk_add_f32 v[162:163], v[162:163], v[154:155]\n"
    "v_pk_add_f32 v[162:163], v[162:163], v[156:157]\n"
    "v_pk_add_f32 v[162:163], v[162:163], v[158:159]\n"
    "v_add_f32 v162, v162, v163\n"
    "v_mov_b32 v164, v162\n"
    "s_nop 1\n"
    "v_permlane16_swap_b32 v162, v164\n"
    "v_add_f32 v162, v162, v164\n"
    "v_mov_b32 v164, v162\n"
    "s_nop 1\n"
    "v_permlane32_swap_b32 v162, v164\n"
    "v_add_f32 v162, v162, v164\n"
    "ds_write_b32 %[vrw], v162 offset:0\n"
    "s_waitcnt lgkmcnt(0)\n"
    "s_barrier\n"
    "ds_read_b32 v170, %[vrr] offset:0\n"
    "ds_read_b32 v171, %[vrr] offset:64\n"
    "ds_read_b32 v172, %[vrr] offset:128\n"
    "ds_read_b32 v173, %[vrr] offset:192\n"
    "s_waitcnt lgkmcnt(2)\n"
    "v_add_f32 v170, v170, v171\n"
    "s_waitcnt lgkmcnt(0)\n"
    "v_add_f32 v172, v172, v173\n"
    "v_add_f32 v170, v170, v172\n"
    "v_mul_f32 v160, 0x3acccccd, v170\n"
    "v_pk_add_f32 v[168:169], v[120:121], v[160:161] op_sel_hi:[1,0] neg_lo:[0,1] neg_hi:[0,1]\n"
    "v_pk_mul_f32 v[162:163], v[168:169], v[168:169]\n"
    "v_pk_add_f32 v[168:169], v[122:123], v[160:161] op_sel_hi:[1,0] neg_lo:[0,1] neg_hi:[0,1]\n"
    "v_pk_fma_f32 v[162:163], v[168:169], v[168:169], v[162:163]\n"
    "v_pk_add_f32 v[168:169], v[124:125], v[160:161] op_sel_hi:[1,0] neg_lo:[0,1] neg_hi:[0,1]\n"
    "v_pk_fma_f32 v[162:163], v[168:169], v[168:169], v[162:163]\n"
    "v_pk_add_f32 v[168:169], v[126:127], v[160:161] op_sel_hi:[1,0] neg_lo:[0,1] neg_hi:[0,1]\n"
    "v_pk_fma_f32 v[162:163], v[168:169], v[168:169], v[162:163]\n"
    "v_pk_add_f32 v[168:169], v[128:129], v[160:161] op_sel_hi:[1,0] neg_lo:[0,1] neg_hi:[0,1]\n"
    "v_pk_fma_f32 v[162:163], v[168:169], v[168:169], v[162:163]\n"
    "v_pk_add_f32 v[168:169], v[130:131], v[160:161] op_sel_hi:[1,0] neg_lo:[0,1] neg_hi:[0,1]\n"
    "v_pk_fma_f32 v[162:163], v[168:169], v[168:169], v[162:163]\n"
    "v_pk_add_f32 v[168:169], v[132:133], v[160:161] op_sel_hi:[1,0] neg_lo:[0,1] neg_hi:[0,1]\n"
    "v_pk_fma_f32 v[162:163], v[168:169], v[168:169], v[162:163]\n"
    "v_pk_add_f32 v[168:169], v[134:135], v[160:161] op_sel_hi:[1,0] neg_lo:[0,1] neg_hi:[0,1]\n"
    "v_pk_fma_f32 v[162:163], v[168:169], v[168:169], v[162:163]\n"
    "v_pk_add_f32 v[168:169], v[136:137], v[160:161] op_sel_hi:[1,0] neg_lo:[0,1] neg_hi:[0,1]\n"
    "v_pk_fma_f32 v[162:163], v[168:169], v[168:169], v[162:163]\n"
    "v_pk_add_f32 v[168:169], v[138:139], v[160:161] op_sel_hi:[1,0] neg_lo:[0,1] neg_hi:[0,1]\n"
    "v_pk_fma_f32 v[162:163], v[168:169], v[168:169], v[162:163]\n"
    "v_pk_add_f32 v[168:169], v[140:141], v[160:161] op_sel_hi:[1,0] neg_lo:[0,1] neg_hi:[0,1]\n"
    "v_pk_fma_f32 v[162:163], v[168:169], v[168:169], v[162:163]\n"
    "v_pk_add_f32 v[168:169], v[142:143], v[160:161] op_sel_hi:[1,0] neg_lo:[0,1] neg_hi:[0,1]\n"
    "v_pk_fma_f32 v[162:163], v[168:169], v[168:169], v[162:163]\n"
    "v_pk_add_f32 v[168:169], v[144:145], v[160:161] op_sel_hi:[1,0] neg_lo:[0,1] neg_hi:[0,1]\n"
    "v_pk_fma_f32 v[162:163], v[168:169], v[168:169], v[162:163]\n"
    "v_pk_add_f32 v[168:169], v[146:147], v[160:161] op_sel_hi:[1,0] neg_lo:[0,1] neg_hi:[0,1]\n"
    "v_pk_fma_f32 v[162:163], v[168:169], v[168:169], v[162:163]\n"
    "v_pk_add_f32 v[168:169], v[148:149], v[160:161] op_sel_hi:[1,0] neg_lo:[0,1] neg_hi:[0,1]\n"
    "v_pk_fma_f32 v[162:163], v[168:169], v[168:169], v[162:163]\n"
    "v_pk_add_f32 v[168:169], v[150:151], v[160:161] op_sel_hi:[1,0] neg_lo:[0,1] neg_hi:[0,1]\n"
    "v_pk_fma_f32 v[162:163], v[168:169], v[168:169], v[162:163]\n"
    "v_pk_add_f32 v[168:169], v[152:153], v[160:161] op_sel_hi:[1,0] neg_lo:[0,1] neg_hi:[0,1]\n"
    "v_pk_fma_f32 v[162:163], v[168:169], v[168:169], v[162:163]\n"
    "v_pk_add_f32 v[168:169], v[154:155], v[160:161] op_sel_hi:[1,0] neg_lo:[0,1] neg_hi:[0,1]\n"
    "v_pk_fma_f32 v[162:163], v[168:169], v[168:169], v[162:163]\n"
    "v_pk_add_f32 v[168:169], v[156:157], v[160:161] op_sel_hi:[1,0] neg_lo:[0,1] neg_hi:[0,1]\n"
    "v_pk_fma_f32 v[162:163], v[168:169], v[168:169], v[162:163]\n"
    "v_pk_add_f32 v[168:169], v[158:159], v[160:161] op_sel_hi:[1,0] neg_lo:[0,1] neg_hi:[0,1]\n"
    "v_pk_fma_f32 v[162:163], v[168:169], v[168:169], v[162:163]\n"
    "v_add_f32 v162, v162, v163\n"
    "v_mov_b32 v164, v162\n"
    "s_nop 1\n"
    "v_permlane16_swap_b32 v162, v164\n"
    "v_add_f32 v162, v162, v164\n"
    "v_mov_b32 v164, v162\n"
    "s_nop 1\n"
    "v_permlane32_swap_b32 v162, v164\n"
    "v_add_f32 v162, v162, v164\n"
    "ds_write_b32 %[vrw], v162 offset:256\n"
    "s_waitcnt lgkmcnt(0)\n"
    "s_barrier\n"
    "ds_read_b32 v170, %[vrr] offset:256\n"
    "ds_read_b32 v171, %[vrr] offset:320\n"
    "ds_read_b32 v172, %[vrr] offset:384\n"
    "ds_read_b32 v173, %[vrr] offset:448\n"
    "s_waitcnt lgkmcnt(2)\n"
    "v_add_f32 v170, v170, v171\n"
    "s_waitcnt lgkmcnt(0)\n"
    "v_add_f32 v172, v172, v173\n"
    "v_add_f32 v170, v170, v172\n"
    "v_mul_f32 v166, 0x3acccccd, v170\n"
    "v_cvt_pk_bf16_f32 v166, v166, v166\n"
    "v_and_b32 v166, 0xffff0000, v166\n"
    "v_add_f32 v166, 0x3727c5ac, v166\n"
    "v_cvt_pk_bf16_f32 v166, v166, v166\n"
    "v_and_b32 v166, 0xffff0000, v166\n"
    "v_sqrt_f32 v166, v166\n"
    "s_nop 0\n"
    "v_cvt_pk_bf16_f32 v166, v166, v166\n"
    "v_and_b32 v166, 0xffff0000, v166\n"
    "v_rcp_f32 v166, v166\n"
    "v_cvt_pk_bf16_f32 v160, v160, v160\n"
    "v_and_b32 v160, 0xffff0000, v160\n"
    "s_mov_b64 s[62:63], %[lo]\n"
    "v_pk_add_f32 v[168:169], v[120:121], v[160:161] op_sel_hi:[1,0] neg_lo:[0,1] neg_hi:[0,1]\n"
    "v_cvt_pk_bf16_f32 v169, v168, v169\n"
    "v_lshlrev_b32 v168, 16, v169\n"
    "v_and_b32 v169, 0xffff0000, v169\n"
    "v_pk_mul_f32 v[168:169], v[168:169], v[166:167] op_sel_hi:[1,0]\n"
    "v_cvt_pk_bf16_f32 v100, v168, v169\n"
    "v_pk_add_f32 v[168:169], v[122:123], v[160:161] op_sel_hi:[1,0] neg_lo:[0,1] neg_hi:[0,1]\n"
    "v_cvt_pk_bf16_f32 v169, v168, v169\n"
    "v_lshlrev_b32 v168, 16, v169\n"
    "v_and_b32 v169, 0xffff0000, v169\n"
    "v_pk_mul_f32 v[168:169], v[168:169], v[166:167] op_sel_hi:[1,0]\n"
    "v_cvt_pk_bf16_f32 v101, v168, v169\n"
    "v_pk_add_f32 v[168:169], v[124:125], v[160:161] op_sel_hi:[1,0] neg_lo:[0,1] neg_hi:[0,1]\n"
    "v_cvt_pk_bf16_f32 v169, v168, v169\n"
    "v_lshlrev_b32 v168, 16, v169\n"
    "v_and_b32 v169, 0xffff0000, v169\n"
    "v_pk_mul_f32 v[168:169], v[168:169], v[166:167] op_sel_hi:[1,0]\n"
    "v_cvt_pk_bf16_f32 v102, v168, v169\n"
    "v_pk_add_f32 v[168:169], v[126:127], v[160:161] op_sel_hi:[1,0] neg_lo:[0,1] neg_hi:[0,1]\n"
    "v_cvt_pk_bf16_f32 v169, v168, v169\n"
    "v_lshlrev_b32 v168, 16, v169\n"
    "v_and_b32 v169, 0xffff0000, v169\n"
    "v_pk_mul_f32 v[168:169], v[168:169], v[166:167] op_sel_hi:[1,0]\n"
    "v_cvt_pk_bf16_f32 v103, v168, v169\n"
    "global_store_dwordx4 %[vxo], v[100:103], s[62:63] offset:0 sc1\n"
    "s_add_u32 s62, s62, 4096\n"
    "s_addc_u32 s63, s63, 0\n"
    "v_pk_add_f32 v[168:169], v[128:129], v[160:161] op_sel_hi:[1,0] neg_lo:[0,1] neg_hi:[0,1]\n"
    "v_cvt_pk_bf16_f32 v169, v168, v169\n"
    "v_lshlrev_b32 v168, 16, v169\n"
    "v_and_b32 v169, 0xffff0000, v169\n"
    "v_pk_mul_f32 v[168:169], v[168:169], v[166:167] op_sel_hi:[1,0]\n"
    "v_cvt_pk_bf16_f32 v104, v168, v169\n"
    "v_pk_add_f32 v[168:169], v[130:131], v[160:161] op_sel_hi:[1,0] neg_lo:[0,1] neg_hi:[0,1]\n"
    "v_cvt_pk_bf16_f32 v169, v168, v169\n"
    "v_lshlrev_b32 v168, 16, v169\n"
    "v_and_b32 v169, 0xffff0000, v169\n"
    "v_pk_mul_f32 v[168:169], v[168:169], v[166:167] op_sel_hi:[1,0]\n"
    "v_cvt_pk_bf16_f32 v105, v168, v169\n"
    "v_pk_add_f32 v[168:169], v[132:133], v[160:161] op_sel_hi:[1,0] neg_lo:[0,1] neg_hi:[0,1]\n"
    "v_cvt_pk_bf16_f32 v169, v168, v169\n"
    "v_lshlrev_b32 v168, 16, v169\n"
    "v_and_b32 v169, 0xffff0000, v169\n"
    "v_pk_mul_f32 v[168:169], v[168:169], v[166:167] op_sel_hi:[1,0]\n"
    "v_cvt_pk_bf16_f32 v106, v168, v169\n"
    "v_pk_add_f32 v[168:169], v[134:135], v[160:161] op_sel_hi:[1,0] neg_lo:[0,1] neg_hi:[0,1]\n"
    "v_cvt_pk_bf16_f32 v169, v168, v169\n"
    "v_lshlrev_b32 v168, 16, v169\n"
    "v_and_b32 v169, 0xffff0000, v169\n"
    "v_pk_mul_f32 v[168:169], v[168:169], v[166:167] op_sel_hi:[1,0]\n"
    "v_cvt_pk_bf16_f32 v107, v168, v169\n"
    "global_store_dwordx4 %[vxo], v[104:107], s[62:63] offset:0 sc1\n"
    "s_add_u32 s62, s62, 4096\n"
    "s_addc_u32 s63, s63, 0\n"
    "v_pk_add_f32 v[168:169], v[136:137], v[160:161] op_sel_hi:[1,0] neg_lo:[0,1] neg_hi:[0,1]\n"
    "v_cvt_pk_bf16_f32 v169, v168, v169\n"
    "v_lshlrev_b32 v168, 16, v169\n"
    "v_and_b32 v169, 0xffff0000, v169\n"
    "v_pk_mul_f32 v[168:169], v[168:169], v[166:167] op_sel_hi:[1,0]\n"
    "v_cvt_pk_bf16_f32 v108, v168, v169\n"
    "v_pk_add_f32 v[168:169], v[138:139], v[160:161] op_sel_hi:[1,0] neg_lo:[0,1] neg_hi:[0,1]\n"
    "v_cvt_pk_bf16_f32 v169, v168, v169\n"
    "v_lshlrev_b32 v168, 16, v169\n"
    "v_and_b32 v169, 0xffff0000, v169\n"
    "v_pk_mul_f32 v[168:169], v[168:169], v[166:167] op_sel_hi:[1,0]\n"
    "v_cvt_pk_bf16_f32 v109, v168, v169\n"
    "v_pk_add_f32 v[168:169], v[140:141], v[160:161] op_sel_hi:[1,0] neg_lo:[0,1] neg_hi:[0,1]\n"
    "v_cvt_pk_bf16_f32 v169, v168, v169\n"
    "v_lshlrev_b32 v168, 16, v169\n"
    "v_and_b32 v169, 0xffff0000, v169\n"
    "v_pk_mul_f32 v[168:169], v[168:169], v[166:167] op_sel_hi:[1,0]\n"
    "v_cvt_pk_bf16_f32 v110, v168, v169\n"
    "v_pk_add_f32 v[168:169], v[142:143], v[160:161] op_sel_hi:[1,0] neg_lo:[0,1] neg_hi:[0,1]\n"
    "v_cvt_pk_bf16_f32 v169, v168, v169\n"
    "v_lshlrev_b32 v168, 16, v169\n"
    "v_and_b32 v169, 0xffff0000, v169\n"
    "v_pk_mul_f32 v[168:169], v[168:169], v[166:167] op_sel_hi:[1,0]\n"
    "v_cvt_pk_bf16_f32 v111, v168, v169\n"
    "global_store_dwordx4 %[vxo], v[108:111], s[62:63] offset:0 sc1\n"
    "s_add_u32 s62, s62, 4096\n"
    "s_addc_u32 s63, s63, 0\n"
    "v_pk_add_f32 v[168:169], v[144:145], v[160:161] op_sel_hi:[1,0] neg_lo:[0,1] neg_hi:[0,1]\n"
    "v_cvt_pk_bf16_f32 v169, v168, v169\n"
    "v_lshlrev_b32 v168, 16, v169\n"
    "v_and_b32 v169, 0xffff0000, v169\n"
    "v_pk_mul_f32 v[168:169], v[168:169], v[166:167] op_sel_hi:[1,0]\n"
    "v_cvt_pk_bf16_f32 v112, v168, v169\n"
    "v_pk_add_f32 v[168:169], v[146:147], v[160:161] op_sel_hi:[1,0] neg_lo:[0,1] neg_hi:[0,1]\n"
    "v_cvt_pk_bf16_f32 v169, v168, v169\n"
    "v_lshlrev_b32 v168, 16, v169\n"
    "v_and_b32 v169, 0xffff0000, v169\n"
    "v_pk_mul_f32 v[168:169], v[168:169], v[166:167] op_sel_hi:[1,0]\n"
    "v_cvt_pk_bf16_f32 v113, v168, v169\n"
    "v_pk_add_f32 v[168:169], v[148:149], v[160:161] op_sel_hi:[1,0] neg_lo:[0,1] neg_hi:[0,1]\n"
    "v_cvt_pk_bf16_f32 v169, v168, v169\n"
    "v_lshlrev_b32 v168, 16, v169\n"
    "v_and_b32 v169, 0xffff0000, v169\n"
    "v_pk_mul_f32 v[168:169], v[168:169], v[166:167] op_sel_hi:[1,0]\n"
    "v_cvt_pk_bf16_f32 v114, v168, v169\n"
    "v_pk_add_f32 v[168:169], v[150:151], v[160:161] op_sel_hi:[1,0] neg_lo:[0,1] neg_hi:[0,1]\n"
    "v_cvt_pk_bf16_f32 v169, v168, v169\n"
    "v_lshlrev_b32 v168, 16, v169\n"
    "v_and_b32 v169, 0xffff0000, v169\n"
    "v_pk_mul_f32 v[168:169], v[168:169], v[166:167] op_sel_hi:[1,0]\n"
    "v_cvt_pk_bf16_f32 v115, v168, v169\n"
    "global_store_dwordx4 %[vxo], v[112:115], s[62:63] offset:0 sc1\n"
    "s_add_u32 s62, s62, 4096\n"
    "s_addc_u32 s63, s63, 0\n"
    "v_pk_add_f32 v[168:169], v[152:153], v[160:161] op_sel_hi:[1,0] neg_lo:[0,1] neg_hi:[0,1]\n"
    "v_cvt_pk_bf16_f32 v169, v168, v169\n"
    "v_lshlrev_b32 v168, 16, v169\n"
    "v_and_b32 v169, 0xffff0000, v169\n"
    "v_pk_mul_f32 v[168:169], v[168:169], v[166:167] op_sel_hi:[1,0]\n"
    "v_cvt_pk_bf16_f32 v116, v168, v169\n"
    "v_pk_add_f32 v[168:169], v[154:155], v[160:161] op_sel_hi:[1,0] neg_lo:[0,1] neg_hi:[0,1]\n"
    "v_cvt_pk_bf16_f32 v169, v168, v169\n"
    "v_lshlrev_b32 v168, 16, v169\n"
    "v_and_b32 v169, 0xffff0000, v169\n"
    "v_pk_mul_f32 v[168:169], v[168:169], v[166:167] op_sel_hi:[1,0]\n"
    "v_cvt_pk_bf16_f32 v117, v168, v169\n"
    "v_pk_add_f32 v[168:169], v[156:157], v[160:161] op_sel_hi:[1,0] neg_lo:[0,1] neg_hi:[0,1]\n"
    "v_cvt_pk_bf16_f32 v169, v168, v169\n"
    "v_lshlrev_b32 v168, 16, v169\n"
    "v_and_b32 v169, 0xffff0000, v169\n"
    "v_pk_mul_f32 v[168:169], v[168:169], v[166:167] op_sel_hi:[1,0]\n"
    "v_cvt_pk_bf16_f32 v118, v168, v169\n"
    "v_pk_add_f32 v[168:169], v[158:159], v[160:161] op_sel_hi:[1,0] neg_lo:[0,1] neg_hi:[0,1]\n"
    "v_cvt_pk_bf16_f32 v169, v168, v169\n"
    "v_lshlrev_b32 v168, 16, v169\n"
    "v_and_b32 v169, 0xffff0000, v169\n"
    "v_pk_mul_f32 v[168:169], v[168:169], v[166:167] op_sel_hi:[1,0]\n"
    "v_cvt_pk_bf16_f32 v119, v168, v169\n"
    "global_store_dwordx4 %[vxo], v[116:119], s[62:63] offset:0 sc1\n"
    "s_add_u32 s62, s62, 4096\n"
    "s_addc_u32 s63, s63, 0\n"
    "Vl1_%=:\n"
    "s_waitcnt vmcnt(20)\n"
    "s_waitcnt lgkmcnt(0)\n"
    "s_barrier\n"
    "s_add_u32 m0, s60, 98304\n"
    "s_nop 0\n"
    "global_load_lds_dwordx4 %[vw], s[44:45] offset:0 nt\n"
    "global_load_lds_dwordx4 v90, s[44:45] offset:1024 nt\n"
    "s_add_u32 s44, s44, 262144\n"
    "s_addc_u32 s45, s45, 0\n"
    "s_add_u32 m0, s60, 106496\n"
    "s_nop 0\n"
    "global_load_lds_dwordx4 %[vw], s[44:45] offset:0 nt\n"
    "global_load_lds_dwordx4 v90, s[44:45] offset:1024 nt\n"
    "s_add_u32 s44, s44, 262144\n"
    "s_addc_u32 s45, s45, 0\n"
    "ds_read_b64_tr_b16 a[0:1], %[vrd0] offset:0\n"
    "ds_read_b64_tr_b16 a[2:3], %[vrd0] offset:1024\n"
    "ds_read_b64_tr_b16 a[80:81], %[vrd1] offset:0\n"
    "ds_read_b64_tr_b16 a[82:83], %[vrd1] offset:1024\n"
    "ds_read_b64_tr_b16 a[4:5], %[vrd0] offset:8192\n"
    "ds_read_b64_tr_b16 a[6:7], %[vrd0] offset:9216\n"
    "ds_read_b64_tr_b16 a[84:85], %[vrd1] offset:8192\n"
    "ds_read_b64_tr_b16 a[86:87], %[vrd1] offset:9216\n"
    "s_waitcnt vmcnt(20)\n"
    "s_waitcnt lgkmcnt(0)\n"
    "s_barrier\n"
    "s_add_u32 m0, s60, 0\n"
    "s_nop 0\n"
    "global_load_lds_dwordx4 %[vw], s[44:45] offset:0 nt\n"
    "global_load_lds_dwordx4 v90, s[44:45] offset:1024 nt\n"
    "s_add_u32 s44, s44, 262144\n"
    "s_addc_u32 s45, s45, 0\n"
    "s_add_u32 m0, s60, 8192\n"
    "s_nop 0\n"
    "global_load_lds_dwordx4 %[vw], s[44:45] offset:0 nt\n"
    "global_load_lds_dwordx4 v90, s[44:45] offset:1024 nt\n"
    "s_add_u32 s44, s44, 262144\n"
    "s_addc_u32 s45, s45, 0\n"
    "ds_read_b64_tr_b16 a[8:9], %[vrd0] offset:16384\n"
    "ds_read_b64_tr_b16 a[10:11], %[vrd0] offset:17408\n"
    "ds_read_b64_tr_b16 a[88:89], %[vrd1] offset:16384\n"
    "ds_read_b64_tr_b16 a[90:91], %[vrd1] offset:17408\n"
    "ds_read_b64_tr_b16 a[12:13], %[vrd0] offset:24576\n"
    "ds_read_b64_tr_b16 a[14:15], %[vrd0] offset:25600\n"
    "ds_read_b64_tr_b16 a[92:93], %[vrd1] offset:24576\n"
    "ds_read_b64_tr_b16 a[94:95], %[vrd1] offset:25600\n"
    "s_waitcnt vmcnt(20)\n"
    "s_waitcnt lgkmcnt(0)\n"
    "s_barrier\n"
    "s_add_u32 m0, s60, 16384\n"
    "s_nop 0\n"
    "global_load_lds_dwordx4 %[vw], s[44:45] offset:0 nt\n"
    "global_load_lds_dwordx4 v90, s[44:45] offset:1024 nt\n"
    "s_add_u32 s44, s44, 262144\n"
    "s_addc_u32 s45, s45, 0\n"
    "s_add_u32 m0, s60, 24576\n"
    "s_nop 0\n"
    "global_load_lds_dwordx4 %[vw], s[44:45] offset:0 nt\n"
    "global_load_lds_dwordx4 v90, s[44:45] offset:1024 nt\n"
    "s_add_u32 s44, s44, 262144\n"
    "s_addc_u32 s45, s45, 0\n"
    "ds_read_b64_tr_b16 a[16:17], %[vrd0] offset:32768\n"
    "ds_read_b64_tr_b16 a[18:19], %[vrd0] offset:33792\n"
    "ds_read_b64_tr_b16 a[96:97], %[vrd1] offset:32768\n"
    "ds_read_b64_tr_b16 a[98:99], %[vrd1] offset:33792\n"
    "ds_read_b64_tr_b16 a[20:21], %[vrd0] offset:40960\n"
    "ds_read_b64_tr_b16 a[22:23], %[vrd0] offset:41984\n"
    "ds_read_b64_tr_b16 a[100:101], %[vrd1] offset:40960\n"
    "ds_read_b64_tr_b16 a[102:103], %[vrd1] offset:41984\n"
    "s_waitcnt vmcnt(20)\n"
    "s_waitcnt lgkmcnt(0)\n"
    "s_barrier\n"
    "s_add_u32 m0, s60, 32768\n"
    "s_nop 0\n"
    "global_load_lds_dwordx4 %[vw], s[44:45] offset:0 nt\n"
    "global_load_lds_dwordx4 v90, s[44:45] offset:1024 nt\n"
    "s_add_u32 s44, s44, 262144\n"
    "s_addc_u32 s45, s45, 0\n"
    "s_add_u32 m0, s60, 40960\n"
    "s_nop 0\n"
    "global_load_lds_dwordx4 %[vw], s[44:45] offset:0 nt\n"
    "global_load_lds_dwordx4 v90, s[44:45] offset:1024 nt\n"
    "s_add_u32 s44, s44, 262144\n"
    "s_addc_u32 s45, s45, 0\n"
    "ds_read_b64_tr_b16 a[24:25], %[vrd0] offset:49152\n"
    "ds_read_b64_tr_b16 a[26:27], %[vrd0] offset:50176\n"
    "ds_read_b64_tr_b16 a[104:105], %[vrd1] offset:49152\n"
    "ds_read_b64_tr_b16 a[106:107], %[vrd1] offset:50176\n"
    "ds_read_b64_tr_b16 a[28:29], %[vrd0] offset:57344\n"
    "ds_read_b64_tr_b16 a[30:31], %[vrd0] offset:58368\n"
    "ds_read_b64_tr_b16 a[108:109], %[vrd1] offset:57344\n"
    "ds_read_b64_tr_b16 a[110:111], %[vrd1] offset:58368\n"
    "s_waitcnt vmcnt(20)\n"
    "s_waitcnt lgkmcnt(0)\n"
    "s_barrier\n"
    "ds_read_b64_tr_b16 a[32:33], v94 offset:0\n"
    "ds_read_b64_tr_b16 a[34:35], v94 offset:1024\n"
    "ds_read_b64_tr_b16 a[112:113], v95 offset:0\n"
    "ds_read_b64_tr_b16 a[114:115], v95 offset:1024\n"
    "ds_read_b64_tr_b16 a[36:37], v94 offset:8192\n"
    "ds_read_b64_tr_b16 a[38:39], v94 offset:9216\n"
    "ds_read_b64_tr_b16 a[116:117], v95 offset:8192\n"
    "ds_read_b64_tr_b16 a[118:119], v95 offset:9216\n"
    "s_waitcnt vmcnt(16)\n"
    "s_waitcnt lgkmcnt(0)\n"
    "s_barrier\n"
    "ds_read_b64_tr_b16 a[40:41], v94 offset:16384\n"
    "ds_read_b64_tr_b16 a[42:43], v94 offset:17408\n"
    "ds_read_b64_tr_b16 a[120:121], v95 offset:16384\n"
    "ds_read_b64_tr_b16 a[122:123], v95 offset:17408\n"
    "ds_read_b64_tr_b16 a[44:45], v94 offset:24576\n"
    "ds_read_b64_tr_b16 a[46:47], v94 offset:25600\n"
    "ds_read_b64_tr_b16 a[124:125], v95 offset:24576\n"
    "ds_read_b64_tr_b16 a[126:127], v95 offset:25600\n"
    "s_waitcnt vmcnt(12)\n"
    "s_waitcnt lgkmcnt(0)\n"
    "s_barrier\n"
    "s_cmp_lt_i32 %[li], 0\n"
    "s_cbranch_scc1 Vl2_%=\n"
    "s_cmp_lg_u32 %[w], 0\n"
    "s_cbranch_scc1 Vl2_%=\n"
    "v_mov_b32 v120, s64\n"
    "v_mov_b32 v121, s65\n"
    "v_mov_b32 v122, 0\n"
    "s_mov_b64 exec, 1\n"
    "global_store_dwordx2 v122, v[120:121], %[iw] offset:0 sc1\n"
    "s_mov_b64 exec, -1\n"
    "Vl2_%=:\n"
    "ds_read_b64_tr_b16 a[48:49], v94 offset:32768\n"
    "ds_read_b64_tr_b16 a[50:51], v94 offset:33792\n"
    "ds_read_b64_tr_b16 a[128:129], v95 offset:32768\n"
    "ds_read_b64_tr_b16 a[130:131], v95 offset:33792\n"
    "ds_read_b64_tr_b16 a[52:53], v94 offset:40960\n"
    "ds_read_b64_tr_b16 a[54:55], v94 offset:41984\n"
    "ds_read_b64_tr_b16 a[132:133], v95 offset:40960\n"
    "ds_read_b64_tr_b16 a[134:135], v95 offset:41984\n"
    "s_waitcnt vmcnt(8)\n"
    "s_waitcnt lgkmcnt(0)\n"
    "s_barrier\n"
    "ds_read_b64_tr_b16 a[56:57], %[vrd0] offset:0\n"
    "ds_read_b64_tr_b16 a[58:59], %[vrd0] offset:1024\n"
    "ds_read_b64_tr_b16 a[136:137], %[vrd1] offset:0\n"
    "ds_read_b64_tr_b16 a[138:139], %[vrd1] offset:1024\n"
    "ds_read_b64_tr_b16 a[60:61], %[vrd0] offset:8192\n"
    "ds_read_b64_tr_b16 a[62:63], %[vrd0] offset:9216\n"
    "ds_read_b64_tr_b16 a[140:141], %[vrd1] offset:8192\n"
    "ds_read_b64_tr_b16 a[142:143], %[vrd1] offset:9216\n"
    "s_waitcnt vmcnt(4)\n"
    "s_waitcnt lgkmcnt(0)\n"
    "s_barrier\n"
    "ds_read_b64_tr_b16 a[64:65], %[vrd0] offset:16384\n"
    "ds_read_b64_tr_b16 a[66:67], %[vrd0] offset:17408\n"
    "ds_read_b64_tr_b16 a[144:145], %[vrd1] offset:16384\n"
    "ds_read_b64_tr_b16 a[146:147], %[vrd1] offset:17408\n"
    "ds_read_b64_tr_b16 a[68:69], %[vrd0] offset:24576\n"
    "ds_read_b64_tr_b16 a[70:71], %[vrd0] offset:25600\n"
    "ds_read_b64_tr_b16 a[148:149], %[vrd1] offset:24576\n"
    "ds_read_b64_tr_b16 a[150:151], %[vrd1] offset:25600\n"
    "s_waitcnt vmcnt(0)\n"
    "s_waitcnt lgkmcnt(0)\n"
    "s_barrier\n"
    "s_mov_b32 s43, 0\n"
    "v_subrev_u32 v88, %[lb], %[vlr]\n"
    "v_lshlrev_b32 v89, 3, v88\n"
    "v_min_u32 v89, 4992, v89\n"
    "v_add_u32 v89, 2147328, v89\n"
    "s_branch Vi0b_%=\n"
    "Vi0_%=:\n"
    "s_sleep 4\n"
    "s_add_u32 s43, s43, 1\n"
    "s_cmp_gt_u32 s43, 524288\n"
    "s_cbranch_scc1 Vdead_%=\n"
    "Vi0b_%=:\n"
    "global_load_dwordx2 v[16:17], v89, %[ws] offset:0 sc1\n"
    "s_waitcnt vmcnt(0)\n"
    "v_mov_b32 v18, v17\n"
    "v_and_b32 v17, -8, v17\n"
    "v_cmp_ne_u64 vcc, s[64:65], v[16:17]\n"
    "s_cbranch_vccnz Vi0_%=\n"
    "s_mov_b32 s55, s52\n"
    "s_add_u32 m0, s55, 0\n"
    "s_nop 0\n"
    "global_load_lds_dwordx4 %[vxv], s[46:47] offset:0 sc1\n"
    "global_load_lds_dwordx4 %[vxv], s[46:47] offset:1024 sc1\n"
    "global_load_lds_dwordx4 %[vxv], s[46:47] offset:2048 sc1\n"
    "global_load_lds_dwordx4 %[vxv], s[46:47] offset:3072 sc1\n"
    "s_add_u32 s46, s46, 4096\n"
    "s_addc_u32 s47, s47, 0\n"
    "s_add_u32 m0, s55, 4096\n"
    "s_nop 0\n"
    "global_load_lds_dwordx4 %[vxv], s[46:47] offset:0 sc1\n"
    "global_load_lds_dwordx4 %[vxv], s[46:47] offset:1024 sc1\n"
    "global_load_lds_dwordx4 %[vxv], s[46:47] offset:2048 sc1\n"
    "global_load_lds_dwordx4 %[vxv], s[46:47] offset:3072 sc1\n"
    "s_add_u32 s46, s46, 4096\n"
    "s_addc_u32 s47, s47, 0\n"
    "s_add_u32 m0, s55, 8192\n"
    "s_nop 0\n"
    "global_load_lds_dwordx4 %[vxv], s[46:47] offset:0 sc1\n"
    "global_load_lds_dwordx4 %[vxv], s[46:47] offset:1024 sc1\n"
    "s_add_u32 s46, s46, 32768\n"
    "s_addc_u32 s47, s47, 0\n"
    "v_lshlrev_b32 v89, 3, v88\n"
    "v_min_u32 v89, 128, v89\n"
    "v_add_u32 v89, 2146304, v89\n"
    "s_branch Vi2b_%=\n"
    "Vi2_%=:\n"
    "s_sleep 4\n"
    "s_add_u32 s43, s43, 1\n"
    "s_cmp_gt_u32 s43, 524288\n"
    "s_cbranch_scc1 Vdead_%=\n"
    "Vi2b_%=:\n"
    "global_load_dwordx2 v[16:17], v89, %[ws] offset:0 sc1\n"
    "s_waitcnt vmcnt(0)\n"
    "v_mov_b32 v18, v17\n"
    "v_and_b32 v17, -8, v17\n"
    "v_cmp_ne_u64 vcc, s[64:65], v[16:17]\n"
    "s_cbranch_vccnz Vi2_%=\n"
    "v_readlane_b32 s42, v18, 1\n"
    "s_nop 4\n"
    "s_mov_b64 s[48:49], %[og]\n"
    "s_and_b32 s42, s42, 1\n"
    "s_xor_b32 s42, s42, 1\n"
    "s_lshl_b32 s42, s42, 13\n"
    "s_add_u32 s48, s48, s42\n"
    "s_addc_u32 s49, s49, 0\n"
    "ds_read_b64_tr_b16 a[72:73], %[vrd0] offset:32768\n"
    "ds_read_b64_tr_b16 a[74:75], %[vrd0] offset:33792\n"
    "ds_read_b64_tr_b16 a[152:153], %[vrd1] offset:32768\n"
    "ds_read_b64_tr_b16 a[154:155], %[vrd1] offset:33792\n"
    "ds_read_b64_tr_b16 a[76:77], %[vrd0] offset:40960\n"
    "ds_read_b64_tr_b16 a[78:79], %[vrd0] offset:41984\n"
    "ds_read_b64_tr_b16 a[156:157], %[vrd1] offset:40960\n"
    "ds_read_b64_tr_b16 a[158:159], %[vrd1] offset:41984\n"
    "s_waitcnt lgkmcnt(0)\n"
    "s_barrier\n"
    "s_add_u32 s55, s52, 40960\n"
    "s_add_u32 m0, s55, 0\n"
    "s_nop 0\n"
    "global_load_lds_dwordx4 %[vxv], s[46:47] offset:0 sc1\n"
    "global_load_lds_dwordx4 %[vxv], s[46:47] offset:1024 sc1\n"
    "global_load_lds_dwordx4 %[vxv], s[46:47] offset:2048 sc1\n"
    "global_load_lds_dwordx4 %[vxv], s[46:47] offset:3072 sc1\n"
    "s_add_u32 s46, s46, 4096\n"
    "s_addc_u32 s47, s47, 0\n"
    "s_add_u32 m0, s55, 4096\n"
    "s_nop 0\n"
    "global_load_lds_dwordx4 %[vxv], s[46:47] offset:0 sc1\n"
    "global_load_lds_dwordx4 %[vxv], s[46:47] offset:1024 sc1\n"
    "global_load_lds_dwordx4 %[vxv], s[46:47] offset:2048 sc1\n"
    "global_load_lds_dwordx4 %[vxv], s[46:47] offset:3072 sc1\n"
    "s_add_u32 s46, s46, 4096\n"
    "s_addc_u32 s47, s47, 0\n"
    "s_add_u32 m0, s55, 8192\n"
    "s_nop 0\n"
    "global_load_lds_dwordx4 %[vxv], s[46:47] offset:0 sc1\n"
    "global_load_lds_dwordx4 %[vxv], s[46:47] offset:1024 sc1\n"
    "s_add_u32 s46, s46, 32768\n"
    "s_addc_u32 s47, s47, 0\n"
    "s_mov_b64 s[56:57], %[fl]\n"
    "s_mov_b32 s40, 0\n"
    "s_mov_b32 s53, 0\n"
    "s_mov_b32 s54, 40960\n"
    "v_mov_b32 v88, %[vlr]\n"
    "v_add_u32 v89, 40960, %[vlr]\n"
    "v_mov_b32 v93, 0\n"
    "ds_read_b128 v[16:19], v88 offset:0\n"
    "ds_read_b128 v[20:23], v88 offset:20480\n"
    "ds_read_b128 v[24:27], v88 offset:1024\n"
    "ds_read_b128 v[28:31], v88 offset:21504\n"
    "ds_read_b128 v[32:35], v88 offset:2048\n"
    "ds_read_b128 v[36:39], v88 offset:22528\n"
    "s_waitcnt lgkmcnt(5)\n"
    "v_mfma_f32_16x16x32_bf16 v[48:51], v[16:19], a[0:3], 0\n"
    "ds_read_b128 v[40:43], v88 offset:3072\n"
    "v_mfma_f32_16x16x32_bf16 v[56:59], v[16:19], a[80:83], 0\n"
    "s_waitcnt lgkmcnt(5)\n"
    "v_mfma_f32_16x16x32_bf16 v[52:55], v[20:23], a[0:3], 0\n"
    "ds_read_b128 v[44:47], v88 offset:23552\n"
    "v_mfma_f32_16x16x32_bf16 v[60:63], v[20:23], a[80:83], 0\n"
    "s_waitcnt lgkmcnt(5)\n"
    "v_mfma_f32_16x16x32_bf16 v[48:51], v[24:27], a[4:7], v[48:51]\n"
    "ds_read_b128 v[16:19], v88 offset:4096\n"
    "v_mfma_f32_16x16x32_bf16 v[56:59], v[24:27], a[84:87], v[56:59]\n"
    "s_waitcnt lgkmcnt(5)\n"
    "v_mfma_f32_16x16x32_bf16 v[52:55], v[28:31], a[4:7], v[52:55]\n"
    "ds_read_b128 v[20:23], v88 offset:24576\n"
    "v_mfma_f32_16x16x32_bf16 v[60:63], v[28:31], a[84:87], v[60:63]\n"
    "s_waitcnt lgkmcnt(5)\n"
    "v_mfma_f32_16x16x32_bf16 v[48:51], v[32:35], a[8:11], v[48:51]\n"
    "ds_read_b128 v[24:27], v88 offset:5120\n"
    "v_mfma_f32_16x16x32_bf16 v[56:59], v[32:35], a[88:91], v[56:59]\n"
    "s_waitcnt lgkmcnt(5)\n"
    "v_mfma_f32_16x16x32_bf16 v[52:55], v[36:39], a[8:11], v[52:55]\n"
    "ds_read_b128 v[28:31], v88 offset:25600\n"
    "v_mfma_f32_16x16x32_bf16 v[60:63], v[36:39], a[88:91], v[60:63]\n"
    "s_waitcnt lgkmcnt(5)\n"
    "v_mfma_f32_16x16x32_bf16 v[48:51], v[40:43], a[12:15], v[48:51]\n"
    "ds_read_b128 v[32:35], v88 offset:6144\n"
    "v_mfma_f32_16x16x32_bf16 v[56:59], v[40:43], a[92:95], v[56:59]\n"
    "s_waitcnt lgkmcnt(5)\n"
    "v_mfma_f32_16x16x32_bf16 v[52:55], v[44:47], a[12:15], v[52:55]\n"
    "ds_read_b128 v[36:39], v88 offset:26624\n"
    "v_mfma_f32_16x16x32_bf16 v[60:63], v[44:47], a[92:95], v[60:63]\n"
    "s_waitcnt lgkmcnt(5)\n"
    "v_mfma_f32_16x16x32_bf16 v[48:51], v[16:19], a[16:19], v[48:51]\n"
    "ds_read_b128 v[40:43], v88 offset:7168\n"
    "v_mfma_f32_16x16x32_bf16 v[56:59], v[16:19], a[96:99], v[56:59]\n"
    "s_waitcnt lgkmcnt(5)\n"
    "v_mfma_f32_16x16x32_bf16 v[52:55], v[20:23], a[16:19], v[52:55]\n"
    "ds_read_b128 v[44:47], v88 offset:27648\n"
    "v_mfma_f32_16x16x32_bf16 v[60:63], v[20:23], a[96:99], v[60:63]\n"
    "s_waitcnt lgkmcnt(5)\n"
    "v_mfma_f32_16x16x32_bf16 v[48:51], v[24:27], a[20:23], v[48:51]\n"
    "ds_read_b128 v[16:19], v88 offset:8192\n"
    "v_mfma_f32_16x16x32_bf16 v[56:59], v[24:27], a[100:103], v[56:59]\n"
    "s_waitcnt lgkmcnt(5)\n"
    "v_mfma_f32_16x16x32_bf16 v[52:55], v[28:31], a[20:23], v[52:55]\n"
    "ds_read_b128 v[20:23], v88 offset:28672\n"
    "v_mfma_f32_16x16x32_bf16 v[60:63], v[28:31], a[100:103], v[60:63]\n"
    "s_waitcnt lgkmcnt(5)\n"
    "v_mfma_f32_16x16x32_bf16 v[48:51], v[32:35], a[24:27], v[48:51]\n"
    "ds_read_b128 v[24:27], v88 offset:9216\n"
    "v_mfma_f32_16x16x32_bf16 v[56:59], v[32:35], a[104:107], v[56:59]\n"
    "s_waitcnt lgkmcnt(5)\n"
    "v_mfma_f32_16x16x32_bf16 v[52:55], v[36:39], a[24:27], v[52:55]\n"
    "ds_read_b128 v[28:31], v88 offset:29696\n"
    "v_mfma_f32_16x16x32_bf16 v[60:63], v[36:39], a[104:107], v[60:63]\n"
    "s_waitcnt lgkmcnt(5)\n"
    "v_mfma_f32_16x16x32_bf16 v[48:51], v[40:43], a[28:31], v[48:51]\n"
    "ds_read_b128 v[32:35], v88 offset:10240\n"
    "v_mfma_f32_16x16x32_bf16 v[56:59], v[40:43], a[108:111], v[56:59]\n"
    "s_waitcnt lgkmcnt(5)\n"
    "v_mfma_f32_16x16x32_bf16 v[52:55], v[44:47], a[28:31], v[52:55]\n"
    "ds_read_b128 v[36:39], v88 offset:30720\n"
    "v_mfma_f32_16x16x32_bf16 v[60:63], v[44:47], a[108:111], v[60:63]\n"
    "s_waitcnt lgkmcnt(5)\n"
    "v_mfma_f32_16x16x32_bf16 v[48:51], v[16:19], a[32:35], v[48:51]\n"
    "ds_read_b128 v[40:43], v88 offset:11264\n"
    "v_mfma_f32_16x16x32_bf16 v[56:59], v[16:19], a[112:115], v[56:59]\n"
    "s_waitcnt lgkmcnt(5)\n"
    "v_mfma_f32_16x16x32_bf16 v[52:55], v[20:23], a[32:35], v[52:55]\n"
    "ds_read_b128 v[44:47], v88 offset:31744\n"
    "v_mfma_f32_16x16x32_bf16 v[60:63], v[20:23], a[112:115], v[60:63]\n"
    "s_waitcnt lgkmcnt(5)\n"
    "v_mfma_f32_16x16x32_bf16 v[48:51], v[24:27], a[36:39], v[48:51]\n"
    "ds_read_b128 v[16:19], v88 offset:12288\n"
    "v_mfma_f32_16x16x32_bf16 v[56:59], v[24:27], a[116:119], v[56:59]\n"
    "s_waitcnt lgkmcnt(5)\n"
    "v_mfma_f32_16x16x32_bf16 v[52:55], v[28:31], a[36:39], v[52:55]\n"
    "ds_read_b128 v[20:23], v88 offset:32768\n"
    "v_mfma_f32_16x16x32_bf16 v[60:63], v[28:31], a[116:119], v[60:63]\n"
    "s_waitcnt lgkmcnt(5)\n"
    "v_mfma_f32_16x16x32_bf16 v[48:51], v[32:35], a[40:43], v[48:51]\n"
    "ds_read_b128 v[24:27], v88 offset:13312\n"
    "v_mfma_f32_16x16x32_bf16 v[56:59], v[32:35], a[120:123], v[56:59]\n"
    "s_waitcnt lgkmcnt(5)\n"
    "v_mfma_f32_16x16x32_bf16 v[52:55], v[36:39], a[40:43], v[52:55]\n"
    "ds_read_b128 v[28:31], v88 offset:33792\n"
    "v_mfma_f32_16x16x32_bf16 v[60:63], v[36:39], a[120:123], v[60:63]\n"
    "s_waitcnt lgkmcnt(5)\n"
    "v_mfma_f32_16x16x32_bf16 v[48:51], v[40:43], a[44:47], v[48:51]\n"
    "ds_read_b128 v[32:35], v88 offset:14336\n"
    "v_mfma_f32_16x16x32_bf16 v[56:59], v[40:43], a[124:127], v[56:59]\n"
    "s_waitcnt lgkmcnt(5)\n"
    "v_mfma_f32_16x16x32_bf16 v[52:55], v[44:47], a[44:47], v[52:55]\n"
    "ds_read_b128 v[36:39], v88 offset:34816\n"
    "v_mfma_f32_16x16x32_bf16 v[60:63], v[44:47], a[124:127], v[60:63]\n"
    "s_waitcnt lgkmcnt(5)\n"
    "v_mfma_f32_16x16x32_bf16 v[48:51], v[16:19], a[48:51], v[48:51]\n"
    "ds_read_b128 v[40:43], v88 offset:15360\n"
    "v_mfma_f32_16x16x32_bf16 v[56:59], v[16:19], a[128:131], v[56:59]\n"
    "s_waitcnt lgkmcnt(5)\n"
    "v_mfma_f32_16x16x32_bf16 v[52:55], v[20:23], a[48:51], v[52:55]\n"
    "ds_read_b128 v[44:47], v88 offset:35840\n"
    "v_mfma_f32_16x16x32_bf16 v[60:63], v[20:23], a[128:131], v[60:63]\n"
    "s_waitcnt lgkmcnt(5)\n"
    "v_mfma_f32_16x16x32_bf16 v[48:51], v[24:27], a[52:55], v[48:51]\n"
    "ds_read_b128 v[16:19], v88 offset:16384\n"
    "v_mfma_f32_16x16x32_bf16 v[56:59], v[24:27], a[132:135], v[56:59]\n"
    "s_waitcnt lgkmcnt(5)\n"
    "v_mfma_f32_16x16x32_bf16 v[52:55], v[28:31], a[52:55], v[52:55]\n"
    "ds_read_b128 v[20:23], v88 offset:36864\n"
    "v_mfma_f32_16x16x32_bf16 v[60:63], v[28:31], a[132:135], v[60:63]\n"
    "s_waitcnt lgkmcnt(5)\n"
    "v_mfma_f32_16x16x32_bf16 v[48:51], v[32:35], a[56:59], v[48:51]\n"
    "ds_read_b128 v[24:27], v88 offset:17408\n"
    "v_mfma_f32_16x16x32_bf16 v[56:59], v[32:35], a[136:139], v[56:59]\n"
    "s_waitcnt lgkmcnt(5)\n"
    "v_mfma_f32_16x16x32_bf16 v[52:55], v[36:39], a[56:59], v[52:55]\n"
    "ds_read_b128 v[28:31], v88 offset:37888\n"
    "v_mfma_f32_16x16x32_bf16 v[60:63], v[36:39], a[136:139], v[60:63]\n"
    "s_waitcnt lgkmcnt(5)\n"
    "v_mfma_f32_16x16x32_bf16 v[48:51], v[40:43], a[60:63], v[48:51]\n"
    "ds_read_b128 v[32:35], v88 offset:18432\n"
    "v_mfma_f32_16x16x32_bf16 v[56:59], v[40:43], a[140:143], v[56:59]\n"
    "s_waitcnt lgkmcnt(5)\n"
    "v_mfma_f32_16x16x32_bf16 v[52:55], v[44:47], a[60:63], v[52:55]\n"
    "ds_read_b128 v[36:39], v88 offset:38912\n"
    "v_mfma_f32_16x16x32_bf16 v[60:63], v[44:47], a[140:143], v[60:63]\n"
    "s_waitcnt lgkmcnt(5)\n"
    "v_mfma_f32_16x16x32_bf16 v[48:51], v[16:19], a[64:67], v[48:51]\n"
    "ds_read_b128 v[40:43], v88 offset:19456\n"
    "v_mfma_f32_16x16x32_bf16 v[56:59], v[16:19], a[144:147], v[56:59]\n"
    "s_waitcnt lgkmcnt(5)\n"
    "v_mfma_f32_16x16x32_bf16 v[52:55], v[20:23], a[64:67], v[52:55]\n"
    "ds_read_b128 v[44:47], v88 offset:39936\n"
    "v_mfma_f32_16x16x32_bf16 v[60:63], v[20:23], a[144:147], v[60:63]\n"
    "s_waitcnt lgkmcnt(5)\n"
    "v_mfma_f32_16x16x32_bf16 v[48:51], v[24:27], a[68:71], v[48:51]\n"
    "s_waitcnt vmcnt(0)\n"
    "s_mov_b32 s42, 0\n"
    "s_barrier\n"
    "s_cmp_lg_u32 %[w], 0\n"
    "s_cbranch_scc1 Vnf01_%=\n"
    "v_mov_b32 v92, s42\n"
    "s_mov_b64 exec, 1\n"
    "global_store_dword v93, v92, s[56:57] offset:0 sc1\n"
    "s_mov_b64 exec, -1\n"
    "Vnf01_%=:\n"
    "s_sub_u32 s55, 122880, s53\n"
    "s_sub_u32 s55, s55, s54\n"
    "s_mov_b32 s53, s54\n"
    "s_mov_b32 s54, s55\n"
    "s_add_u32 s55, s55, s52\n"
    "ds_read_b128 v[16:19], v89 offset:0\n"
    "v_mfma_f32_16x16x32_bf16 v[56:59], v[24:27], a[148:151], v[56:59]\n"
    "s_waitcnt lgkmcnt(5)\n"
    "s_add_u32 m0, s55, 0\n"
    "v_mfma_f32_16x16x32_bf16 v[52:55], v[28:31], a[68:71], v[52:55]\n"
    "ds_read_b128 v[20:23], v89 offset:20480\n"
    "v_mfma_f32_16x16x32_bf16 v[60:63], v[28:31], a[148:151], v[60:63]\n"
    "s_waitcnt lgkmcnt(5)\n"
    "global_load_lds_dwordx4 %[vxv], s[46:47] offset:0 sc1\n"
    "v_mfma_f32_16x16x32_bf16 v[48:51], v[32:35], a[72:75], v[48:51]\n"
    "ds_read_b128 v[24:27], v89 offset:1024\n"
    "v_mfma_f32_16x16x32_bf16 v[56:59], v[32:35], a[152:155], v[56:59]\n"
    "s_waitcnt lgkmcnt(5)\n"
    "global_load_lds_dwordx4 %[vxv], s[46:47] offset:1024 sc1\n"
    "v_mfma_f32_16x16x32_bf16 v[52:55], v[36:39], a[72:75], v[52:55]\n"
    "ds_read_b128 v[28:31], v89 offset:21504\n"
    "v_mfma_f32_16x16x32_bf16 v[60:63], v[36:39], a[152:155], v[60:63]\n"
    "s_waitcnt lgkmcnt(5)\n"
    "global_load_lds_dwordx4 %[vxv], s[46:47] offset:2048 sc1\n"
    "v_mfma_f32_16x16x32_bf16 v[48:51], v[40:43], a[76:79], v[48:51]\n"
    "ds_read_b128 v[32:35], v89 offset:2048\n"
    "v_mfma_f32_16x16x32_bf16 v[56:59], v[40:43], a[156:159], v[56:59]\n"
    "s_waitcnt lgkmcnt(5)\n"
    "global_load_lds_dwordx4 %[vxv], s[46:47] offset:3072 sc1\n"
    "v_mfma_f32_16x16x32_bf16 v[52:55], v[44:47], a[76:79], v[52:55]\n"
    "ds_read_b128 v[36:39], v89 offset:22528\n"
    "v_mfma_f32_16x16x32_bf16 v[60:63], v[44:47], a[156:159], v[60:63]\n"
    "s_add_u32 s46, s46, 4096\n"
    "s_addc_u32 s47, s47, 0\n"
    "v_mov_b32 v88, v89\n"
    "v_add_u32 v89, s54, %[vlr]\n"
    "s_add_u32 s40, s40, 1\n"
    "s_nop 7\n"
    "s_add_u32 s50, s48, 4096\n"
    "s_addc_u32 s51, s49, 0\n"
    "v_cvt_pk_bf16_f32 v80, v48, v49\n"
    "v_cvt_pk_bf16_f32 v81, v50, v51\n"
    "v_cvt_pk_bf16_f32 v82, v52, v53\n"
    "v_cvt_pk_bf16_f32 v83, v54, v55\n"
    "global_store_dwordx4 %[vovo], v[80:83], s[48:49] offset:0 sc1\n"
    "v_cvt_pk_bf16_f32 v84, v56, v57\n"
    "v_cvt_pk_bf16_f32 v85, v58, v59\n"
    "v_cvt_pk_bf16_f32 v86, v60, v61\n"
    "v_cvt_pk_bf16_f32 v87, v62, v63\n"
    "global_store_dwordx4 %[vovo], v[84:87], s[48:49] offset:1024 sc1\n"
    "s_waitcnt vmcnt(0)\n"
    "s_barrier\n"
    "s_cmp_lg_u32 %[w], 0\n"
    "s_cbranch_scc1 Vloop_%=\n"
    "v_mov_b32 v92, 1\n"
    "s_mov_b64 exec, 1\n"
    "global_store_dword v93, v92, s[56:57] offset:0 sc1\n"
    "s_mov_b64 exec, -1\n"
    "Vloop_%=:\n"
    "s_waitcnt lgkmcnt(5)\n"
    "v_mfma_f32_16x16x32_bf16 v[64:67], v[16:19], a[0:3], 0\n"
    "ds_read_b128 v[40:43], v88 offset:3072\n"
    "v_mfma_f32_16x16x32_bf16 v[72:75], v[16:19], a[80:83], 0\n"
    "s_waitcnt lgkmcnt(5)\n"
    "s_add_u32 m0, s55, 4096\n"
    "v_mfma_f32_16x16x32_bf16 v[68:71], v[20:23], a[0:3], 0\n"
    "ds_read_b128 v[44:47], v88 offset:23552\n"
    "v_mfma_f32_16x16x32_bf16 v[76:79], v[20:23], a[80:83], 0\n"
    "s_waitcnt lgkmcnt(5)\n"
    "global_load_lds_dwordx4 %[vxv], s[46:47] offset:0 sc1\n"
    "v_mfma_f32_16x16x32_bf16 v[64:67], v[24:27], a[4:7], v[64:67]\n"
    "ds_read_b128 v[16:19], v88 offset:4096\n"
    "v_mfma_f32_16x16x32_bf16 v[72:75], v[24:27], a[84:87], v[72:75]\n"
    "s_waitcnt lgkmcnt(5)\n"
    "global_load_lds_dwordx4 %[vxv], s[46:47] offset:1024 sc1\n"
    "v_mfma_f32_16x16x32_bf16 v[68:71], v[28:31], a[4:7], v[68:71]\n"
    "ds_read_b128 v[20:23], v88 offset:24576\n"
    "v_mfma_f32_16x16x32_bf16 v[76:79], v[28:31], a[84:87], v[76:79]\n"
    "s_waitcnt lgkmcnt(5)\n"
    "global_load_lds_dwordx4 %[vxv], s[46:47] offset:2048 sc1\n"
    "v_mfma_f32_16x16x32_bf16 v[64:67], v[32:35], a[8:11], v[64:67]\n"
    "ds_read_b128 v[24:27], v88 offset:5120\n"
    "v_mfma_f32_16x16x32_bf16 v[72:75], v[32:35], a[88:91], v[72:75]\n"
    "s_waitcnt lgkmcnt(5)\n"
    "global_load_lds_dwordx4 %[vxv], s[46:47] offset:3072 sc1\n"
    "v_mfma_f32_16x16x32_bf16 v[68:71], v[36:39], a[8:11], v[68:71]\n"
    "ds_read_b128 v[28:31], v88 offset:25600\n"
    "v_mfma_f32_16x16x32_bf16 v[76:79], v[36:39], a[88:91], v[76:79]\n"
    "s_waitcnt lgkmcnt(5)\n"
    "s_add_u32 s46, s46, 4096\n"
    "s_addc_u32 s47, s47, 0\n"
    "v_mfma_f32_16x16x32_bf16 v[64:67], v[40:43], a[12:15], v[64:67]\n"
    "ds_read_b128 v[32:35], v88 offset:6144\n"
    "v_mfma_f32_16x16x32_bf16 v[72:75], v[40:43], a[92:95], v[72:75]\n"
    "s_waitcnt lgkmcnt(5)\n"
    "s_add_u32 m0, s55, 8192\n"
    "v_mfma_f32_16x16x32_bf16 v[68:71], v[44:47], a[12:15], v[68:71]\n"
    "ds_read_b128 v[36:39], v88 offset:26624\n"
    "v_mfma_f32_16x16x32_bf16 v[76:79], v[44:47], a[92:95], v[76:79]\n"
    "s_waitcnt lgkmcnt(5)\n"
    "global_load_lds_dwordx4 %[vxv], s[46:47] offset:0 sc1\n"
    "v_mfma_f32_16x16x32_bf16 v[64:67], v[16:19], a[16:19], v[64:67]\n"
    "ds_read_b128 v[40:43], v88 offset:7168\n"
    "v_mfma_f32_16x16x32_bf16 v[72:75], v[16:19], a[96:99], v[72:75]\n"
    "s_waitcnt lgkmcnt(5)\n"
    "global_load_lds_dwordx4 %[vxv], s[46:47] offset:1024 sc1\n"
    "v_mfma_f32_16x16x32_bf16 v[68:71], v[20:23], a[16:19], v[68:71]\n"
    "ds_read_b128 v[44:47], v88 offset:27648\n"
    "v_mfma_f32_16x16x32_bf16 v[76:79], v[20:23], a[96:99], v[76:79]\n"
    "s_waitcnt lgkmcnt(5)\n"
    "s_add_u32 s46, s46, 32768\n"
    "s_addc_u32 s47, s47, 0\n"
    "v_mfma_f32_16x16x32_bf16 v[64:67], v[24:27], a[20:23], v[64:67]\n"
    "ds_read_b128 v[16:19], v88 offset:8192\n"
    "v_mfma_f32_16x16x32_bf16 v[72:75], v[24:27], a[100:103], v[72:75]\n"
    "s_waitcnt lgkmcnt(5)\n"
    "s_add_u32 s50, s48, 4096\n"
    "s_addc_u32 s51, s49, 0\n"
    "v_cvt_pk_bf16_f32 v80, v48, v49\n"
    "v_mfma_f32_16x16x32_bf16 v[68:71], v[28:31], a[20:23], v[68:71]\n"
    "ds_read_b128 v[20:23], v88 offset:28672\n"
    "v_mfma_f32_16x16x32_bf16 v[76:79], v[28:31], a[100:103], v[76:79]\n"
    "s_waitcnt lgkmcnt(5)\n"
    "v_cvt_pk_bf16_f32 v81, v50, v51\n"
    "v_cvt_pk_bf16_f32 v82, v52, v53\n"
    "v_mfma_f32_16x16x32_bf16 v[64:67], v[32:35], a[24:27], v[64:67]\n"
    "ds_read_b128 v[24:27], v88 offset:9216\n"
    "v_mfma_f32_16x16x32_bf16 v[72:75], v[32:35], a[104:107], v[72:75]\n"
    "s_waitcnt lgkmcnt(5)\n"
    "v_cvt_pk_bf16_f32 v83, v54, v55\n"
    "global_store_dwordx4 %[vovo], v[80:83], s[48:49] offset:0 sc1\n"
    "v_mfma_f32_16x16x32_bf16 v[68:71], v[36:39], a[24:27], v[68:71]\n"
    "ds_read_b128 v[28:31], v88 offset:29696\n"
    "v_mfma_f32_16x16x32_bf16 v[76:79], v[36:39], a[104:107], v[76:79]\n"
    "s_waitcnt lgkmcnt(5)\n"
    "v_cvt_pk_bf16_f32 v84, v56, v57\n"
    "v_cvt_pk_bf16_f32 v85, v58, v59\n"
    "v_mfma_f32_16x16x32_bf16 v[64:67], v[40:43], a[28:31], v[64:67]\n"
    "ds_read_b128 v[32:35], v88 offset:10240\n"
    "v_mfma_f32_16x16x32_bf16 v[72:75], v[40:43], a[108:111], v[72:75]\n"
    "s_waitcnt lgkmcnt(5)\n"
    "v_cvt_pk_bf16_f32 v86, v60, v61\n"
    "v_cvt_pk_bf16_f32 v87, v62, v63\n"
    "v_mfma_f32_16x16x32_bf16 v[68:71], v[44:47], a[28:31], v[68:71]\n"
    "ds_read_b128 v[36:39], v88 offset:30720\n"
    "v_mfma_f32_16x16x32_bf16 v[76:79], v[44:47], a[108:111], v[76:79]\n"
    "s_waitcnt lgkmcnt(5)\n"
    "global_store_dwordx4 %[vovo], v[84:87], s[48:49] offset:1024 sc1\n"
    "s_add_u32 s48, s48, 524288\n"
    "s_addc_u32 s49, s49, 0\n"
    "s_and_b32 s41, s48, 0x180000\n"
    "s_cselect_b32 s41, 0, 8192\n"
    "s_xor_b32 s48, s48, s41\n"
    "v_mfma_f32_16x16x32_bf16 v[64:67], v[16:19], a[32:35], v[64:67]\n"
    "ds_read_b128 v[40:43], v88 offset:11264\n"
    "v_mfma_f32_16x16x32_bf16 v[72:75], v[16:19], a[112:115], v[72:75]\n"
    "s_waitcnt lgkmcnt(5)\n"
    "v_mfma_f32_16x16x32_bf16 v[68:71], v[20:23], a[32:35], v[68:71]\n"
    "ds_read_b128 v[44:47], v88 offset:31744\n"
    "v_mfma_f32_16x16x32_bf16 v[76:79], v[20:23], a[112:115], v[76:79]\n"
    "s_waitcnt lgkmcnt(5)\n"
    "v_mfma_f32_16x16x32_bf16 v[64:67], v[24:27], a[36:39], v[64:67]\n"
    "ds_read_b128 v[16:19], v88 offset:12288\n"
    "v_mfma_f32_16x16x32_bf16 v[72:75], v[24:27], a[116:119], v[72:75]\n"
    "s_waitcnt lgkmcnt(5)\n"
    "v_mfma_f32_16x16x32_bf16 v[68:71], v[28:31], a[36:39], v[68:71]\n"
    "ds_read_b128 v[20:23], v88 offset:32768\n"
    "v_mfma_f32_16x16x32_bf16 v[76:79], v[28:31], a[116:119], v[76:79]\n"
    "s_waitcnt lgkmcnt(5)\n"
    "v_mfma_f32_16x16x32_bf16 v[64:67], v[32:35], a[40:43], v[64:67]\n"
    "ds_read_b128 v[24:27], v88 offset:13312\n"
    "v_mfma_f32_16x16x32_bf16 v[72:75], v[32:35], a[120:123], v[72:75]\n"
    "s_waitcnt lgkmcnt(5)\n"
    "v_mfma_f32_16x16x32_bf16 v[68:71], v[36:39], a[40:43], v[68:71]\n"
    "ds_read_b128 v[28:31], v88 offset:33792\n"
    "v_mfma_f32_16x16x32_bf16 v[76:79], v[36:39], a[120:123], v[76:79]\n"
    "s_waitcnt lgkmcnt(5)\n"
    "v_mfma_f32_16x16x32_bf16 v[64:67], v[40:43], a[44:47], v[64:67]\n"
    "ds_read_b128 v[32:35], v88 offset:14336\n"
    "v_mfma_f32_16x16x32_bf16 v[72:75], v[40:43], a[124:127], v[72:75]\n"
    "s_waitcnt lgkmcnt(5)\n"
    "v_mfma_f32_16x16x32_bf16 v[68:71], v[44:47], a[44:47], v[68:71]\n"
    "ds_read_b128 v[36:39], v88 offset:34816\n"
    "v_mfma_f32_16x16x32_bf16 v[76:79], v[44:47], a[124:127], v[76:79]\n"
    "s_waitcnt lgkmcnt(5)\n"
    "v_mfma_f32_16x16x32_bf16 v[64:67], v[16:19], a[48:51], v[64:67]\n"
    "ds_read_b128 v[40:43], v88 offset:15360\n"
    "v_mfma_f32_16x16x32_bf16 v[72:75], v[16:19], a[128:131], v[72:75]\n"
    "s_waitcnt lgkmcnt(5)\n"
    "v_mfma_f32_16x16x32_bf16 v[68:71], v[20:23], a[48:51], v[68:71]\n"
    "ds_read_b128 v[44:47], v88 offset:35840\n"
    "v_mfma_f32_16x16x32_bf16 v[76:79], v[20:23], a[128:131], v[76:79]\n"
    "s_waitcnt lgkmcnt(5)\n"
    "v_mfma_f32_16x16x32_bf16 v[64:67], v[24:27], a[52:55], v[64:67]\n"
    "ds_read_b128 v[16:19], v88 offset:16384\n"
    "v_mfma_f32_16x16x32_bf16 v[72:75], v[24:27], a[132:135], v[72:75]\n"
    "s_waitcnt lgkmcnt(5)\n"
    "v_mfma_f32_16x16x32_bf16 v[68:71], v[28:31], a[52:55], v[68:71]\n"
    "ds_read_b128 v[20:23], v88 offset:36864\n"
    "v_mfma_f32_16x16x32_bf16 v[76:79], v[28:31], a[132:135], v[76:79]\n"
    "s_waitcnt lgkmcnt(5)\n"
    "v_mfma_f32_16x16x32_bf16 v[64:67], v[32:35], a[56:59], v[64:67]\n"
    "ds_read_b128 v[24:27], v88 offset:17408\n"
    "v_mfma_f32_16x16x32_bf16 v[72:75], v[32:35], a[136:139], v[72:75]\n"
    "s_waitcnt lgkmcnt(5)\n"
    "v_mfma_f32_16x16x32_bf16 v[68:71], v[36:39], a[56:59], v[68:71]\n"
    "ds_read_b128 v[28:31], v88 offset:37888\n"
    "v_mfma_f32_16x16x32_bf16 v[76:79], v[36:39], a[136:139], v[76:79]\n"
    "s_waitcnt lgkmcnt(5)\n"
    "v_mfma_f32_16x16x32_bf16 v[64:67], v[40:43], a[60:63], v[64:67]\n"
    "ds_read_b128 v[32:35], v88 offset:18432\n"
    "v_mfma_f32_16x16x32_bf16 v[72:75], v[40:43], a[140:143], v[72:75]\n"
    "s_waitcnt lgkmcnt(5)\n"
    "v_mfma_f32_16x16x32_bf16 v[68:71], v[44:47], a[60:63], v[68:71]\n"
    "ds_read_b128 v[36:39], v88 offset:38912\n"
    "v_mfma_f32_16x16x32_bf16 v[76:79], v[44:47], a[140:143], v[76:79]\n"
    "s_waitcnt lgkmcnt(5)\n"
    "v_mfma_f32_16x16x32_bf16 v[64:67], v[16:19], a[64:67], v[64:67]\n"
    "ds_read_b128 v[40:43], v88 offset:19456\n"
    "v_mfma_f32_16x16x32_bf16 v[72:75], v[16:19], a[144:147], v[72:75]\n"
    "s_waitcnt lgkmcnt(5)\n"
    "v_mfma_f32_16x16x32_bf16 v[68:71], v[20:23], a[64:67], v[68:71]\n"
    "ds_read_b128 v[44:47], v88 offset:39936\n"
    "v_mfma_f32_16x16x32_bf16 v[76:79], v[20:23], a[144:147], v[76:79]\n"
    "s_waitcnt lgkmcnt(5)\n"
    "v_mfma_f32_16x16x32_bf16 v[64:67], v[24:27], a[68:71], v[64:67]\n"
    "s_waitcnt vmcnt(2)\n"
    "s_sub_u32 s42, s40, 1\n"
    "s_cmp_gt_u32 s40, 4\n"
    "s_cbranch_scc1 Vnw1_%=\n"
    "s_waitcnt vmcnt(0)\n"
    "s_mov_b32 s42, s40\n"
    "Vnw1_%=:\n"
    "s_barrier\n"
    "s_cmp_lg_u32 %[w], 0\n"
    "s_cbranch_scc1 Vnf10_%=\n"
    "v_mov_b32 v92, s42\n"
    "s_mov_b64 exec, 1\n"
    "global_store_dword v93, v92, s[56:57] offset:0 sc1\n"
    "s_mov_b64 exec, -1\n"
    "Vnf10_%=:\n"
    "s_sub_u32 s55, 122880, s53\n"
    "s_sub_u32 s55, s55, s54\n"
    "s_mov_b32 s53, s54\n"
    "s_mov_b32 s54, s55\n"
    "s_add_u32 s55, s55, s52\n"
    "ds_read_b128 v[16:19], v89 offset:0\n"
    "v_mfma_f32_16x16x32_bf16 v[72:75], v[24:27], a[148:151], v[72:75]\n"
    "s_waitcnt lgkmcnt(5)\n"
    "s_add_u32 m0, s55, 0\n"
    "v_mfma_f32_16x16x32_bf16 v[68:71], v[28:31], a[68:71], v[68:71]\n"
    "ds_read_b128 v[20:23], v89 offset:20480\n"
    "v_mfma_f32_16x16x32_bf16 v[76:79], v[28:31], a[148:151], v[76:79]\n"
    "s_waitcnt lgkmcnt(5)\n"
    "global_load_lds_dwordx4 %[vxv], s[46:47] offset:0 sc1\n"
    "v_mfma_f32_16x16x32_bf16 v[64:67], v[32:35], a[72:75], v[64:67]\n"
    "ds_read_b128 v[24:27], v89 offset:1024\n"
    "v_mfma_f32_16x16x32_bf16 v[72:75], v[32:35], a[152:155], v[72:75]\n"
    "s_waitcnt lgkmcnt(5)\n"
    "global_load_lds_dwordx4 %[vxv], s[46:47] offset:1024 sc1\n"
    "v_mfma_f32_16x16x32_bf16 v[68:71], v[36:39], a[72:75], v[68:71]\n"
    "ds_read_b128 v[28:31], v89 offset:21504\n"
    "v_mfma_f32_16x16x32_bf16 v[76:79], v[36:39], a[152:155], v[76:79]\n"
    "s_waitcnt lgkmcnt(5)\n"
    "global_load_lds_dwordx4 %[vxv], s[46:47] offset:2048 sc1\n"
    "v_mfma_f32_16x16x32_bf16 v[64:67], v[40:43], a[76:79], v[64:67]\n"
    "ds_read_b128 v[32:35], v89 offset:2048\n"
    "v_mfma_f32_16x16x32_bf16 v[72:75], v[40:43], a[156:159], v[72:75]\n"
    "s_waitcnt lgkmcnt(5)\n"
    "global_load_lds_dwordx4 %[vxv], s[46:47] offset:3072 sc1\n"
    "v_mfma_f32_16x16x32_bf16 v[68:71], v[44:47], a[76:79], v[68:71]\n"
    "ds_read_b128 v[36:39], v89 offset:22528\n"
    "v_mfma_f32_16x16x32_bf16 v[76:79], v[44:47], a[156:159], v[76:79]\n"
    "s_add_u32 s46, s46, 4096\n"
    "s_addc_u32 s47, s47, 0\n"
    "v_mov_b32 v88, v89\n"
    "v_add_u32 v89, s54, %[vlr]\n"
    "s_add_u32 s40, s40, 1\n"
    "s_cmp_eq_u32 s40, 64\n"
    "s_cbranch_scc1 Vdone_%=\n"
    "s_waitcnt lgkmcnt(5)\n"
    "v_mfma_f32_16x16x32_bf16 v[48:51], v[16:19], a[0:3], 0\n"
    "ds_read_b128 v[40:43], v88 offset:3072\n"
    "v_mfma_f32_16x16x32_bf16 v[56:59], v[16:19], a[80:83], 0\n"
    "s_waitcnt lgkmcnt(5)\n"
    "s_add_u32 m0, s55, 4096\n"
    "v_mfma_f32_16x16x32_bf16 v[52:55], v[20:23], a[0:3], 0\n"
    "ds_read_b128 v[44:47], v88 offset:23552\n"
    "v_mfma_f32_16x16x32_bf16 v[60:63], v[20:23], a[80:83], 0\n"
    "s_waitcnt lgkmcnt(5)\n"
    "global_load_lds_dwordx4 %[vxv], s[46:47] offset:0 sc1\n"
    "v_mfma_f32_16x16x32_bf16 v[48:51], v[24:27], a[4:7], v[48:51]\n"
    "ds_read_b128 v[16:19], v88 offset:4096\n"
    "v_mfma_f32_16x16x32_bf16 v[56:59], v[24:27], a[84:87], v[56:59]\n"
    "s_waitcnt lgkmcnt(5)\n"
    "global_load_lds_dwordx4 %[vxv], s[46:47] offset:1024 sc1\n"
    "v_mfma_f32_16x16x32_bf16 v[52:55], v[28:31], a[4:7], v[52:55]\n"
    "ds_read_b128 v[20:23], v88 offset:24576\n"
    "v_mfma_f32_16x16x32_bf16 v[60:63], v[28:31], a[84:87], v[60:63]\n"
    "s_waitcnt lgkmcnt(5)\n"
    "global_load_lds_dwordx4 %[vxv], s[46:47] offset:2048 sc1\n"
    "v_mfma_f32_16x16x32_bf16 v[48:51], v[32:35], a[8:11], v[48:51]\n"
    "ds_read_b128 v[24:27], v88 offset:5120\n"
    "v_mfma_f32_16x16x32_bf16 v[56:59], v[32:35], a[88:91], v[56:59]\n"
    "s_waitcnt lgkmcnt(5)\n"
    "global_load_lds_dwordx4 %[vxv], s[46:47] offset:3072 sc1\n"
    "v_mfma_f32_16x16x32_bf16 v[52:55], v[36:39], a[8:11], v[52:55]\n"
    "ds_read_b128 v[28:31], v88 offset:25600\n"
    "v_mfma_f32_16x16x32_bf16 v[60:63], v[36:39], a[88:91], v[60:63]\n"
    "s_waitcnt lgkmcnt(5)\n"
    "s_add_u32 s46, s46, 4096\n"
    "s_addc_u32 s47, s47, 0\n"
    "v_mfma_f32_16x16x32_bf16 v[48:51], v[40:43], a[12:15], v[48:51]\n"
    "ds_read_b128 v[32:35], v88 offset:6144\n"
    "v_mfma_f32_16x16x32_bf16 v[56:59], v[40:43], a[92:95], v[56:59]\n"
    "s_waitcnt lgkmcnt(5)\n"
    "s_add_u32 m0, s55, 8192\n"
    "v_mfma_f32_16x16x32_bf16 v[52:55], v[44:47], a[12:15], v[52:55]\n"
    "ds_read_b128 v[36:39], v88 offset:26624\n"
    "v_mfma_f32_16x16x32_bf16 v[60:63], v[44:47], a[92:95], v[60:63]\n"
    "s_waitcnt lgkmcnt(5)\n"
    "global_load_lds_dwordx4 %[vxv], s[46:47] offset:0 sc1\n"
    "v_mfma_f32_16x16x32_bf16 v[48:51], v[16:19], a[16:19], v[48:51]\n"
    "ds_read_b128 v[40:43], v88 offset:7168\n"
    "v_mfma_f32_16x16x32_bf16 v[56:59], v[16:19], a[96:99], v[56:59]\n"
    "s_waitcnt lgkmcnt(5)\n"
    "global_load_lds_dwordx4 %[vxv], s[46:47] offset:1024 sc1\n"
    "v_mfma_f32_16x16x32_bf16 v[52:55], v[20:23], a[16:19], v[52:55]\n"
    "ds_read_b128 v[44:47], v88 offset:27648\n"
    "v_mfma_f32_16x16x32_bf16 v[60:63], v[20:23], a[96:99], v[60:63]\n"
    "s_waitcnt lgkmcnt(5)\n"
    "s_add_u32 s46, s46, 32768\n"
    "s_addc_u32 s47, s47, 0\n"
    "v_mfma_f32_16x16x32_bf16 v[48:51], v[24:27], a[20:23], v[48:51]\n"
    "ds_read_b128 v[16:19], v88 offset:8192\n"
    "v_mfma_f32_16x16x32_bf16 v[56:59], v[24:27], a[100:103], v[56:59]\n"
    "s_waitcnt lgkmcnt(5)\n"
    "s_add_u32 s50, s48, 4096\n"
    "s_addc_u32 s51, s49, 0\n"
    "v_cvt_pk_bf16_f32 v80, v64, v65\n"
    "v_mfma_f32_16x16x32_bf16 v[52:55], v[28:31], a[20:23], v[52:55]\n"
    "ds_read_b128 v[20:23], v88 offset:28672\n"
    "v_mfma_f32_16x16x32_bf16 v[60:63], v[28:31], a[100:103], v[60:63]\n"
    "s_waitcnt lgkmcnt(5)\n"
    "v_cvt_pk_bf16_f32 v81, v66, v67\n"
    "v_cvt_pk_bf16_f32 v82, v68, v69\n"
    "v_mfma_f32_16x16x32_bf16 v[48:51], v[32:35], a[24:27], v[48:51]\n"
    "ds_read_b128 v[24:27], v88 offset:9216\n"
    "v_mfma_f32_16x16x32_bf16 v[56:59], v[32:35], a[104:107], v[56:59]\n"
    "s_waitcnt lgkmcnt(5)\n"
    "v_cvt_pk_bf16_f32 v83, v70, v71\n"
    "global_store_dwordx4 %[vovo], v[80:83], s[48:49] offset:0 sc1\n"
    "v_mfma_f32_16x16x32_bf16 v[52:55], v[36:39], a[24:27], v[52:55]\n"
    "ds_read_b128 v[28:31], v88 offset:29696\n"
    "v_mfma_f32_16x16x32_bf16 v[60:63], v[36:39], a[104:107], v[60:63]\n"
    "s_waitcnt lgkmcnt(5)\n"
    "v_cvt_pk_bf16_f32 v84, v72, v73\n"
    "v_cvt_pk_bf16_f32 v85, v74, v75\n"
    "v_mfma_f32_16x16x32_bf16 v[48:51], v[40:43], a[28:31], v[48:51]\n"
    "ds_read_b128 v[32:35], v88 offset:10240\n"
    "v_mfma_f32_16x16x32_bf16 v[56:59], v[40:43], a[108:111], v[56:59]\n"
    "s_waitcnt lgkmcnt(5)\n"
    "v_cvt_pk_bf16_f32 v86, v76, v77\n"
    "v_cvt_pk_bf16_f32 v87, v78, v79\n"
    "v_mfma_f32_16x16x32_bf16 v[52:55], v[44:47], a[28:31], v[52:55]\n"
    "ds_read_b128 v[36:39], v88 offset:30720\n"
    "v_mfma_f32_16x16x32_bf16 v[60:63], v[44:47], a[108:111], v[60:63]\n"
    "s_waitcnt lgkmcnt(5)\n"
    "global_store_dwordx4 %[vovo], v[84:87], s[48:49] offset:1024 sc1\n"
    "s_add_u32 s48, s48, 524288\n"
    "s_addc_u32 s49, s49, 0\n"
    "s_and_b32 s41, s48, 0x180000\n"
    "s_cselect_b32 s41, 0, 8192\n"
    "s_xor_b32 s48, s48, s41\n"
    "v_mfma_f32_16x16x32_bf16 v[48:51], v[16:19], a[32:35], v[48:51]\n"
    "ds_read_b128 v[40:43], v88 offset:11264\n"
    "v_mfma_f32_16x16x32_bf16 v[56:59], v[16:19], a[112:115], v[56:59]\n"
    "s_waitcnt lgkmcnt(5)\n"
    "v_mfma_f32_16x16x32_bf16 v[52:55], v[20:23], a[32:35], v[52:55]\n"
    "ds_read_b128 v[44:47], v88 offset:31744\n"
    "v_mfma_f32_16x16x32_bf16 v[60:63], v[20:23], a[112:115], v[60:63]\n"
    "s_waitcnt lgkmcnt(5)\n"
    "v_mfma_f32_16x16x32_bf16 v[48:51], v[24:27], a[36:39], v[48:51]\n"
    "ds_read_b128 v[16:19], v88 offset:12288\n"
    "v_mfma_f32_16x16x32_bf16 v[56:59], v[24:27], a[116:119], v[56:59]\n"
    "s_waitcnt lgkmcnt(5)\n"
    "v_mfma_f32_16x16x32_bf16 v[52:55], v[28:31], a[36:39], v[52:55]\n"
    "ds_read_b128 v[20:23], v88 offset:32768\n"
    "v_mfma_f32_16x16x32_bf16 v[60:63], v[28:31], a[116:119], v[60:63]\n"
    "s_waitcnt lgkmcnt(5)\n"
    "v_mfma_f32_16x16x32_bf16 v[48:51], v[32:35], a[40:43], v[48:51]\n"
    "ds_read_b128 v[24:27], v88 offset:13312\n"
    "v_mfma_f32_16x16x32_bf16 v[56:59], v[32:35], a[120:123], v[56:59]\n"
    "s_waitcnt lgkmcnt(5)\n"
    "v_mfma_f32_16x16x32_bf16 v[52:55], v[36:39], a[40:43], v[52:55]\n"
    "ds_read_b128 v[28:31], v88 offset:33792\n"
    "v_mfma_f32_16x16x32_bf16 v[60:63], v[36:39], a[120:123], v[60:63]\n"
    "s_waitcnt lgkmcnt(5)\n"
    "v_mfma_f32_16x16x32_bf16 v[48:51], v[40:43], a[44:47], v[48:51]\n"
    "ds_read_b128 v[32:35], v88 offset:14336\n"
    "v_mfma_f32_16x16x32_bf16 v[56:59], v[40:43], a[124:127], v[56:59]\n"
    "s_waitcnt lgkmcnt(5)\n"
    "v_mfma_f32_16x16x32_bf16 v[52:55], v[44:47], a[44:47], v[52:55]\n"
    "ds_read_b128 v[36:39], v88 offset:34816\n"
    "v_mfma_f32_16x16x32_bf16 v[60:63], v[44:47], a[124:127], v[60:63]\n"
    "s_waitcnt lgkmcnt(5)\n"
    "v_mfma_f32_16x16x32_bf16 v[48:51], v[16:19], a[48:51], v[48:51]\n"
    "ds_read_b128 v[40:43], v88 offset:15360\n"
    "v_mfma_f32_16x16x32_bf16 v[56:59], v[16:19], a[128:131], v[56:59]\n"
    "s_waitcnt lgkmcnt(5)\n"
    "v_mfma_f32_16x16x32_bf16 v[52:55], v[20:23], a[48:51], v[52:55]\n"
    "ds_read_b128 v[44:47], v88 offset:35840\n"
    "v_mfma_f32_16x16x32_bf16 v[60:63], v[20:23], a[128:131], v[60:63]\n"
    "s_waitcnt lgkmcnt(5)\n"
    "v_mfma_f32_16x16x32_bf16 v[48:51], v[24:27], a[52:55], v[48:51]\n"
    "ds_read_b128 v[16:19], v88 offset:16384\n"
    "v_mfma_f32_16x16x32_bf16 v[56:59], v[24:27], a[132:135], v[56:59]\n"
    "s_waitcnt lgkmcnt(5)\n"
    "v_mfma_f32_16x16x32_bf16 v[52:55], v[28:31], a[52:55], v[52:55]\n"
    "ds_read_b128 v[20:23], v88 offset:36864\n"
    "v_mfma_f32_16x16x32_bf16 v[60:63], v[28:31], a[132:135], v[60:63]\n"
    "s_waitcnt lgkmcnt(5)\n"
    "v_mfma_f32_16x16x32_bf16 v[48:51], v[32:35], a[56:59], v[48:51]\n"
    "ds_read_b128 v[24:27], v88 offset:17408\n"
    "v_mfma_f32_16x16x32_bf16 v[56:59], v[32:35], a[136:139], v[56:59]\n"
    "s_waitcnt lgkmcnt(5)\n"
    "v_mfma_f32_16x16x32_bf16 v[52:55], v[36:39], a[56:59], v[52:55]\n"
    "ds_read_b128 v[28:31], v88 offset:37888\n"
    "v_mfma_f32_16x16x32_bf16 v[60:63], v[36:39], a[136:139], v[60:63]\n"
    "s_waitcnt lgkmcnt(5)\n"
    "v_mfma_f32_16x16x32_bf16 v[48:51], v[40:43], a[60:63], v[48:51]\n"
    "ds_read_b128 v[32:35], v88 offset:18432\n"
    "v_mfma_f32_16x16x32_bf16 v[56:59], v[40:43], a[140:143], v[56:59]\n"
    "s_waitcnt lgkmcnt(5)\n"
    "v_mfma_f32_16x16x32_bf16 v[52:55], v[44:47], a[60:63], v[52:55]\n"
    "ds_read_b128 v[36:39], v88 offset:38912\n"
    "v_mfma_f32_16x16x32_bf16 v[60:63], v[44:47], a[140:143], v[60:63]\n"
    "s_waitcnt lgkmcnt(5)\n"
    "v_mfma_f32_16x16x32_bf16 v[48:51], v[16:19], a[64:67], v[48:51]\n"
    "ds_read_b128 v[40:43], v88 offset:19456\n"
    "v_mfma_f32_16x16x32_bf16 v[56:59], v[16:19], a[144:147], v[56:59]\n"
    "s_waitcnt lgkmcnt(5)\n"
    "v_mfma_f32_16x16x32_bf16 v[52:55], v[20:23], a[64:67], v[52:55]\n"
    "ds_read_b128 v[44:47], v88 offset:39936\n"
    "v_mfma_f32_16x16x32_bf16 v[60:63], v[20:23], a[144:147], v[60:63]\n"
    "s_waitcnt lgkmcnt(5)\n"
    "v_mfma_f32_16x16x32_bf16 v[48:51], v[24:27], a[68:71], v[48:51]\n"
    "s_waitcnt vmcnt(2)\n"
    "s_sub_u32 s42, s40, 1\n"
    "s_cmp_gt_u32 s40, 4\n"
    "s_cbranch_scc1 Vnw0_%=\n"
    "s_waitcnt vmcnt(0)\n"
    "s_mov_b32 s42, s40\n"
    "Vnw0_%=:\n"
    "s_barrier\n"
    "s_cmp_lg_u32 %[w], 0\n"
    "s_cbranch_scc1 Vnf00_%=\n"
    "v_mov_b32 v92, s42\n"
    "s_mov_b64 exec, 1\n"
    "global_store_dword v93, v92, s[56:57] offset:0 sc1\n"
    "s_mov_b64 exec, -1\n"
    "Vnf00_%=:\n"
    "s_sub_u32 s55, 122880, s53\n"
    "s_sub_u32 s55, s55, s54\n"
    "s_mov_b32 s53, s54\n"
    "s_mov_b32 s54, s55\n"
    "s_add_u32 s55, s55, s52\n"
    "ds_read_b128 v[16:19], v89 offset:0\n"
    "v_mfma_f32_16x16x32_bf16 v[56:59], v[24:27], a[148:151], v[56:59]\n"
    "s_waitcnt lgkmcnt(5)\n"
    "s_add_u32 m0, s55, 0\n"
    "v_mfma_f32_16x16x32_bf16 v[52:55], v[28:31], a[68:71], v[52:55]\n"
    "ds_read_b128 v[20:23], v89 offset:20480\n"
    "v_mfma_f32_16x16x32_bf16 v[60:63], v[28:31], a[148:151], v[60:63]\n"
    "s_waitcnt lgkmcnt(5)\n"
    "global_load_lds_dwordx4 %[vxv], s[46:47] offset:0 sc1\n"
    "v_mfma_f32_16x16x32_bf16 v[48:51], v[32:35], a[72:75], v[48:51]\n"
    "ds_read_b128 v[24:27], v89 offset:1024\n"
    "v_mfma_f32_16x16x32_bf16 v[56:59], v[32:35], a[152:155], v[56:59]\n"
    "s_waitcnt lgkmcnt(5)\n"
    "global_load_lds_dwordx4 %[vxv], s[46:47] offset:1024 sc1\n"
    "v_mfma_f32_16x16x32_bf16 v[52:55], v[36:39], a[72:75], v[52:55]\n"
    "ds_read_b128 v[28:31], v89 offset:21504\n"
    "v_mfma_f32_16x16x32_bf16 v[60:63], v[36:39], a[152:155], v[60:63]\n"
    "s_waitcnt lgkmcnt(5)\n"
    "global_load_lds_dwordx4 %[vxv], s[46:47] offset:2048 sc1\n"
    "v_mfma_f32_16x16x32_bf16 v[48:51], v[40:43], a[76:79], v[48:51]\n"
    "ds_read_b128 v[32:35], v89 offset:2048\n"
    "v_mfma_f32_16x16x32_bf16 v[56:59], v[40:43], a[156:159], v[56:59]\n"
    "s_waitcnt lgkmcnt(5)\n"
    "global_load_lds_dwordx4 %[vxv], s[46:47] offset:3072 sc1\n"
    "v_mfma_f32_16x16x32_bf16 v[52:55], v[44:47], a[76:79], v[52:55]\n"
    "ds_read_b128 v[36:39], v89 offset:22528\n"
    "v_mfma_f32_16x16x32_bf16 v[60:63], v[44:47], a[156:159], v[60:63]\n"
    "s_add_u32 s46, s46, 4096\n"
    "s_addc_u32 s47, s47, 0\n"
    "v_mov_b32 v88, v89\n"
    "v_add_u32 v89, s54, %[vlr]\n"
    "s_add_u32 s40, s40, 1\n"
    "s_cmp_lg_u32 s40, 9\n"
    "s_cbranch_scc1 Vloop_%=\n"
    "Vi3_%=:\n"
    "v_subrev_u32 v104, %[lb], %[vlr]\n"
    "s_cmp_lg_u32 %[w], 1\n"
    "s_cbranch_scc1 Vi4_%=\n"
    "v_lshlrev_b32 v105, 3, v104\n"
    "v_min_u32 v105, 5504, v105\n"
    "v_add_u32 v105, 2152448, v105\n"
    "s_branch Vi1b_%=\n"
    "Vi1_%=:\n"
    "s_sleep 4\n"
    "s_add_u32 s43, s43, 1\n"
    "s_cmp_gt_u32 s43, 524288\n"
    "s_cbranch_scc1 Vdead_%=\n"
    "Vi1b_%=:\n"
    "global_load_dwordx2 v[100:101], v105, %[ws] offset:0 sc1\n"
    "s_waitcnt vmcnt(0)\n"
    "v_mov_b32 v102, v101\n"
    "v_and_b32 v101, -8, v101\n"
    "v_cmp_ne_u64 vcc, s[64:65], v[100:101]\n"
    "s_cbranch_vccnz Vi1_%=\n"
    "Vi4_%=:\n"
    "s_cmp_lg_u32 %[w], 2\n"
    "s_cbranch_scc1 Vloop_%=\n"
    "v_lshlrev_b32 v105, 3, v104\n"
    "v_min_u32 v105, 5504, v105\n"
    "v_add_u32 v105, 2158080, v105\n"
    "s_branch Vi5b_%=\n"
    "Vi5_%=:\n"
    "s_sleep 4\n"
    "s_add_u32 s43, s43, 1\n"
    "s_cmp_gt_u32 s43, 524288\n"
    "s_cbranch_scc1 Vdead_%=\n"
    "Vi5b_%=:\n"
    "global_load_dwordx2 v[100:101], v105, %[ws] offset:0 sc1\n"
    "s_waitcnt vmcnt(0)\n"
    "v_mov_b32 v102, v101\n"
    "v_and_b32 v101, -8, v101\n"
    "v_cmp_ne_u64 vcc, s[64:65], v[100:101]\n"
    "s_cbranch_vccnz Vi5_%=\n"
    "s_branch Vloop_%=\n"
    "Vdone_%=:\n"
    "s_waitcnt lgkmcnt(0)\n"
    "s_nop 7\n"
    "s_add_u32 s50, s48, 4096\n"
    "s_addc_u32 s51, s49, 0\n"
    "v_cvt_pk_bf16_f32 v80, v64, v65\n"
    "v_cvt_pk_bf16_f32 v81, v66, v67\n"
    "v_cvt_pk_bf16_f32 v82, v68, v69\n"
    "v_cvt_pk_bf16_f32 v83, v70, v71\n"
    "global_store_dwordx4 %[vovo], v[80:83], s[48:49] offset:0 sc1\n"
    "v_cvt_pk_bf16_f32 v84, v72, v73\n"
    "v_cvt_pk_bf16_f32 v85, v74, v75\n"
    "v_cvt_pk_bf16_f32 v86, v76, v77\n"
    "v_cvt_pk_bf16_f32 v87, v78, v79\n"
    "global_store_dwordx4 %[vovo], v[84:87], s[48:49] offset:1024 sc1\n"
    "s_add_u32 s48, s48, 524288\n"
    "s_addc_u32 s49, s49, 0\n"
    "s_and_b32 s41, s48, 0x180000\n"
    "s_cselect_b32 s41, 0, 8192\n"
    "s_xor_b32 s48, s48, s41\n"
    "s_waitcnt vmcnt(0)\n"
    "s_barrier\n"
    "s_cmp_lg_u32 %[w], 0\n"
    "s_cbranch_scc1 Vend_%=\n"
    "v_mov_b32 v92, 64\n"
    "s_mov_b64 exec, 1\n"
    "global_store_dword v93, v92, s[56:57] offset:0 sc1\n"
    "s_mov_b64 exec, -1\n"
    "s_branch Vend_%=\n"
    "Vdead_%=:\n"
    "v_mov_b32 v254, 1024\n"
    "v_mov_b32 v255, 2098432\n"
    "global_store_dword v255, v254, %[ws] offset:0 sc1\n"
    "s_waitcnt vmcnt(0)\n"
    "s_endpgm\n"
    "Vend_%=:\n"
    "s_mov_b32 m0, s99\n"
    QKV_OPS);
  } else {
    asm volatile(
    "s_mov_b32 s99, m0\n"
    "s_mul_i32 s52, %[w], 10240\n"
    "s_add_u32 s52, s52, %[lb]\n"
    "s_mov_b64 s[46:47], %[ws]\n"
    "s_add_u32 s46, s46, 3145728\n"
    "s_addc_u32 s47, s47, 0\n"
    "s_lshl_b32 s60, %[w], 11\n"
    "s_add_u32 s60, s60, %[lb]\n"
    "s_add_u32 s60, s60, 40960\n"
    "v_add_u32 v90, 31744, %[vw]\n"
    "s_cmp_lt_i32 %[li], 0\n"
    "s_cbranch_scc1 Ql0_%=\n"
    "global_load_dwordx4 v[100:103], %[vxi], %[lx] offset:0\n"
    "global_load_dwordx4 v[104:107], %[vxi], %[lx] offset:256\n"
    "global_load_dwordx4 v[108:111], %[vxi], %[lx] offset:512\n"
    "global_load_dwordx4 v[112:115], %[vxi], %[lx] offset:768\n"
    "global_load_dwordx4 v[116:119], %[vxi], %[lx] offset:1024\n"
    "Ql0_%=:\n"
    "s_mov_b32 s64, 2097152\n"
    "s_load_dwordx2 s[64:65], %[ws], s64\n"
    "v_add_u32 v94, 65536, %[vrd0]\n"
    "v_add_u32 v95, 65536, %[vrd1]\n"
    "s_mov_b64 s[44:45], %[wp]\n"
    "s_add_u32 m0, s60, 0\n"
    "s_nop 0\n"
    "global_load_lds_dwordx4 %[vw], s[44:45] offset:0 nt\n"
    "global_load_lds_dwordx4 v90, s[44:45] offset:1024 nt\n"
    "s_add_u32 s44, s44, 262144\n"
    "s_addc_u32 s45, s45, 0\n"
    "s_add_u32 m0, s60, 8192\n"
    "s_nop 0\n"
    "global_load_lds_dwordx4 %[vw], s[44:45] offset:0 nt\n"
    "global_load_lds_dwordx4 v90, s[44:45] offset:1024 nt\n"
    "s_add_u32 s44, s44, 262144\n"
    "s_addc_u32 s45, s45, 0\n"
    "s_add_u32 m0, s60, 16384\n"
    "s_nop 0\n"
    "global_load_lds_dwordx4 %[vw], s[44:45] offset:0 nt\n"
    "global_load_lds_dwordx4 v90, s[44:45] offset:1024 nt\n"
    "s_add_u32 s44, s44, 262144\n"
    "s_addc_u32 s45, s45, 0\n"
    "s_add_u32 m0, s60, 24576\n"
    "s_nop 0\n"
    "global_load_lds_dwordx4 %[vw], s[44:45] offset:0 nt\n"
    "global_load_lds_dwordx4 v90, s[44:45] offset:1024 nt\n"
    "s_add_u32 s44, s44, 262144\n"
    "s_addc_u32 s45, s45, 0\n"
    "s_add_u32 m0, s60, 32768\n"
    "s_nop 0\n"
    "global_load_lds_dwordx4 %[vw], s[44:45] offset:0 nt\n"
    "global_load_lds_dwordx4 v90, s[44:45] offset:1024 nt\n"
    "s_add_u32 s44, s44, 262144\n"
    "s_addc_u32 s45, s45, 0\n"
    "s_add_u32 m0, s60, 40960\n"
    "s_nop 0\n"
    "global_load_lds_dwordx4 %[vw], s[44:45] offset:0 nt\n"
    "global_load_lds_dwordx4 v90, s[44:45] offset:1024 nt\n"
    "s_add_u32 s44, s44, 262144\n"
    "s_addc_u32 s45, s45, 0\n"
    "s_add_u32 m0, s60, 49152\n"
    "s_nop 0\n"
    "global_load_lds_dwordx4 %[vw], s[44:45] offset:0 nt\n"
    "global_load_lds_dwordx4 v90, s[44:45] offset:1024 nt\n"
    "s_add_u32 s44, s44, 262144\n"
    "s_addc_u32 s45, s45, 0\n"
    "s_add_u32 m0, s60, 57344\n"
    "s_nop 0\n"
    "global_load_lds_dwordx4 %[vw], s[44:45] offset:0 nt\n"
    "global_load_lds_dwordx4 v90, s[44:45] offset:1024 nt\n"
    "s_add_u32 s44, s44, 262144\n"
    "s_addc_u32 s45, s45, 0\n"
    "s_add_u32 m0, s60, 65536\n"
    "s_nop 0\n"
    "global_load_lds_dwordx4 %[vw], s[44:45] offset:0 nt\n"
    "global_load_lds_dwordx4 v90, s[44:45] offset:1024 nt\n"
    "s_add_u32 s44, s44, 262144\n"
    "s_addc_u32 s45, s45, 0\n"
    "s_add_u32 m0, s60, 73728\n"
    "s_nop 0\n"
    "global_load_lds_dwordx4 %[vw], s[44:45] offset:0 nt\n"
    "global_load_lds_dwordx4 v90, s[44:45] offset:1024 nt\n"
    "s_add_u32 s44, s44, 262144\n"
    "s_addc_u32 s45, s45, 0\n"
    "s_add_u32 m0, s60, 81920\n"
    "s_nop 0\n"
    "global_load_lds_dwordx4 %[vw], s[44:45] offset:0 nt\n"
    "global_load_lds_dwordx4 v90, s[44:45] offset:1024 nt\n"
    "s_add_u32 s44, s44, 262144\n"
    "s_addc_u32 s45, s45, 0\n"
    "s_add_u32 m0, s60, 90112\n"
    "s_nop 0\n"
    "global_load_lds_dwordx4 %[vw], s[44:45] offset:0 nt\n"
    "global_load_lds_dwordx4 v90, s[44:45] offset:1024 nt\n"
    "s_add_u32 s44, s44, 262144\n"
    "s_addc_u32 s45, s45, 0\n"
    "s_waitcnt lgkmcnt(0)\n"
    "s_add_u32 s64, s64, 1\n"
    "s_and_b32 s65, s65, -8\n"
    "s_cmp_lt_i32 %[li], 0\n"
    "s_cbranch_scc1 Ql1_%=\n"
    "s_waitcnt vmcnt(24)\n"
    "v_lshlrev_b32 v120, 16, v100\n"
    "v_and_b32 v121, 0xffff0000, v100\n"
    "v_lshlrev_b32 v122, 16, v101\n"
    "v_and_b32 v123, 0xffff0000, v101\n"
    "v_lshlrev_b32 v124, 16, v102\n"
    "v_and_b32 v125, 0xffff0000, v102\n"
    "v_lshlrev_b32 v126, 16, v103\n"
    "v_and_b32 v127, 0xffff0000, v103\n"
    "v_lshlrev_b32 v128, 16, v104\n"
    "v_and_b32 v129, 0xffff0000, v104\n"
    "v_lshlrev_b32 v130, 16, v105\n"
    "v_and_b32 v131, 0xffff0000, v105\n"
    "v_lshlrev_b32 v132, 16, v106\n"
    "v_and_b32 v133, 0xffff0000, v106\n"
    "v_lshlrev_b32 v134, 16, v107\n"
    "v_and_b32 v135, 0xffff0000, v107\n"
    "v_lshlrev_b32 v136, 16, v108\n"
    "v_and_b32 v137, 0xffff0000, v108\n"
    "v_lshlrev_b32 v138, 16, v109\n"
    "v_and_b32 v139, 0xffff0000, v109\n"
    "v_lshlrev_b32 v140, 16, v110\n"
    "v_and_b32 v141, 0xffff0000, v110\n"
    "v_lshlrev_b32 v142, 16, v111\n"
    "v_and_b32 v143, 0xffff0000, v111\n"
    "v_lshlrev_b32 v144, 16, v112\n"
    "v_and_b32 v145, 0xffff0000, v112\n"
    "v_lshlrev_b32 v146, 16, v113\n"
    "v_and_b32 v147, 0xffff0000, v113\n"
    "v_lshlrev_b32 v148, 16, v114\n"
    "v_and_b32 v149, 0xffff0000, v114\n"
    "v_lshlrev_b32 v150, 16, v115\n"
    "v_and_b32 v151, 0xffff0000, v115\n"
    "v_lshlrev_b32 v152, 16, v116\n"
    "v_and_b32 v153, 0xffff0000, v116\n"
    "v_lshlrev_b32 v154, 16, v117\n"
    "v_and_b32 v155, 0xffff0000, v117\n"
    "v_lshlrev_b32 v156, 16, v118\n"
    "v_and_b32 v157, 0xffff0000, v118\n"
    "v_lshlrev_b32 v158, 16, v119\n"
    "v_and_b32 v159, 0xffff0000, v119\n"
    "v_pk_add_f32 v[162:163], v[120:121], v[122:123]\n"
    "v_pk_add_f32 v[162:163], v[162:163], v[124:125]\n"
    "v_pk_add_f32 v[162:163], v[162:163], v[126:127]\n"
    "v_pk_add_f32 v[162:163], v[162:163], v[128:129]\n"
    "v_pk_add_f32 v[162:163], v[162:163], v[130:131]\n"
    "v_pk_add_f32 v[162:163], v[162:163], v[132:133]\n"
    "v_pk_add_f32 v[162:163], v[162:163], v[134:135]\n"
    "v_pk_add_f32 v[162:163], v[162:163], v[136:137]\n"
    "v_pk_add_f32 v[162:163], v[162:163], v[138:139]\n"
    "v_pk_add_f32 v[162:163], v[162:163], v[140:141]\n"
    "v_pk_add_f32 v[162:163], v[162:163], v[142:143]\n"
    "v_pk_add_f32 v[162:163], v[162:163], v[144:145]\n"
    "v_pk_add_f32 v[162:163], v[162:163], v[146:147]\n"
    "v_pk_add_f32 v[162:163], v[162:163], v[148:149]\n"
    "v_pk_add_f32 v[162:163], v[162:163], v[150:151]\n"
    "v_pk_add_f32 v[162:163], v[162:163], v[152:153]\n"
    "v_pk_add_f32 v[162:163], v[162:163], v[154:155]\n"
    "v_pk_add_f32 v[162:163], v[162:163], v[156:157]\n"
    "v_pk_add_f32 v[162:163], v[162:163], v[158:159]\n"
    "v_add_f32 v162, v162, v163\n"
    "v_mov_b32 v164, v162\n"
    "s_nop 1\n"
    "v_permlane16_swap_b32 v162, v164\n"
    "v_add_f32 v162, v162, v164\n"
    "v_mov_b32 v164, v162\n"
    "s_nop 1\n"
    "v_permlane32_swap_b32 v162, v164\n"
    "v_add_f32 v162, v162, v164\n"
    "ds_write_b32 %[vrw], v162 offset:0\n"
    "s_waitcnt lgkmcnt(0)\n"
    "s_barrier\n"
    "ds_read_b32 v170, %[vrr] offset:0\n"
    "ds_read_b32 v171, %[vrr] offset:64\n"
    "ds_read_b32 v172, %[vrr] offset:128\n"
    "ds_read_b32 v173, %[vrr] offset:192\n"
    "s_waitcnt lgkmcnt(2)\n"
    "v_add_f32 v170, v170, v171\n"
    "s_waitcnt lgkmcnt(0)\n"
    "v_add_f32 v172, v172, v173\n"
    "v_add_f32 v170, v170, v172\n"
    "v_mul_f32 v160, 0x3acccccd, v170\n"
    "v_pk_add_f32 v[168:169], v[120:121], v[160:161] op_sel_hi:[1,0] neg_lo:[0,1] neg_hi:[0,1]\n"
    "v_pk_mul_f32 v[162:163], v[168:169], v[168:169]\n"
    "v_pk_add_f32 v[168:169], v[122:123], v[160:161] op_sel_hi:[1,0] neg_lo:[0,1] neg_hi:[0,1]\n"
    "v_pk_fma_f32 v[162:163], v[168:169], v[168:169], v[162:163]\n"
    "v_pk_add_f32 v[168:169], v[124:125], v[160:161] op_sel_hi:[1,0] neg_lo:[0,1] neg_hi:[0,1]\n"
    "v_pk_fma_f32 v[162:163], v[168:169], v[168:169], v[162:163]\n"
    "v_pk_add_f32 v[168:169], v[126:127], v[160:161] op_sel_hi:[1,0] neg_lo:[0,1] neg_hi:[0,1]\n"
    "v_pk_fma_f32 v[162:163], v[168:169], v[168:169], v[162:163]\n"
    "v_pk_add_f32 v[168:169], v[128:129], v[160:161] op_sel_hi:[1,0] neg_lo:[0,1] neg_hi:[0,1]\n"
    "v_pk_fma_f32 v[162:163], v[168:169], v[168:169], v[162:163]\n"
    "v_pk_add_f32 v[168:169], v[130:131], v[160:161] op_sel_hi:[1,0] neg_lo:[0,1] neg_hi:[0,1]\n"
    "v_pk_fma_f32 v[162:163], v[168:169], v[168:169], v[162:163]\n"
    "v_pk_add_f32 v[168:169], v[132:133], v[160:161] op_sel_hi:[1,0] neg_lo:[0,1] neg_hi:[0,1]\n"
    "v_pk_fma_f32 v[162:163], v[168:169], v[168:169], v[162:163]\n"
    "v_pk_add_f32 v[168:169], v[134:135], v[160:161] op_sel_hi:[1,0] neg_lo:[0,1] neg_hi:[0,1]\n"
    "v_pk_fma_f32 v[162:163], v[168:169], v[168:169], v[162:163]\n"
    "v_pk_add_f32 v[168:169], v[136:137], v[160:161] op_sel_hi:[1,0] neg_lo:[0,1] neg_hi:[0,1]\n"
    "v_pk_fma_f32 v[162:163], v[168:169], v[168:169], v[162:163]\n"
    "v_pk_add_f32 v[168:169], v[138:139], v[160:161] op_sel_hi:[1,0] neg_lo:[0,1] neg_hi:[0,1]\n"
    "v_pk_fma_f32 v[162:163], v[168:169], v[168:169], v[162:163]\n"
    "v_pk_add_f32 v[168:169], v[140:141], v[160:161] op_sel_hi:[1,0] neg_lo:[0,1] neg_hi:[0,1]\n"
    "v_pk_fma_f32 v[162:163], v[168:169], v[168:169], v[162:163]\n"
    "v_pk_add_f32 v[168:169], v[142:143], v[160:161] op_sel_hi:[1,0] neg_lo:[0,1] neg_hi:[0,1]\n"
    "v_pk_fma_f32 v[162:163], v[168:169], v[168:169], v[162:163]\n"
    "v_pk_add_f32 v[168:169], v[144:145], v[160:161] op_sel_hi:[1,0] neg_lo:[0,1] neg_hi:[0,1]\n"
    "v_pk_fma_f32 v[162:163], v[168:169], v[168:169], v[162:163]\n"
    "v_pk_add_f32 v[168:169], v[146:147], v[160:161] op_sel_hi:[1,0] neg_lo:[0,1] neg_hi:[0,1]\n"
    "v_pk_fma_f32 v[162:163], v[168:169], v[168:169], v[162:163]\n"
    "v_pk_add_f32 v[168:169], v[148:149], v[160:161] op_sel_hi:[1,0] neg_lo:[0,1] neg_hi:[0,1]\n"
    "v_pk_fma_f32 v[162:163], v[168:169], v[168:169], v[162:163]\n"
    "v_pk_add_f32 v[168:169], v[150:151], v[160:161] op_sel_hi:[1,0] neg_lo:[0,1] neg_hi:[0,1]\n"
    "v_pk_fma_f32 v[162:163], v[168:169], v[168:169], v[162:163]\n"
    "v_pk_add_f32 v[168:169], v[152:153], v[160:161] op_sel_hi:[1,0] neg_lo:[0,1] neg_hi:[0,1]\n"
    "v_pk_fma_f32 v[162:163], v[168:169], v[168:169], v[162:163]\n"
    "v_pk_add_f32 v[168:169], v[154:155], v[160:161] op_sel_hi:[1,0] neg_lo:[0,1] neg_hi:[0,1]\n"
    "v_pk_fma_f32 v[162:163], v[168:169], v[168:169], v[162:163]\n"
    "v_pk_add_f32 v[168:169], v[156:157], v[160:161] op_sel_hi:[1,0] neg_lo:[0,1] neg_hi:[0,1]\n"
    "v_pk_fma_f32 v[162:163], v[168:169], v[168:169], v[162:163]\n"
    "v_pk_add_f32 v[168:169], v[158:159], v[160:161] op_sel_hi:[1,0] neg_lo:[0,1] neg_hi:[0,1]\n"
    "v_pk_fma_f32 v[162:163], v[168:169], v[168:169], v[162:163]\n"
    "v_add_f32 v162, v162, v163\n"
    "v_mov_b32 v164, v162\n"
    "s_nop 1\n"
    "v_permlane16_swap_b32 v162, v164\n"
    "v_add_f32 v162, v162, v164\n"
    "v_mov_b32 v164, v162\n"
    "s_nop 1\n"
    "v_permlane32_swap_b32 v162, v164\n"
    "v_add_f32 v162, v162, v164\n"
    "ds_write_b32 %[vrw], v162 offset:256\n"
    "s_waitcnt lgkmcnt(0)\n"
    "s_barrier\n"
    "ds_read_b32 v170, %[vrr] offset:256\n"
    "ds_read_b32 v171, %[vrr] offset:320\n"
    "ds_read_b32 v172, %[vrr] offset:384\n"
    "ds_read_b32 v173, %[vrr] offset:448\n"
    "s_waitcnt lgkmcnt(2)\n"
    "v_add_f32 v170, v170, v171\n"
    "s_waitcnt lgkmcnt(0)\n"
    "v_add_f32 v172, v172, v173\n"
    "v_add_f32 v170, v170, v172\n"
    "v_mul_f32 v166, 0x3acccccd, v170\n"
    "v_cvt_pk_bf16_f32 v166, v166, v166\n"
    "v_and_b32 v166, 0xffff0000, v166\n"
    "v_add_f32 v166, 0x3727c5ac, v166\n"
    "v_cvt_pk_bf16_f32 v166, v166, v166\n"
    "v_and_b32 v166, 0xffff0000, v166\n"
    "v_sqrt_f32 v166, v166\n"
    "s_nop 0\n"
    "v_cvt_pk_bf16_f32 v166, v166, v166\n"
    "v_and_b32 v166, 0xffff0000, v166\n"
    "v_rcp_f32 v166, v166\n"
    "v_cvt_pk_bf16_f32 v160, v160, v160\n"
    "v_and_b32 v160, 0xffff0000, v160\n"
    "s_mov_b64 s[62:63], %[lo]\n"
    "v_pk_add_f32 v[168:169], v[120:121], v[160:161] op_sel_hi:[1,0] neg_lo:[0,1] neg_hi:[0,1]\n"
    "v_cvt_pk_bf16_f32 v169, v168, v169\n"
    "v_lshlrev_b32 v168, 16, v169\n"
    "v_and_b32 v169, 0xffff0000, v169\n"
    "v_pk_mul_f32 v[168:169], v[168:169], v[166:167] op_sel_hi:[1,0]\n"
    "v_cvt_pk_bf16_f32 v100, v168, v169\n"
    "v_pk_add_f32 v[168:169], v[122:123], v[160:161] op_sel_hi:[1,0] neg_lo:[0,1] neg_hi:[0,1]\n"
    "v_cvt_pk_bf16_f32 v169, v168, v169\n"
    "v_lshlrev_b32 v168, 16, v169\n"
    "v_and_b32 v169, 0xffff0000, v169\n"
    "v_pk_mul_f32 v[168:169], v[168:169], v[166:167] op_sel_hi:[1,0]\n"
    "v_cvt_pk_bf16_f32 v101, v168, v169\n"
    "v_pk_add_f32 v[168:169], v[124:125], v[160:161] op_sel_hi:[1,0] neg_lo:[0,1] neg_hi:[0,1]\n"
    "v_cvt_pk_bf16_f32 v169, v168, v169\n"
    "v_lshlrev_b32 v168, 16, v169\n"
    "v_and_b32 v169, 0xffff0000, v169\n"
    "v_pk_mul_f32 v[168:169], v[168:169], v[166:167] op_sel_hi:[1,0]\n"
    "v_cvt_pk_bf16_f32 v102, v168, v169\n"
    "v_pk_add_f32 v[168:169], v[126:127], v[160:161] op_sel_hi:[1,0] neg_lo:[0,1] neg_hi:[0,1]\n"
    "v_cvt_pk_bf16_f32 v169, v168, v169\n"
    "v_lshlrev_b32 v168, 16, v169\n"
    "v_and_b32 v169, 0xffff0000, v169\n"
    "v_pk_mul_f32 v[168:169], v[168:169], v[166:167] op_sel_hi:[1,0]\n"
    "v_cvt_pk_bf16_f32 v103, v168, v169\n"
    "global_store_dwordx4 %[vxo], v[100:103], s[62:63] offset:0 sc1\n"
    "s_add_u32 s62, s62, 4096\n"
    "s_addc_u32 s63, s63, 0\n"
    "v_pk_add_f32 v[168:169], v[128:129], v[160:161] op_sel_hi:[1,0] neg_lo:[0,1] neg_hi:[0,1]\n"
    "v_cvt_pk_bf16_f32 v169, v168, v169\n"
    "v_lshlrev_b32 v168, 16, v169\n"
    "v_and_b32 v169, 0xffff0000, v169\n"
    "v_pk_mul_f32 v[168:169], v[168:169], v[166:167] op_sel_hi:[1,0]\n"
    "v_cvt_pk_bf16_f32 v104, v168, v169\n"
    "v_pk_add_f32 v[168:169], v[130:131], v[160:161] op_sel_hi:[1,0] neg_lo:[0,1] neg_hi:[0,1]\n"
    "v_cvt_pk_bf16_f32 v169, v168, v169\n"
    "v_lshlrev_b32 v168, 16, v169\n"
    "v_and_b32 v169, 0xffff0000, v169\n"
    "v_pk_mul_f32 v[168:169], v[168:169], v[166:167] op_sel_hi:[1,0]\n"
    "v_cvt_pk_bf16_f32 v105, v168, v169\n"
    "v_pk_add_f32 v[168:169], v[132:133], v[160:161] op_sel_hi:[1,0] neg_lo:[0,1] neg_hi:[0,1]\n"
    "v_cvt_pk_bf16_f32 v169, v168, v169\n"
    "v_lshlrev_b32 v168, 16, v169\n"
    "v_and_b32 v169, 0xffff0000, v169\n"
    "v_pk_mul_f32 v[168:169], v[168:169], v[166:167] op_sel_hi:[1,0]\n"
    "v_cvt_pk_bf16_f32 v106, v168, v169\n"
    "v_pk_add_f32 v[168:169], v[134:135], v[160:161] op_sel_hi:[1,0] neg_lo:[0,1] neg_hi:[0,1]\n"
    "v_cvt_pk_bf16_f32 v169, v168, v169\n"
    "v_lshlrev_b32 v168, 16, v169\n"
    "v_and_b32 v169, 0xffff0000, v169\n"
    "v_pk_mul_f32 v[168:169], v[168:169], v[166:167] op_sel_hi:[1,0]\n"
    "v_cvt_pk_bf16_f32 v107, v168, v169\n"
    "global_store_dwordx4 %[vxo], v[104:107], s[62:63] offset:0 sc1\n"
    "s_add_u32 s62, s62, 4096\n"
    "s_addc_u32 s63, s63, 0\n"
    "v_pk_add_f32 v[168:169], v[136:137], v[160:161] op_sel_hi:[1,0] neg_lo:[0,1] neg_hi:[0,1]\n"
    "v_cvt_pk_bf16_f32 v169, v168, v169\n"
    "v_lshlrev_b32 v168, 16, v169\n"
    "v_and_b32 v169, 0xffff0000, v169\n"
    "v_pk_mul_f32 v[168:169], v[168:169], v[166:167] op_sel_hi:[1,0]\n"
    "v_cvt_pk_bf16_f32 v108, v168, v169\n"
    "v_pk_add_f32 v[168:169], v[138:139], v[160:161] op_sel_hi:[1,0] neg_lo:[0,1] neg_hi:[0,1]\n"
    "v_cvt_pk_bf16_f32 v169, v168, v169\n"
    "v_lshlrev_b32 v168, 16, v169\n"
    "v_and_b32 v169, 0xffff0000, v169\n"
    "v_pk_mul_f32 v[168:169], v[168:169], v[166:167] op_sel_hi:[1,0]\n"
    "v_cvt_pk_bf16_f32 v109, v168, v169\n"
    "v_pk_add_f32 v[168:169], v[140:141], v[160:161] op_sel_hi:[1,0] neg_lo:[0,1] neg_hi:[0,1]\n"
    "v_cvt_pk_bf16_f32 v169, v168, v169\n"
    "v_lshlrev_b32 v168, 16, v169\n"
    "v_and_b32 v169, 0xffff0000, v169\n"
    "v_pk_mul_f32 v[168:169], v[168:169], v[166:167] op_sel_hi:[1,0]\n"
    "v_cvt_pk_bf16_f32 v110, v168, v169\n"
    "v_pk_add_f32 v[168:169], v[142:143], v[160:161] op_sel_hi:[1,0] neg_lo:[0,1] neg_hi:[0,1]\n"
    "v_cvt_pk_bf16_f32 v169, v168, v169\n"
    "v_lshlrev_b32 v168, 16, v169\n"
    "v_and_b32 v169, 0xffff0000, v169\n"
    "v_pk_mul_f32 v[168:169], v[168:169], v[166:167] op_sel_hi:[1,0]\n"
    "v_cvt_pk_bf16_f32 v111, v168, v169\n"
    "global_store_dwordx4 %[vxo], v[108:111], s[62:63] offset:0 sc1\n"
    "s_add_u32 s62, s62, 4096\n"
    "s_addc_u32 s63, s63, 0\n"
    "v_pk_add_f32 v[168:169], v[144:145], v[160:161] op_sel_hi:[1,0] neg_lo:[0,1] neg_hi:[0,1]\n"
    "v_cvt_pk_bf16_f32 v169, v168, v169\n"
    "v_lshlrev_b32 v168, 16, v169\n"
    "v_and_b32 v169, 0xffff0000, v169\n"
    "v_pk_mul_f32 v[168:169], v[168:169], v[166:167] op_sel_hi:[1,0]\n"
    "v_cvt_pk_bf16_f32 v112, v168, v169\n"
    "v_pk_add_f32 v[168:169], v[146:147], v[160:161] op_sel_hi:[1,0] neg_lo:[0,1] neg_hi:[0,1]\n"
    "v_cvt_pk_bf16_f32 v169, v168, v169\n"
    "v_lshlrev_b32 v168, 16, v169\n"
    "v_and_b32 v169, 0xffff0000, v169\n"
    "v_pk_mul_f32 v[168:169], v[168:169], v[166:167] op_sel_hi:[1,0]\n"
    "v_cvt_pk_bf16_f32 v113, v168, v169\n"
    "v_pk_add_f32 v[168:169], v[148:149], v[160:161] op_sel_hi:[1,0] neg_lo:[0,1] neg_hi:[0,1]\n"
    "v_cvt_pk_bf16_f32 v169, v168, v169\n"
    "v_lshlrev_b32 v168, 16, v169\n"
    "v_and_b32 v169, 0xffff0000, v169\n"
    "v_pk_mul_f32 v[168:169], v[168:169], v[166:167] op_sel_hi:[1,0]\n"
    "v_cvt_pk_bf16_f32 v114, v168, v169\n"
    "v_pk_add_f32 v[168:169], v[150:151], v[160:161] op_sel_hi:[1,0] neg_lo:[0,1] neg_hi:[0,1]\n"
    "v_cvt_pk_bf16_f32 v169, v168, v169\n"
    "v_lshlrev_b32 v168, 16, v169\n"
    "v_and_b32 v169, 0xffff0000, v169\n"
    "v_pk_mul_f32 v[168:169], v[168:169], v[166:167] op_sel_hi:[1,0]\n"
    "v_cvt_pk_bf16_f32 v115, v168, v169\n"
    "global_store_dwordx4 %[vxo], v[112:115], s[62:63] offset:0 sc1\n"
    "s_add_u32 s62, s62, 4096\n"
    "s_addc_u32 s63, s63, 0\n"
    "v_pk_add_f32 v[168:169], v[152:153], v[160:161] op_sel_hi:[1,0] neg_lo:[0,1] neg_hi:[0,1]\n"
    "v_cvt_pk_bf16_f32 v169, v168, v169\n"
    "v_lshlrev_b32 v168, 16, v169\n"
    "v_and_b32 v169, 0xffff0000, v169\n"
    "v_pk_mul_f32 v[168:169], v[168:169], v[166:167] op_sel_hi:[1,0]\n"
    "v_cvt_pk_bf16_f32 v116, v168, v169\n"
    "v_pk_add_f32 v[168:169], v[154:155], v[160:161] op_sel_hi:[1,0] neg_lo:[0,1] neg_hi:[0,1]\n"
    "v_cvt_pk_bf16_f32 v169, v168, v169\n"
    "v_lshlrev_b32 v168, 16, v169\n"
    "v_and_b32 v169, 0xffff0000, v169\n"
    "v_pk_mul_f32 v[168:169], v[168:169], v[166:167] op_sel_hi:[1,0]\n"
    "v_cvt_pk_bf16_f32 v117, v168, v169\n"
    "v_pk_add_f32 v[168:169], v[156:157], v[160:161] op_sel_hi:[1,0] neg_lo:[0,1] neg_hi:[0,1]\n"
    "v_cvt_pk_bf16_f32 v169, v168, v169\n"
    "v_lshlrev_b32 v168, 16, v169\n"
    "v_and_b32 v169, 0xffff0000, v169\n"
    "v_pk_mul_f32 v[168:169], v[168:169], v[166:167] op_sel_hi:[1,0]\n"
    "v_cvt_pk_bf16_f32 v118, v168, v169\n"
    "v_pk_add_f32 v[168:169], v[158:159], v[160:161] op_sel_hi:[1,0] neg_lo:[0,1] neg_hi:[0,1]\n"
    "v_cvt_pk_bf16_f32 v169, v168, v169\n"
    "v_lshlrev_b32 v168, 16, v169\n"
    "v_and_b32 v169, 0xffff0000, v169\n"
    "v_pk_mul_f32 v[168:169], v[168:169], v[166:167] op_sel_hi:[1,0]\n"
    "v_cvt_pk_bf16_f32 v119, v168, v169\n"
    "global_store_dwordx4 %[vxo], v[116:119], s[62:63] offset:0 sc1\n"
    "s_add_u32 s62, s62, 4096\n"
    "s_addc_u32 s63, s63, 0\n"
    "Ql1_%=:\n"
    "s_waitcnt vmcnt(20)\n"
    "s_waitcnt lgkmcnt(0)\n"
    "s_barrier\n"
    "s_add_u32 m0, s60, 98304\n"
    "s_nop 0\n"
    "global_load_lds_dwordx4 %[vw], s[44:45] offset:0 nt\n"
    "global_load_lds_dwordx4 v90, s[44:45] offset:1024 nt\n"
    "s_add_u32 s44, s44, 262144\n"
    "s_addc_u32 s45, s45, 0\n"
    "s_add_u32 m0, s60, 106496\n"
    "s_nop 0\n"
    "global_load_lds_dwordx4 %[vw], s[44:45] offset:0 nt\n"
    "global_load_lds_dwordx4 v90, s[44:45] offset:1024 nt\n"
    "s_add_u32 s44, s44, 262144\n"
    "s_addc_u32 s45, s45, 0\n"
    "ds_read_b64_tr_b16 a[0:1], %[vrd0] offset:0\n"
    "ds_read_b64_tr_b16 a[2:3], %[vrd0] offset:1024\n"
    "ds_read_b64_tr_b16 a[80:81], %[vrd1] offset:0\n"
    "ds_read_b64_tr_b16 a[82:83], %[vrd1] offset:1024\n"
    "ds_read_b64_tr_b16 a[4:5], %[vrd0] offset:8192\n"
    "ds_read_b64_tr_b16 a[6:7], %[vrd0] offset:9216\n"
    "ds_read_b64_tr_b16 a[84:85], %[vrd1] offset:8192\n"
    "ds_read_b64_tr_b16 a[86:87], %[vrd1] offset:9216\n"
    "s_waitcnt vmcnt(20)\n"
    "s_waitcnt lgkmcnt(0)\n"
    "s_barrier\n"
    "s_add_u32 m0, s60, 0\n"
    "s_nop 0\n"
    "global_load_lds_dwordx4 %[vw], s[44:45] offset:0 nt\n"
    "global_load_lds_dwordx4 v90, s[44:45] offset:1024 nt\n"
    "s_add_u32 s44, s44, 262144\n"
    "s_addc_u32 s45, s45, 0\n"
    "s_add_u32 m0, s60, 8192\n"
    "s_nop 0\n"
    "global_load_lds_dwordx4 %[vw], s[44:45] offset:0 nt\n"
    "global_load_lds_dwordx4 v90, s[44:45] offset:1024 nt\n"
    "s_add_u32 s44, s44, 262144\n"
    "s_addc_u32 s45, s45, 0\n"
    "ds_read_b64_tr_b16 a[8:9], %[vrd0] offset:16384\n"
    "ds_read_b64_tr_b16 a[10:11], %[vrd0] offset:17408\n"
    "ds_read_b64_tr_b16 a[88:89], %[vrd1] offset:16384\n"
    "ds_read_b64_tr_b16 a[90:91], %[vrd1] offset:17408\n"
    "ds_read_b64_tr_b16 a[12:13], %[vrd0] offset:24576\n"
    "ds_read_b64_tr_b16 a[14:15], %[vrd0] offset:25600\n"
    "ds_read_b64_tr_b16 a[92:93], %[vrd1] offset:24576\n"
    "ds_read_b64_tr_b16 a[94:95], %[vrd1] offset:25600\n"
    "s_waitcnt vmcnt(20)\n"
    "s_waitcnt lgkmcnt(0)\n"
    "s_barrier\n"
    "s_add_u32 m0, s60, 16384\n"
    "s_nop 0\n"
    "global_load_lds_dwordx4 %[vw], s[44:45] offset:0 nt\n"
    "global_load_lds_dwordx4 v90, s[44:45] offset:1024 nt\n"
    "s_add_u32 s44, s44, 262144\n"
    "s_addc_u32 s45, s45, 0\n"
    "s_add_u32 m0, s60, 24576\n"
    "s_nop 0\n"
    "global_load_lds_dwordx4 %[vw], s[44:45] offset:0 nt\n"
    "global_load_lds_dwordx4 v90, s[44:45] offset:1024 nt\n"
    "s_add_u32 s44, s44, 262144\n"
    "s_addc_u32 s45, s45, 0\n"
    "ds_read_b64_tr_b16 a[16:17], %[vrd0] offset:32768\n"
    "ds_read_b64_tr_b16 a[18:19], %[vrd0] offset:33792\n"
    "ds_read_b64_tr_b16 a[96:97], %[vrd1] offset:32768\n"
    "ds_read_b64_tr_b16 a[98:99], %[vrd1] offset:33792\n"
    "ds_read_b64_tr_b16 a[20:21], %[vrd0] offset:40960\n"
    "ds_read_b64_tr_b16 a[22:23], %[vrd0] offset:41984\n"
    "ds_read_b64_tr_b16 a[100:101], %[vrd1] offset:40960\n"
    "ds_read_b64_tr_b16 a[102:103], %[vrd1] offset:41984\n"
    "s_waitcnt vmcnt(20)\n"
    "s_waitcnt lgkmcnt(0)\n"
    "s_barrier\n"
    "s_add_u32 m0, s60, 32768\n"
    "s_nop 0\n"
    "global_load_lds_dwordx4 %[vw], s[44:45] offset:0 nt\n"
    "global_load_lds_dwordx4 v90, s[44:45] offset:1024 nt\n"
    "s_add_u32 s44, s44, 262144\n"
    "s_addc_u32 s45, s45, 0\n"
    "s_add_u32 m0, s60, 40960\n"
    "s_nop 0\n"
    "global_load_lds_dwordx4 %[vw], s[44:45] offset:0 nt\n"
    "global_load_lds_dwordx4 v90, s[44:45] offset:1024 nt\n"
    "s_add_u32 s44, s44, 262144\n"
    "s_addc_u32 s45, s45, 0\n"
    "ds_read_b64_tr_b16 a[24:25], %[vrd0] offset:49152\n"
    "ds_read_b64_tr_b16 a[26:27], %[vrd0] offset:50176\n"
    "ds_read_b64_tr_b16 a[104:105], %[vrd1] offset:49152\n"
    "ds_read_b64_tr_b16 a[106:107], %[vrd1] offset:50176\n"
    "ds_read_b64_tr_b16 a[28:29], %[vrd0] offset:57344\n"
    "ds_read_b64_tr_b16 a[30:31], %[vrd0] offset:58368\n"
    "ds_read_b64_tr_b16 a[108:109], %[vrd1] offset:57344\n"
    "ds_read_b64_tr_b16 a[110:111], %[vrd1] offset:58368\n"
    "s_waitcnt vmcnt(20)\n"
    "s_waitcnt lgkmcnt(0)\n"
    "s_barrier\n"
    "ds_read_b64_tr_b16 a[32:33], v94 offset:0\n"
    "ds_read_b64_tr_b16 a[34:35], v94 offset:1024\n"
    "ds_read_b64_tr_b16 a[112:113], v95 offset:0\n"
    "ds_read_b64_tr_b16 a[114:115], v95 offset:1024\n"
    "ds_read_b64_tr_b16 a[36:37], v94 offset:8192\n"
    "ds_read_b64_tr_b16 a[38:39], v94 offset:9216\n"
    "ds_read_b64_tr_b16 a[116:117], v95 offset:8192\n"
    "ds_read_b64_tr_b16 a[118:119], v95 offset:9216\n"
    "s_waitcnt vmcnt(16)\n"
    "s_waitcnt lgkmcnt(0)\n"
    "s_barrier\n"
    "ds_read_b64_tr_b16 a[40:41], v94 offset:16384\n"
    "ds_read_b64_tr_b16 a[42:43], v94 offset:17408\n"
    "ds_read_b64_tr_b16 a[120:121], v95 offset:16384\n"
    "ds_read_b64_tr_b16 a[122:123], v95 offset:17408\n"
    "ds_read_b64_tr_b16 a[44:45], v94 offset:24576\n"
    "ds_read_b64_tr_b16 a[46:47], v94 offset:25600\n"
    "ds_read_b64_tr_b16 a[124:125], v95 offset:24576\n"
    "ds_read_b64_tr_b16 a[126:127], v95 offset:25600\n"
    "s_waitcnt vmcnt(12)\n"
    "s_waitcnt lgkmcnt(0)\n"
    "s_barrier\n"
    "s_cmp_lt_i32 %[li], 0\n"
    "s_cbranch_scc1 Ql2_%=\n"
    "s_cmp_lg_u32 %[w], 0\n"
    "s_cbranch_scc1 Ql2_%=\n"
    "v_mov_b32 v120, s64\n"
    "v_mov_b32 v121, s65\n"
    "v_mov_b32 v122, 0\n"
    "s_mov_b64 exec, 1\n"
    "global_store_dwordx2 v122, v[120:121], %[iw] offset:0 sc1\n"
    "s_mov_b64 exec, -1\n"
    "Ql2_%=:\n"
    "ds_read_b64_tr_b16 a[48:49], v94 offset:32768\n"
    "ds_read_b64_tr_b16 a[50:51], v94 offset:33792\n"
    "ds_read_b64_tr_b16 a[128:129], v95 offset:32768\n"
    "ds_read_b64_tr_b16 a[130:131], v95 offset:33792\n"
    "ds_read_b64_tr_b16 a[52:53], v94 offset:40960\n"
    "ds_read_b64_tr_b16 a[54:55], v94 offset:41984\n"
    "ds_read_b64_tr_b16 a[132:133], v95 offset:40960\n"
    "ds_read_b64_tr_b16 a[134:135], v95 offset:41984\n"
    "s_waitcnt vmcnt(8)\n"
    "s_waitcnt lgkmcnt(0)\n"
    "s_barrier\n"
    "ds_read_b64_tr_b16 a[56:57], %[vrd0] offset:0\n"
    "ds_read_b64_tr_b16 a[58:59], %[vrd0] offset:1024\n"
    "ds_read_b64_tr_b16 a[136:137], %[vrd1] offset:0\n"
    "ds_read_b64_tr_b16 a[138:139], %[vrd1] offset:1024\n"
    "ds_read_b64_tr_b16 a[60:61], %[vrd0] offset:8192\n"
    "ds_read_b64_tr_b16 a[62:63], %[vrd0] offset:9216\n"
    "ds_read_b64_tr_b16 a[140:141], %[vrd1] offset:8192\n"
    "ds_read_b64_tr_b16 a[142:143], %[vrd1] offset:9216\n"
    "s_waitcnt vmcnt(4)\n"
    "s_waitcnt lgkmcnt(0)\n"
    "s_barrier\n"
    "ds_read_b64_tr_b16 a[64:65], %[vrd0] offset:16384\n"
    "ds_read_b64_tr_b16 a[66:67], %[vrd0] offset:17408\n"
    "ds_read_b64_tr_b16 a[144:145], %[vrd1] offset:16384\n"
    "ds_read_b64_tr_b16 a[146:147], %[vrd1] offset:17408\n"
    "ds_read_b64_tr_b16 a[68:69], %[vrd0] offset:24576\n"
    "ds_read_b64_tr_b16 a[70:71], %[vrd0] offset:25600\n"
    "ds_read_b64_tr_b16 a[148:149], %[vrd1] offset:24576\n"
    "ds_read_b64_tr_b16 a[150:151], %[vrd1] offset:25600\n"
    "s_waitcnt vmcnt(0)\n"
    "s_waitcnt lgkmcnt(0)\n"
    "s_barrier\n"
    "s_mov_b32 s43, 0\n"
    "v_subrev_u32 v88, %[lb], %[vlr]\n"
    "v_lshlrev_b32 v89, 3, v88\n"
    "v_min_u32 v89, 4992, v89\n"
    "v_add_u32 v89, 2147328, v89\n"
    "s_branch Qi0b_%=\n"
    "Qi0_%=:\n"
    "s_sleep 4\n"
    "s_add_u32 s43, s43, 1\n"
    "s_cmp_gt_u32 s43, 524288\n"
    "s_cbranch_scc1 Qdead_%=\n"
    "Qi0b_%=:\n"
    "global_load_dwordx2 v[16:17], v89, %[ws] offset:0 sc1\n"
    "s_waitcnt vmcnt(0)\n"
    "v_mov_b32 v18, v17\n"
    "v_and_b32 v17, -8, v17\n"
    "v_cmp_ne_u64 vcc, s[64:65], v[16:17]\n"
    "s_cbranch_vccnz Qi0_%=\n"
    "s_mov_b32 s55, s52\n"
    "s_add_u32 m0, s55, 0\n"
    "s_nop 0\n"
    "global_load_lds_dwordx4 %[vxv], s[46:47] offset:0 sc1\n"
    "global_load_lds_dwordx4 %[vxv], s[46:47] offset:1024 sc1\n"
    "global_load_lds_dwordx4 %[vxv], s[46:47] offset:2048 sc1\n"
    "global_load_lds_dwordx4 %[vxv], s[46:47] offset:3072 sc1\n"
    "s_add_u32 s46, s46, 4096\n"
    "s_addc_u32 s47, s47, 0\n"
    "s_add_u32 m0, s55, 4096\n"
    "s_nop 0\n"
    "global_load_lds_dwordx4 %[vxv], s[46:47] offset:0 sc1\n"
    "global_load_lds_dwordx4 %[vxv], s[46:47] offset:1024 sc1\n"
    "global_load_lds_dwordx4 %[vxv], s[46:47] offset:2048 sc1\n"
    "global_load_lds_dwordx4 %[vxv], s[46:47] offset:3072 sc1\n"
    "s_add_u32 s46, s46, 4096\n"
    "s_addc_u32 s47, s47, 0\n"
    "s_add_u32 m0, s55, 8192\n"
    "s_nop 0\n"
    "global_load_lds_dwordx4 %[vxv], s[46:47] offset:0 sc1\n"
    "global_load_lds_dwordx4 %[vxv], s[46:47] offset:1024 sc1\n"
    "s_add_u32 s46, s46, 32768\n"
    "s_addc_u32 s47, s47, 0\n"
    "v_lshlrev_b32 v89, 3, v88\n"
    "v_min_u32 v89, 128, v89\n"
    "v_add_u32 v89, 2146304, v89\n"
    "s_branch Qi2b_%=\n"
    "Qi2_%=:\n"
    "s_sleep 4\n"
    "s_add_u32 s43, s43, 1\n"
    "s_cmp_gt_u32 s43, 524288\n"
    "s_cbranch_scc1 Qdead_%=\n"
    "Qi2b_%=:\n"
    "global_load_dwordx2 v[16:17], v89, %[ws] offset:0 sc1\n"
    "s_waitcnt vmcnt(0)\n"
    "v_mov_b32 v18, v17\n"
    "v_and_b32 v17, -8, v17\n"
    "v_cmp_ne_u64 vcc, s[64:65], v[16:17]\n"
    "s_cbranch_vccnz Qi2_%=\n"
    "v_readlane_b32 s42, v18, 1\n"
    "s_nop 4\n"
    "s_mov_b64 s[48:49], %[og]\n"
    "s_and_b32 s42, s42, 1\n"
    "s_xor_b32 s42, s42, 1\n"
    "s_lshl_b32 s42, s42, 13\n"
    "s_add_u32 s48, s48, s42\n"
    "s_addc_u32 s49, s49, 0\n"
    "ds_read_b64_tr_b16 a[72:73], %[vrd0] offset:32768\n"
    "ds_read_b64_tr_b16 a[74:75], %[vrd0] offset:33792\n"
    "ds_read_b64_tr_b16 a[152:153], %[vrd1] offset:32768\n"
    "ds_read_b64_tr_b16 a[154:155], %[vrd1] offset:33792\n"
    "ds_read_b64_tr_b16 a[76:77], %[vrd0] offset:40960\n"
    "ds_read_b64_tr_b16 a[78:79], %[vrd0] offset:41984\n"
    "ds_read_b64_tr_b16 a[156:157], %[vrd1] offset:40960\n"
    "ds_read_b64_tr_b16 a[158:159], %[vrd1] offset:41984\n"
    "s_waitcnt lgkmcnt(0)\n"
    "s_barrier\n"
    "s_add_u32 s55, s52, 40960\n"
    "s_add_u32 m0, s55, 0\n"
    "s_nop 0\n"
    "global_load_lds_dwordx4 %[vxv], s[46:47] offset:0 sc1\n"
    "global_load_lds_dwordx4 %[vxv], s[46:47] offset:1024 sc1\n"
    "global_load_lds_dwordx4 %[vxv], s[46:47] offset:2048 sc1\n"
    "global_load_lds_dwordx4 %[vxv], s[46:47] offset:3072 sc1\n"
    "s_add_u32 s46, s46, 4096\n"
    "s_addc_u32 s47, s47, 0\n"
    "s_add_u32 m0, s55, 4096\n"
    "s_nop 0\n"
    "global_load_lds_dwordx4 %[vxv], s[46:47] offset:0 sc1\n"
    "global_load_lds_dwordx4 %[vxv], s[46:47] offset:1024 sc1\n"
    "global_load_lds_dwordx4 %[vxv], s[46:47] offset:2048 sc1\n"
    "global_load_lds_dwordx4 %[vxv], s[46:47] offset:3072 sc1\n"
    "s_add_u32 s46, s46, 4096\n"
    "s_addc_u32 s47, s47, 0\n"
    "s_add_u32 m0, s55, 8192\n"
    "s_nop 0\n"
    "global_load_lds_dwordx4 %[vxv], s[46:47] offset:0 sc1\n"
    "global_load_lds_dwordx4 %[vxv], s[46:47] offset:1024 sc1\n"
    "s_add_u32 s46, s46, 32768\n"
    "s_addc_u32 s47, s47, 0\n"
    "s_mov_b64 s[56:57], %[fl]\n"
    "s_mov_b32 s40, 0\n"
    "s_mov_b32 s53, 0\n"
    "s_mov_b32 s54, 40960\n"
    "v_mov_b32 v88, %[vlr]\n"
    "v_add_u32 v89, 40960, %[vlr]\n"
    "v_mov_b32 v93, 0\n"
    "ds_read_b128 v[16:19], v88 offset:0\n"
    "ds_read_b128 v[20:23], v88 offset:20480\n"
    "ds_read_b128 v[24:27], v88 offset:1024\n"
    "ds_read_b128 v[28:31], v88 offset:21504\n"
    "ds_read_b128 v[32:35], v88 offset:2048\n"
    "ds_read_b128 v[36:39], v88 offset:22528\n"
    "s_waitcnt lgkmcnt(5)\n"
    "v_mfma_f32_16x16x32_bf16 v[48:51], a[0:3], v[16:19], 0\n"
    "ds_read_b128 v[40:43], v88 offset:3072\n"
    "v_mfma_f32_16x16x32_bf16 v[56:59], a[80:83], v[16:19], 0\n"
    "s_waitcnt lgkmcnt(5)\n"
    "v_mfma_f32_16x16x32_bf16 v[52:55], a[0:3], v[20:23], 0\n"
    "ds_read_b128 v[44:47], v88 offset:23552\n"
    "v_mfma_f32_16x16x32_bf16 v[60:63], a[80:83], v[20:23], 0\n"
    "s_waitcnt lgkmcnt(5)\n"
    "v_mfma_f32_16x16x32_bf16 v[48:51], a[4:7], v[24:27], v[48:51]\n"
    "ds_read_b128 v[16:19], v88 offset:4096\n"
    "v_mfma_f32_16x16x32_bf16 v[56:59], a[84:87], v[24:27], v[56:59]\n"
    "s_waitcnt lgkmcnt(5)\n"
    "v_mfma_f32_16x16x32_bf16 v[52:55], a[4:7], v[28:31], v[52:55]\n"
    "ds_read_b128 v[20:23], v88 offset:24576\n"
    "v_mfma_f32_16x16x32_bf16 v[60:63], a[84:87], v[28:31], v[60:63]\n"
    "s_waitcnt lgkmcnt(5)\n"
    "v_mfma_f32_16x16x32_bf16 v[48:51], a[8:11], v[32:35], v[48:51]\n"
    "ds_read_b128 v[24:27], v88 offset:5120\n"
    "v_mfma_f32_16x16x32_bf16 v[56:59], a[88:91], v[32:35], v[56:59]\n"
    "s_waitcnt lgkmcnt(5)\n"
    "v_mfma_f32_16x16x32_bf16 v[52:55], a[8:11], v[36:39], v[52:55]\n"
    "ds_read_b128 v[28:31], v88 offset:25600\n"
    "v_mfma_f32_16x16x32_bf16 v[60:63], a[88:91], v[36:39], v[60:63]\n"
    "s_waitcnt lgkmcnt(5)\n"
    "v_mfma_f32_16x16x32_bf16 v[48:51], a[12:15], v[40:43], v[48:51]\n"
    "ds_read_b128 v[32:35], v88 offset:6144\n"
    "v_mfma_f32_16x16x32_bf16 v[56:59], a[92:95], v[40:43], v[56:59]\n"
    "s_waitcnt lgkmcnt(5)\n"
    "v_mfma_f32_16x16x32_bf16 v[52:55], a[12:15], v[44:47], v[52:55]\n"
    "ds_read_b128 v[36:39], v88 offset:26624\n"
    "v_mfma_f32_16x16x32_bf16 v[60:63], a[92:95], v[44:47], v[60:63]\n"
    "s_waitcnt lgkmcnt(5)\n"
    "v_mfma_f32_16x16x32_bf16 v[48:51], a[16:19], v[16:19], v[48:51]\n"
    "ds_read_b128 v[40:43], v88 offset:7168\n"
    "v_mfma_f32_16x16x32_bf16 v[56:59], a[96:99], v[16:19], v[56:59]\n"
    "s_waitcnt lgkmcnt(5)\n"
    "v_mfma_f32_16x16x32_bf16 v[52:55], a[16:19], v[20:23], v[52:55]\n"
    "ds_read_b128 v[44:47], v88 offset:27648\n"
    "v_mfma_f32_16x16x32_bf16 v[60:63], a[96:99], v[20:23], v[60:63]\n"
    "s_waitcnt lgkmcnt(5)\n"
    "v_mfma_f32_16x16x32_bf16 v[48:51], a[20:23], v[24:27], v[48:51]\n"
    "ds_read_b128 v[16:19], v88 offset:8192\n"
    "v_mfma_f32_16x16x32_bf16 v[56:59], a[100:103], v[24:27], v[56:59]\n"
    "s_waitcnt lgkmcnt(5)\n"
    "v_mfma_f32_16x16x32_bf16 v[52:55], a[20:23], v[28:31], v[52:55]\n"
    "ds_read_b128 v[20:23], v88 offset:28672\n"
    "v_mfma_f32_16x16x32_bf16 v[60:63], a[100:103], v[28:31], v[60:63]\n"
    "s_waitcnt lgkmcnt(5)\n"
    "v_mfma_f32_16x16x32_bf16 v[48:51], a[24:27], v[32:35], v[48:51]\n"
    "ds_read_b128 v[24:27], v88 offset:9216\n"
    "v_mfma_f32_16x16x32_bf16 v[56:59], a[104:107], v[32:35], v[56:59]\n"
    "s_waitcnt lgkmcnt(5)\n"
    "v_mfma_f32_16x16x32_bf16 v[52:55], a[24:27], v[36:39], v[52:55]\n"
    "ds_read_b128 v[28:31], v88 offset:29696\n"
    "v_mfma_f32_16x16x32_bf16 v[60:63], a[104:107], v[36:39], v[60:63]\n"
    "s_waitcnt lgkmcnt(5)\n"
    "v_mfma_f32_16x16x32_bf16 v[48:51], a[28:31], v[40:43], v[48:51]\n"
    "ds_read_b128 v[32:35], v88 offset:10240\n"
    "v_mfma_f32_16x16x32_bf16 v[56:59], a[108:111], v[40:43], v[56:59]\n"
    "s_waitcnt lgkmcnt(5)\n"
    "v_mfma_f32_16x16x32_bf16 v[52:55], a[28:31], v[44:47], v[52:55]\n"
    "ds_read_b128 v[36:39], v88 offset:30720\n"
    "v_mfma_f32_16x16x32_bf16 v[60:63], a[108:111], v[44:47], v[60:63]\n"
    "s_waitcnt lgkmcnt(5)\n"
    "v_mfma_f32_16x16x32_bf16 v[48:51], a[32:35], v[16:19], v[48:51]\n"
    "ds_read_b128 v[40:43], v88 offset:11264\n"
    "v_mfma_f32_16x16x32_bf16 v[56:59], a[112:115], v[16:19], v[56:59]\n"
    "s_waitcnt lgkmcnt(5)\n"
    "v_mfma_f32_16x16x32_bf16 v[52:55], a[32:35], v[20:23], v[52:55]\n"
    "ds_read_b128 v[44:47], v88 offset:31744\n"
    "v_mfma_f32_16x16x32_bf16 v[60:63], a[112:115], v[20:23], v[60:63]\n"
    "s_waitcnt lgkmcnt(5)\n"
    "v_mfma_f32_16x16x32_bf16 v[48:51], a[36:39], v[24:27], v[48:51]\n"
    "ds_read_b128 v[16:19], v88 offset:12288\n"
    "v_mfma_f32_16x16x32_bf16 v[56:59], a[116:119], v[24:27], v[56:59]\n"
    "s_waitcnt lgkmcnt(5)\n"
    "v_mfma_f32_16x16x32_bf16 v[52:55], a[36:39], v[28:31], v[52:55]\n"
    "ds_read_b128 v[20:23], v88 offset:32768\n"
    "v_mfma_f32_16x16x32_bf16 v[60:63], a[116:119], v[28:31], v[60:63]\n"
    "s_waitcnt lgkmcnt(5)\n"
    "v_mfma_f32_16x16x32_bf16 v[48:51], a[40:43], v[32:35], v[48:51]\n"
    "ds_read_b128 v[24:27], v88 offset:13312\n"
    "v_mfma_f32_16x16x32_bf16 v[56:59], a[120:123], v[32:35], v[56:59]\n"
    "s_waitcnt lgkmcnt(5)\n"
    "v_mfma_f32_16x16x32_bf16 v[52:55], a[40:43], v[36:39], v[52:55]\n"
    "ds_read_b128 v[28:31], v88 offset:33792\n"
    "v_mfma_f32_16x16x32_bf16 v[60:63], a[120:123], v[36:39], v[60:63]\n"
    "s_waitcnt lgkmcnt(5)\n"
    "v_mfma_f32_16x16x32_bf16 v[48:51], a[44:47], v[40:43], v[48:51]\n"
    "ds_read_b128 v[32:35], v88 offset:14336\n"
    "v_mfma_f32_16x16x32_bf16 v[56:59], a[124:127], v[40:43], v[56:59]\n"
    "s_waitcnt lgkmcnt(5)\n"
    "v_mfma_f32_16x16x32_bf16 v[52:55], a[44:47], v[44:47], v[52:55]\n"
    "ds_read_b128 v[36:39], v88 offset:34816\n"
    "v_mfma_f32_16x16x32_bf16 v[60:63], a[124:127], v[44:47], v[60:63]\n"
    "s_waitcnt lgkmcnt(5)\n"
    "v_mfma_f32_16x16x32_bf16 v[48:51], a[48:51], v[16:19], v[48:51]\n"
    "ds_read_b128 v[40:43], v88 offset:15360\n"
    "v_mfma_f32_16x16x32_bf16 v[56:59], a[128:131], v[16:19], v[56:59]\n"
    "s_waitcnt lgkmcnt(5)\n"
    "v_mfma_f32_16x16x32_bf16 v[52:55], a[48:51], v[20:23], v[52:55]\n"
    "ds_read_b128 v[44:47], v88 offset:35840\n"
    "v_mfma_f32_16x16x32_bf16 v[60:63], a[128:131], v[20:23], v[60:63]\n"
    "s_waitcnt lgkmcnt(5)\n"
    "v_mfma_f32_16x16x32_bf16 v[48:51], a[52:55], v[24:27], v[48:51]\n"
    "ds_read_b128 v[16:19], v88 offset:16384\n"
    "v_mfma_f32_16x16x32_bf16 v[56:59], a[132:135], v[24:27], v[56:59]\n"
    "s_waitcnt lgkmcnt(5)\n"
    "v_mfma_f32_16x16x32_bf16 v[52:55], a[52:55], v[28:31], v[52:55]\n"
    "ds_read_b128 v[20:23], v88 offset:36864\n"
    "v_mfma_f32_16x16x32_bf16 v[60:63], a[132:135], v[28:31], v[60:63]\n"
    "s_waitcnt lgkmcnt(5)\n"
    "v_mfma_f32_16x16x32_bf16 v[48:51], a[56:59], v[32:35], v[48:51]\n"
    "ds_read_b128 v[24:27], v88 offset:17408\n"
    "v_mfma_f32_16x16x32_bf16 v[56:59], a[136:139], v[32:35], v[56:59]\n"
    "s_waitcnt lgkmcnt(5)\n"
    "v_mfma_f32_16x16x32_bf16 v[52:55], a[56:59], v[36:39], v[52:55]\n"
    "ds_read_b128 v[28:31], v88 offset:37888\n"
    "v_mfma_f32_16x16x32_bf16 v[60:63], a[136:139], v[36:39], v[60:63]\n"
    "s_waitcnt lgkmcnt(5)\n"
    "v_mfma_f32_16x16x32_bf16 v[48:51], a[60:63], v[40:43], v[48:51]\n"
    "ds_read_b128 v[32:35], v88 offset:18432\n"
    "v_mfma_f32_16x16x32_bf16 v[56:59], a[140:143], v[40:43], v[56:59]\n"
    "s_waitcnt lgkmcnt(5)\n"
    "v_mfma_f32_16x16x32_bf16 v[52:55], a[60:63], v[44:47], v[52:55]\n"
    "ds_read_b128 v[36:39], v88 offset:38912\n"
    "v_mfma_f32_16x16x32_bf16 v[60:63], a[140:143], v[44:47], v[60:63]\n"
    "s_waitcnt lgkmcnt(5)\n"
    "v_mfma_f32_16x16x32_bf16 v[48:51], a[64:67], v[16:19], v[48:51]\n"
    "ds_read_b128 v[40:43], v88 offset:19456\n"
    "v_mfma_f32_16x16x32_bf16 v[56:59], a[144:147], v[16:19], v[56:59]\n"
    "s_waitcnt lgkmcnt(5)\n"
    "v_mfma_f32_16x16x32_bf16 v[52:55], a[64:67], v[20:23], v[52:55]\n"
    "ds_read_b128 v[44:47], v88 offset:39936\n"
    "v_mfma_f32_16x16x32_bf16 v[60:63], a[144:147], v[20:23], v[60:63]\n"
    "s_waitcnt lgkmcnt(5)\n"
    "v_mfma_f32_16x16x32_bf16 v[48:51], a[68:71], v[24:27], v[48:51]\n"
    "s_waitcnt vmcnt(0)\n"
    "s_mov_b32 s42, 0\n"
    "s_barrier\n"
    "s_cmp_lg_u32 %[w], 0\n"
    "s_cbranch_scc1 Qnf01_%=\n"
    "v_mov_b32 v92, s42\n"
    "s_mov_b64 exec, 1\n"
    "global_store_dword v93, v92, s[56:57] offset:0 sc1\n"
    "s_mov_b64 exec, -1\n"
    "Qnf01_%=:\n"
    "s_sub_u32 s55, 122880, s53\n"
    "s_sub_u32 s55, s55, s54\n"
    "s_mov_b32 s53, s54\n"
    "s_mov_b32 s54, s55\n"
    "s_add_u32 s55, s55, s52\n"
    "ds_read_b128 v[16:19], v89 offset:0\n"
    "v_mfma_f32_16x16x32_bf16 v[56:59], a[148:151], v[24:27], v[56:59]\n"
    "s_waitcnt lgkmcnt(5)\n"
    "s_add_u32 m0, s55, 0\n"
    "v_mfma_f32_16x16x32_bf16 v[52:55], a[68:71], v[28:31], v[52:55]\n"
    "ds_read_b128 v[20:23], v89 offset:20480\n"
    "v_mfma_f32_16x16x32_bf16 v[60:63], a[148:151], v[28:31], v[60:63]\n"
    "s_waitcnt lgkmcnt(5)\n"
    "global_load_lds_dwordx4 %[vxv], s[46:47] offset:0 sc1\n"
    "v_mfma_f32_16x16x32_bf16 v[48:51], a[72:75], v[32:35], v[48:51]\n"
    "ds_read_b128 v[24:27], v89 offset:1024\n"
    "v_mfma_f32_16x16x32_bf16 v[56:59], a[152:155], v[32:35], v[56:59]\n"
    "s_waitcnt lgkmcnt(5)\n"
    "global_load_lds_dwordx4 %[vxv], s[46:47] offset:1024 sc1\n"
    "v_mfma_f32_16x16x32_bf16 v[52:55], a[72:75], v[36:39], v[52:55]\n"
    "ds_read_b128 v[28:31], v89 offset:21504\n"
    "v_mfma_f32_16x16x32_bf16 v[60:63], a[152:155], v[36:39], v[60:63]\n"
    "s_waitcnt lgkmcnt(5)\n"
    "global_load_lds_dwordx4 %[vxv], s[46:47] offset:2048 sc1\n"
    "v_mfma_f32_16x16x32_bf16 v[48:51], a[76:79], v[40:43], v[48:51]\n"
    "ds_read_b128 v[32:35], v89 offset:2048\n"
    "v_mfma_f32_16x16x32_bf16 v[56:59], a[156:159], v[40:43], v[56:59]\n"
    "s_waitcnt lgkmcnt(5)\n"
    "global_load_lds_dwordx4 %[vxv], s[46:47] offset:3072 sc1\n"
    "v_mfma_f32_16x16x32_bf16 v[52:55], a[76:79], v[44:47], v[52:55]\n"
    "ds_read_b128 v[36:39], v89 offset:22528\n"
    "v_mfma_f32_16x16x32_bf16 v[60:63], a[156:159], v[44:47], v[60:63]\n"
    "s_add_u32 s46, s46, 4096\n"
    "s_addc_u32 s47, s47, 0\n"
    "v_mov_b32 v88, v89\n"
    "v_add_u32 v89, s54, %[vlr]\n"
    "s_add_u32 s40, s40, 1\n"
    "s_nop 7\n"
    "s_add_u32 s50, s48, 4096\n"
    "s_addc_u32 s51, s49, 0\n"
    "v_cvt_pk_bf16_f32 v80, v48, v49\n"
    "v_cvt_pk_bf16_f32 v81, v50, v51\n"
    "v_cvt_pk_bf16_f32 v82, v56, v57\n"
    "v_cvt_pk_bf16_f32 v83, v58, v59\n"
    "global_store_dwordx4 %[vovo], v[80:83], s[48:49] offset:0 sc1\n"
    "v_cvt_pk_bf16_f32 v84, v52, v53\n"
    "v_cvt_pk_bf16_f32 v85, v54, v55\n"
    "v_cvt_pk_bf16_f32 v86, v60, v61\n"
    "v_cvt_pk_bf16_f32 v87, v62, v63\n"
    "global_store_dwordx4 %[vovo], v[84:87], s[50:51] offset:0 sc1\n"
    "s_waitcnt vmcnt(0)\n"
    "s_barrier\n"
    "s_cmp_lg_u32 %[w], 0\n"
    "s_cbranch_scc1 Qloop_%=\n"
    "v_mov_b32 v92, 1\n"
    "s_mov_b64 exec, 1\n"
    "global_store_dword v93, v92, s[56:57] offset:0 sc1\n"
    "s_mov_b64 exec, -1\n"
    "Qloop_%=:\n"
    "s_waitcnt lgkmcnt(5)\n"
    "v_mfma_f32_16x16x32_bf16 v[64:67], a[0:3], v[16:19], 0\n"
    "ds_read_b128 v[40:43], v88 offset:3072\n"
    "v_mfma_f32_16x16x32_bf16 v[72:75], a[80:83], v[16:19], 0\n"
    "s_waitcnt lgkmcnt(5)\n"
    "s_add_u32 m0, s55, 4096\n"
    "v_mfma_f32_16x16x32_bf16 v[68:71], a[0:3], v[20:23], 0\n"
    "ds_read_b128 v[44:47], v88 offset:23552\n"
    "v_mfma_f32_16x16x32_bf16 v[76:79], a[80:83], v[20:23], 0\n"
    "s_waitcnt lgkmcnt(5)\n"
    "global_load_lds_dwordx4 %[vxv], s[46:47] offset:0 sc1\n"
    "v_mfma_f32_16x16x32_bf16 v[64:67], a[4:7], v[24:27], v[64:67]\n"
    "ds_read_b128 v[16:19], v88 offset:4096\n"
    "v_mfma_f32_16x16x32_bf16 v[72:75], a[84:87], v[24:27], v[72:75]\n"
    "s_waitcnt lgkmcnt(5)\n"
    "global_load_lds_dwordx4 %[vxv], s[46:47] offset:1024 sc1\n"
    "v_mfma_f32_16x16x32_bf16 v[68:71], a[4:7], v[28:31], v[68:71]\n"
    "ds_read_b128 v[20:23], v88 offset:24576\n"
    "v_mfma_f32_16x16x32_bf16 v[76:79], a[84:87], v[28:31], v[76:79]\n"
    "s_waitcnt lgkmcnt(5)\n"
    "global_load_lds_dwordx4 %[vxv], s[46:47] offset:2048 sc1\n"
    "v_mfma_f32_16x16x32_bf16 v[64:67], a[8:11], v[32:35], v[64:67]\n"
    "ds_read_b128 v[24:27], v88 offset:5120\n"
    "v_mfma_f32_16x16x32_bf16 v[72:75], a[88:91], v[32:35], v[72:75]\n"
    "s_waitcnt lgkmcnt(5)\n"
    "global_load_lds_dwordx4 %[vxv], s[46:47] offset:3072 sc1\n"
    "v_mfma_f32_16x16x32_bf16 v[68:71], a[8:11], v[36:39], v[68:71]\n"
    "ds_read_b128 v[28:31], v88 offset:25600\n"
    "v_mfma_f32_16x16x32_bf16 v[76:79], a[88:91], v[36:39], v[76:79]\n"
    "s_waitcnt lgkmcnt(5)\n"
    "s_add_u32 s46, s46, 4096\n"
    "s_addc_u32 s47, s47, 0\n"
    "v_mfma_f32_16x16x32_bf16 v[64:67], a[12:15], v[40:43], v[64:67]\n"
    "ds_read_b128 v[32:35], v88 offset:6144\n"
    "v_mfma_f32_16x16x32_bf16 v[72:75], a[92:95], v[40:43], v[72:75]\n"
    "s_waitcnt lgkmcnt(5)\n"
    "s_add_u32 m0, s55, 8192\n"
    "v_mfma_f32_16x16x32_bf16 v[68:71], a[12:15], v[44:47], v[68:71]\n"
    "ds_read_b128 v[36:39], v88 offset:26624\n"
    "v_mfma_f32_16x16x32_bf16 v[76:79], a[92:95], v[44:47], v[76:79]\n"
    "s_waitcnt lgkmcnt(5)\n"
    "global_load_lds_dwordx4 %[vxv], s[46:47] offset:0 sc1\n"
    "v_mfma_f32_16x16x32_bf16 v[64:67], a[16:19], v[16:19], v[64:67]\n"
    "ds_read_b128 v[40:43], v88 offset:7168\n"
    "v_mfma_f32_16x16x32_bf16 v[72:75], a[96:99], v[16:19], v[72:75]\n"
    "s_waitcnt lgkmcnt(5)\n"
    "global_load_lds_dwordx4 %[vxv], s[46:47] offset:1024 sc1\n"
    "v_mfma_f32_16x16x32_bf16 v[68:71], a[16:19], v[20:23], v[68:71]\n"
    "ds_read_b128 v[44:47], v88 offset:27648\n"
    "v_mfma_f32_16x16x32_bf16 v[76:79], a[96:99], v[20:23], v[76:79]\n"
    "s_waitcnt lgkmcnt(5)\n"
    "s_add_u32 s46, s46, 32768\n"
    "s_addc_u32 s47, s47, 0\n"
    "v_mfma_f32_16x16x32_bf16 v[64:67], a[20:23], v[24:27], v[64:67]\n"
    "ds_read_b128 v[16:19], v88 offset:8192\n"
    "v_mfma_f32_16x16x32_bf16 v[72:75], a[100:103], v[24:27], v[72:75]\n"
    "s_waitcnt lgkmcnt(5)\n"
    "s_add_u32 s50, s48, 4096\n"
    "s_addc_u32 s51, s49, 0\n"
    "v_cvt_pk_bf16_f32 v80, v48, v49\n"
    "v_mfma_f32_16x16x32_bf16 v[68:71], a[20:23], v[28:31], v[68:71]\n"
    "ds_read_b128 v[20:23], v88 offset:28672\n"
    "v_mfma_f32_16x16x32_bf16 v[76:79], a[100:103], v[28:31], v[76:79]\n"
    "s_waitcnt lgkmcnt(5)\n"
    "v_cvt_pk_bf16_f32 v81, v50, v51\n"
    "v_cvt_pk_bf16_f32 v82, v56, v57\n"
    "v_mfma_f32_16x16x32_bf16 v[64:67], a[24:27], v[32:35], v[64:67]\n"
    "ds_read_b128 v[24:27], v88 offset:9216\n"
    "v_mfma_f32_16x16x32_bf16 v[72:75], a[104:107], v[32:35], v[72:75]\n"
    "s_waitcnt lgkmcnt(5)\n"
    "v_cvt_pk_bf16_f32 v83, v58, v59\n"
    "global_store_dwordx4 %[vovo], v[80:83], s[48:49] offset:0 sc1\n"
    "v_mfma_f32_16x16x32_bf16 v[68:71], a[24:27], v[36:39], v[68:71]\n"
    "ds_read_b128 v[28:31], v88 offset:29696\n"
    "v_mfma_f32_16x16x32_bf16 v[76:79], a[104:107], v[36:39], v[76:79]\n"
    "s_waitcnt lgkmcnt(5)\n"
    "v_cvt_pk_bf16_f32 v84, v52, v53\n"
    "v_cvt_pk_bf16_f32 v85, v54, v55\n"
    "v_mfma_f32_16x16x32_bf16 v[64:67], a[28:31], v[40:43], v[64:67]\n"
    "ds_read_b128 v[32:35], v88 offset:10240\n"
    "v_mfma_f32_16x16x32_bf16 v[72:75], a[108:111], v[40:43], v[72:75]\n"
    "s_waitcnt lgkmcnt(5)\n"
    "v_cvt_pk_bf16_f32 v86, v60, v61\n"
    "v_cvt_pk_bf16_f32 v87, v62, v63\n"
    "v_mfma_f32_16x16x32_bf16 v[68:71], a[28:31], v[44:47], v[68:71]\n"
    "ds_read_b128 v[36:39], v88 offset:30720\n"
    "v_mfma_f32_16x16x32_bf16 v[76:79], a[108:111], v[44:47], v[76:79]\n"
    "s_waitcnt lgkmcnt(5)\n"
    "global_store_dwordx4 %[vovo], v[84:87], s[50:51] offset:0 sc1\n"
    "s_add_u32 s48, s48, 524288\n"
    "s_addc_u32 s49, s49, 0\n"
    "s_and_b32 s41, s48, 0x180000\n"
    "s_cselect_b32 s41, 0, 8192\n"
    "s_xor_b32 s48, s48, s41\n"
    "v_mfma_f32_16x16x32_bf16 v[64:67], a[32:35], v[16:19], v[64:67]\n"
    "ds_read_b128 v[40:43], v88 offset:11264\n"
    "v_mfma_f32_16x16x32_bf16 v[72:75], a[112:115], v[16:19], v[72:75]\n"
    "s_waitcnt lgkmcnt(5)\n"
    "v_mfma_f32_16x16x32_bf16 v[68:71], a[32:35], v[20:23], v[68:71]\n"
    "ds_read_b128 v[44:47], v88 offset:31744\n"
    "v_mfma_f32_16x16x32_bf16 v[76:79], a[112:115], v[20:23], v[76:79]\n"
    "s_waitcnt lgkmcnt(5)\n"
    "v_mfma_f32_16x16x32_bf16 v[64:67], a[36:39], v[24:27], v[64:67]\n"
    "ds_read_b128 v[16:19], v88 offset:12288\n"
    "v_mfma_f32_16x16x32_bf16 v[72:75], a[116:119], v[24:27], v[72:75]\n"
    "s_waitcnt lgkmcnt(5)\n"
    "v_mfma_f32_16x16x32_bf16 v[68:71], a[36:39], v[28:31], v[68:71]\n"
    "ds_read_b128 v[20:23], v88 offset:32768\n"
    "v_mfma_f32_16x16x32_bf16 v[76:79], a[116:119], v[28:31], v[76:79]\n"
    "s_waitcnt lgkmcnt(5)\n"
    "v_mfma_f32_16x16x32_bf16 v[64:67], a[40:43], v[32:35], v[64:67]\n"
    "ds_read_b128 v[24:27], v88 offset:13312\n"
    "v_mfma_f32_16x16x32_bf16 v[72:75], a[120:123], v[32:35], v[72:75]\n"
    "s_waitcnt lgkmcnt(5)\n"
    "v_mfma_f32_16x16x32_bf16 v[68:71], a[40:43], v[36:39], v[68:71]\n"
    "ds_read_b128 v[28:31], v88 offset:33792\n"
    "v_mfma_f32_16x16x32_bf16 v[76:79], a[120:123], v[36:39], v[76:79]\n"
    "s_waitcnt lgkmcnt(5)\n"
    "v_mfma_f32_16x16x32_bf16 v[64:67], a[44:47], v[40:43], v[64:67]\n"
    "ds_read_b128 v[32:35], v88 offset:14336\n"
    "v_mfma_f32_16x16x32_bf16 v[72:75], a[124:127], v[40:43], v[72:75]\n"
    "s_waitcnt lgkmcnt(5)\n"
    "v_mfma_f32_16x16x32_bf16 v[68:71], a[44:47], v[44:47], v[68:71]\n"
    "ds_read_b128 v[36:39], v88 offset:34816\n"
    "v_mfma_f32_16x16x32_bf16 v[76:79], a[124:127], v[44:47], v[76:79]\n"
    "s_waitcnt lgkmcnt(5)\n"
    "v_mfma_f32_16x16x32_bf16 v[64:67], a[48:51], v[16:19], v[64:67]\n"
    "ds_read_b128 v[40:43], v88 offset:15360\n"
    "v_mfma_f32_16x16x32_bf16 v[72:75], a[128:131], v[16:19], v[72:75]\n"
    "s_waitcnt lgkmcnt(5)\n"
    "v_mfma_f32_16x16x32_bf16 v[68:71], a[48:51], v[20:23], v[68:71]\n"
    "ds_read_b128 v[44:47], v88 offset:35840\n"
    "v_mfma_f32_16x16x32_bf16 v[76:79], a[128:131], v[20:23], v[76:79]\n"
    "s_waitcnt lgkmcnt(5)\n"
    "v_mfma_f32_16x16x32_bf16 v[64:67], a[52:55], v[24:27], v[64:67]\n"
    "ds_read_b128 v[16:19], v88 offset:16384\n"
    "v_mfma_f32_16x16x32_bf16 v[72:75], a[132:135], v[24:27], v[72:75]\n"
    "s_waitcnt lgkmcnt(5)\n"
    "v_mfma_f32_16x16x32_bf16 v[68:71], a[52:55], v[28:31], v[68:71]\n"
    "ds_read_b128 v[20:23], v88 offset:36864\n"
    "v_mfma_f32_16x16x32_bf16 v[76:79], a[132:135], v[28:31], v[76:79]\n"
    "s_waitcnt lgkmcnt(5)\n"
    "v_mfma_f32_16x16x32_bf16 v[64:67], a[56:59], v[32:35], v[64:67]\n"
    "ds_read_b128 v[24:27], v88 offset:17408\n"
    "v_mfma_f32_16x16x32_bf16 v[72:75], a[136:139], v[32:35], v[72:75]\n"
    "s_waitcnt lgkmcnt(5)\n"
    "v_mfma_f32_16x16x32_bf16 v[68:71], a[56:59], v[36:39], v[68:71]\n"
    "ds_read_b128 v[28:31], v88 offset:37888\n"
    "v_mfma_f32_16x16x32_bf16 v[76:79], a[136:139], v[36:39], v[76:79]\n"
    "s_waitcnt lgkmcnt(5)\n"
    "v_mfma_f32_16x16x32_bf16 v[64:67], a[60:63], v[40:43], v[64:67]\n"
    "ds_read_b128 v[32:35], v88 offset:18432\n"
    "v_mfma_f32_16x16x32_bf16 v[72:75], a[140:143], v[40:43], v[72:75]\n"
    "s_waitcnt lgkmcnt(5)\n"
    "v_mfma_f32_16x16x32_bf16 v[68:71], a[60:63], v[44:47], v[68:71]\n"
    "ds_read_b128 v[36:39], v88 offset:38912\n"
    "v_mfma_f32_16x16x32_bf16 v[76:79], a[140:143], v[44:47], v[76:79]\n"
    "s_waitcnt lgkmcnt(5)\n"
    "v_mfma_f32_16x16x32_bf16 v[64:67], a[64:67], v[16:19], v[64:67]\n"
    "ds_read_b128 v[40:43], v88 offset:19456\n"
    "v_mfma_f32_16x16x32_bf16 v[72:75], a[144:147], v[16:19], v[72:75]\n"
    "s_waitcnt lgkmcnt(5)\n"
    "v_mfma_f32_16x16x32_bf16 v[68:71], a[64:67], v[20:23], v[68:71]\n"
    "ds_read_b128 v[44:47], v88 offset:39936\n"
    "v_mfma_f32_16x16x32_bf16 v[76:79], a[144:147], v[20:23], v[76:79]\n"
    "s_waitcnt lgkmcnt(5)\n"
    "v_mfma_f32_16x16x32_bf16 v[64:67], a[68:71], v[24:27], v[64:67]\n"
    "s_waitcnt vmcnt(2)\n"
    "s_sub_u32 s42, s40, 1\n"
    "s_cmp_gt_u32 s40, 4\n"
    "s_cbranch_scc1 Qnw1_%=\n"
    "s_waitcnt vmcnt(0)\n"
    "s_mov_b32 s42, s40\n"
    "Qnw1_%=:\n"
    "s_barrier\n"
    "s_cmp_lg_u32 %[w], 0\n"
    "s_cbranch_scc1 Qnf10_%=\n"
    "v_mov_b32 v92, s42\n"
    "s_mov_b64 exec, 1\n"
    "global_store_dword v93, v92, s[56:57] offset:0 sc1\n"
    "s_mov_b64 exec, -1\n"
    "Qnf10_%=:\n"
    "s_sub_u32 s55, 122880, s53\n"
    "s_sub_u32 s55, s55, s54\n"
    "s_mov_b32 s53, s54\n"
    "s_mov_b32 s54, s55\n"
    "s_add_u32 s55, s55, s52\n"
    "ds_read_b128 v[16:19], v89 offset:0\n"
    "v_mfma_f32_16x16x32_bf16 v[72:75], a[148:151], v[24:27], v[72:75]\n"
    "s_waitcnt lgkmcnt(5)\n"
    "s_add_u32 m0, s55, 0\n"
    "v_mfma_f32_16x16x32_bf16 v[68:71], a[68:71], v[28:31], v[68:71]\n"
    "ds_read_b128 v[20:23], v89 offset:20480\n"
    "v_mfma_f32_16x16x32_bf16 v[76:79], a[148:151], v[28:31], v[76:79]\n"
    "s_waitcnt lgkmcnt(5)\n"
    "global_load_lds_dwordx4 %[vxv], s[46:47] offset:0 sc1\n"
    "v_mfma_f32_16x16x32_bf16 v[64:67], a[72:75], v[32:35], v[64:67]\n"
    "ds_read_b128 v[24:27], v89 offset:1024\n"
    "v_mfma_f32_16x16x32_bf16 v[72:75], a[152:155], v[32:35], v[72:75]\n"
    "s_waitcnt lgkmcnt(5)\n"
    "global_load_lds_dwordx4 %[vxv], s[46:47] offset:1024 sc1\n"
    "v_mfma_f32_16x16x32_bf16 v[68:71], a[72:75], v[36:39], v[68:71]\n"
    "ds_read_b128 v[28:31], v89 offset:21504\n"
    "v_mfma_f32_16x16x32_bf16 v[76:79], a[152:155], v[36:39], v[76:79]\n"
    "s_waitcnt lgkmcnt(5)\n"
    "global_load_lds_dwordx4 %[vxv], s[46:47] offset:2048 sc1\n"
    "v_mfma_f32_16x16x32_bf16 v[64:67], a[76:79], v[40:43], v[64:67]\n"
    "ds_read_b128 v[32:35], v89 offset:2048\n"
    "v_mfma_f32_16x16x32_bf16 v[72:75], a[156:159], v[40:43], v[72:75]\n"
    "s_waitcnt lgkmcnt(5)\n"
    "global_load_lds_dwordx4 %[vxv], s[46:47] offset:3072 sc1\n"
    "v_mfma_f32_16x16x32_bf16 v[68:71], a[76:79], v[44:47], v[68:71]\n"
    "ds_read_b128 v[36:39], v89 offset:22528\n"
    "v_mfma_f32_16x16x32_bf16 v[76:79], a[156:159], v[44:47], v[76:79]\n"
    "s_add_u32 s46, s46, 4096\n"
    "s_addc_u32 s47, s47, 0\n"
    "v_mov_b32 v88, v89\n"
    "v_add_u32 v89, s54, %[vlr]\n"
    "s_add_u32 s40, s40, 1\n"
    "s_cmp_eq_u32 s40, 64\n"
    "s_cbranch_scc1 Qdone_%=\n"
    "s_waitcnt lgkmcnt(5)\n"
    "v_mfma_f32_16x16x32_bf16 v[48:51], a[0:3], v[16:19], 0\n"
    "ds_read_b128 v[40:43], v88 offset:3072\n"
    "v_mfma_f32_16x16x32_bf16 v[56:59], a[80:83], v[16:19], 0\n"
    "s_waitcnt lgkmcnt(5)\n"
    "s_add_u32 m0, s55, 4096\n"
    "v_mfma_f32_16x16x32_bf16 v[52:55], a[0:3], v[20:23], 0\n"
    "ds_read_b128 v[44:47], v88 offset:23552\n"
    "v_mfma_f32_16x16x32_bf16 v[60:63], a[80:83], v[20:23], 0\n"
    "s_waitcnt lgkmcnt(5)\n"
    "global_load_lds_dwordx4 %[vxv], s[46:47] offset:0 sc1\n"
    "v_mfma_f32_16x16x32_bf16 v[48:51], a[4:7], v[24:27], v[48:51]\n"
    "ds_read_b128 v[16:19], v88 offset:4096\n"
    "v_mfma_f32_16x16x32_bf16 v[56:59], a[84:87], v[24:27], v[56:59]\n"
    "s_waitcnt lgkmcnt(5)\n"
    "global_load_lds_dwordx4 %[vxv], s[46:47] offset:1024 sc1\n"
    "v_mfma_f32_16x16x32_bf16 v[52:55], a[4:7], v[28:31], v[52:55]\n"
    "ds_read_b128 v[20:23], v88 offset:24576\n"
    "v_mfma_f32_16x16x32_bf16 v[60:63], a[84:87], v[28:31], v[60:63]\n"
    "s_waitcnt lgkmcnt(5)\n"
    "global_load_lds_dwordx4 %[vxv], s[46:47] offset:2048 sc1\n"
    "v_mfma_f32_16x16x32_bf16 v[48:51], a[8:11], v[32:35], v[48:51]\n"
    "ds_read_b128 v[24:27], v88 offset:5120\n"
    "v_mfma_f32_16x16x32_bf16 v[56:59], a[88:91], v[32:35], v[56:59]\n"
    "s_waitcnt lgkmcnt(5)\n"
    "global_load_lds_dwordx4 %[vxv], s[46:47] offset:3072 sc1\n"
    "v_mfma_f32_16x16x32_bf16 v[52:55], a[8:11], v[36:39], v[52:55]\n"
    "ds_read_b128 v[28:31], v88 offset:25600\n"
    "v_mfma_f32_16x16x32_bf16 v[60:63], a[88:91], v[36:39], v[60:63]\n"
    "s_waitcnt lgkmcnt(5)\n"
    "s_add_u32 s46, s46, 4096\n"
    "s_addc_u32 s47, s47, 0\n"
    "v_mfma_f32_16x16x32_bf16 v[48:51], a[12:15], v[40:43], v[48:51]\n"
    "ds_read_b128 v[32:35], v88 offset:6144\n"
    "v_mfma_f32_16x16x32_bf16 v[56:59], a[92:95], v[40:43], v[56:59]\n"
    "s_waitcnt lgkmcnt(5)\n"
    "s_add_u32 m0, s55, 8192\n"
    "v_mfma_f32_16x16x32_bf16 v[52:55], a[12:15], v[44:47], v[52:55]\n"
    "ds_read_b128 v[36:39], v88 offset:26624\n"
    "v_mfma_f32_16x16x32_bf16 v[60:63], a[92:95], v[44:47], v[60:63]\n"
    "s_waitcnt lgkmcnt(5)\n"
    "global_load_lds_dwordx4 %[vxv], s[46:47] offset:0 sc1\n"
    "v_mfma_f32_16x16x32_bf16 v[48:51], a[16:19], v[16:19], v[48:51]\n"
    "ds_read_b128 v[40:43], v88 offset:7168\n"
    "v_mfma_f32_16x16x32_bf16 v[56:59], a[96:99], v[16:19], v[56:59]\n"
    "s_waitcnt lgkmcnt(5)\n"
    "global_load_lds_dwordx4 %[vxv], s[46:47] offset:1024 sc1\n"
    "v_mfma_f32_16x16x32_bf16 v[52:55], a[16:19], v[20:23], v[52:55]\n"
    "ds_read_b128 v[44:47], v88 offset:27648\n"
    "v_mfma_f32_16x16x32_bf16 v[60:63], a[96:99], v[20:23], v[60:63]\n"
    "s_waitcnt lgkmcnt(5)\n"
    "s_add_u32 s46, s46, 32768\n"
    "s_addc_u32 s47, s47, 0\n"
    "v_mfma_f32_16x16x32_bf16 v[48:51], a[20:23], v[24:27], v[48:51]\n"
    "ds_read_b128 v[16:19], v88 offset:8192\n"
    "v_mfma_f32_16x16x32_bf16 v[56:59], a[100:103], v[24:27], v[56:59]\n"
    "s_waitcnt lgkmcnt(5)\n"
    "s_add_u32 s50, s48, 4096\n"
    "s_addc_u32 s51, s49, 0\n"
    "v_cvt_pk_bf16_f32 v80, v64, v65\n"
    "v_mfma_f32_16x16x32_bf16 v[52:55], a[20:23], v[28:31], v[52:55]\n"
    "ds_read_b128 v[20:23], v88 offset:28672\n"
    "v_mfma_f32_16x16x32_bf16 v[60:63], a[100:103], v[28:31], v[60:63]\n"
    "s_waitcnt lgkmcnt(5)\n"
    "v_cvt_pk_bf16_f32 v81, v66, v67\n"
    "v_cvt_pk_bf16_f32 v82, v72, v73\n"
    "v_mfma_f32_16x16x32_bf16 v[48:51], a[24:27], v[32:35], v[48:51]\n"
    "ds_read_b128 v[24:27], v88 offset:9216\n"
    "v_mfma_f32_16x16x32_bf16 v[56:59], a[104:107], v[32:35], v[56:59]\n"
    "s_waitcnt lgkmcnt(5)\n"
    "v_cvt_pk_bf16_f32 v83, v74, v75\n"
    "global_store_dwordx4 %[vovo], v[80:83], s[48:49] offset:0 sc1\n"
    "v_mfma_f32_16x16x32_bf16 v[52:55], a[24:27], v[36:39], v[52:55]\n"
    "ds_read_b128 v[28:31], v88 offset:29696\n"
    "v_mfma_f32_16x16x32_bf16 v[60:63], a[104:107], v[36:39], v[60:63]\n"
    "s_waitcnt lgkmcnt(5)\n"
    "v_cvt_pk_bf16_f32 v84, v68, v69\n"
    "v_cvt_pk_bf16_f32 v85, v70, v71\n"
    "v_mfma_f32_16x16x32_bf16 v[48:51], a[28:31], v[40:43], v[48:51]\n"
    "ds_read_b128 v[32:35], v88 offset:10240\n"
    "v_mfma_f32_16x16x32_bf16 v[56:59], a[108:111], v[40:43], v[56:59]\n"
    "s_waitcnt lgkmcnt(5)\n"
    "v_cvt_pk_bf16_f32 v86, v76, v77\n"
    "v_cvt_pk_bf16_f32 v87, v78, v79\n"
    "v_mfma_f32_16x16x32_bf16 v[52:55], a[28:31], v[44:47], v[52:55]\n"
    "ds_read_b128 v[36:39], v88 offset:30720\n"
    "v_mfma_f32_16x16x32_bf16 v[60:63], a[108:111], v[44:47], v[60:63]\n"
    "s_waitcnt lgkmcnt(5)\n"
    "global_store_dwordx4 %[vovo], v[84:87], s[50:51] offset:0 sc1\n"
    "s_add_u32 s48, s48, 524288\n"
    "s_addc_u32 s49, s49, 0\n"
    "s_and_b32 s41, s48, 0x180000\n"
    "s_cselect_b32 s41, 0, 8192\n"
    "s_xor_b32 s48, s48, s41\n"
    "v_mfma_f32_16x16x32_bf16 v[48:51], a[32:35], v[16:19], v[48:51]\n"
    "ds_read_b128 v[40:43], v88 offset:11264\n"
    "v_mfma_f32_16x16x32_bf16 v[56:59], a[112:115], v[16:19], v[56:59]\n"
    "s_waitcnt lgkmcnt(5)\n"
    "v_mfma_f32_16x16x32_bf16 v[52:55], a[32:35], v[20:23], v[52:55]\n"
    "ds_read_b128 v[44:47], v88 offset:31744\n"
    "v_mfma_f32_16x16x32_bf16 v[60:63], a[112:115], v[20:23], v[60:63]\n"
    "s_waitcnt lgkmcnt(5)\n"
    "v_mfma_f32_16x16x32_bf16 v[48:51], a[36:39], v[24:27], v[48:51]\n"
    "ds_read_b128 v[16:19], v88 offset:12288\n"
    "v_mfma_f32_16x16x32_bf16 v[56:59], a[116:119], v[24:27], v[56:59]\n"
    "s_waitcnt lgkmcnt(5)\n"
    "v_mfma_f32_16x16x32_bf16 v[52:55], a[36:39], v[28:31], v[52:55]\n"
    "ds_read_b128 v[20:23], v88 offset:32768\n"
    "v_mfma_f32_16x16x32_bf16 v[60:63], a[116:119], v[28:31], v[60:63]\n"
    "s_waitcnt lgkmcnt(5)\n"
    "v_mfma_f32_16x16x32_bf16 v[48:51], a[40:43], v[32:35], v[48:51]\n"
    "ds_read_b128 v[24:27], v88 offset:13312\n"
    "v_mfma_f32_16x16x32_bf16 v[56:59], a[120:123], v[32:35], v[56:59]\n"
    "s_waitcnt lgkmcnt(5)\n"
    "v_mfma_f32_16x16x32_bf16 v[52:55], a[40:43], v[36:39], v[52:55]\n"
    "ds_read_b128 v[28:31], v88 offset:33792\n"
    "v_mfma_f32_16x16x32_bf16 v[60:63], a[120:123], v[36:39], v[60:63]\n"
    "s_waitcnt lgkmcnt(5)\n"
    "v_mfma_f32_16x16x32_bf16 v[48:51], a[44:47], v[40:43], v[48:51]\n"
    "ds_read_b128 v[32:35], v88 offset:14336\n"
    "v_mfma_f32_16x16x32_bf16 v[56:59], a[124:127], v[40:43], v[56:59]\n"
    "s_waitcnt lgkmcnt(5)\n"
    "v_mfma_f32_16x16x32_bf16 v[52:55], a[44:47], v[44:47], v[52:55]\n"
    "ds_read_b128 v[36:39], v88 offset:34816\n"
    "v_mfma_f32_16x16x32_bf16 v[60:63], a[124:127], v[44:47], v[60:63]\n"
    "s_waitcnt lgkmcnt(5)\n"
    "v_mfma_f32_16x16x32_bf16 v[48:51], a[48:51], v[16:19], v[48:51]\n"
    "ds_read_b128 v[40:43], v88 offset:15360\n"
    "v_mfma_f32_16x16x32_bf16 v[56:59], a[128:131], v[16:19], v[56:59]\n"
    "s_waitcnt lgkmcnt(5)\n"
    "v_mfma_f32_16x16x32_bf16 v[52:55], a[48:51], v[20:23], v[52:55]\n"
    "ds_read_b128 v[44:47], v88 offset:35840\n"
    "v_mfma_f32_16x16x32_bf16 v[60:63], a[128:131], v[20:23], v[60:63]\n"
    "s_waitcnt lgkmcnt(5)\n"
    "v_mfma_f32_16x16x32_bf16 v[48:51], a[52:55], v[24:27], v[48:51]\n"
    "ds_read_b128 v[16:19], v88 offset:16384\n"
    "v_mfma_f32_16x16x32_bf16 v[56:59], a[132:135], v[24:27], v[56:59]\n"
    "s_waitcnt lgkmcnt(5)\n"
    "v_mfma_f32_16x16x32_bf16 v[52:55], a[52:55], v[28:31], v[52:55]\n"
    "ds_read_b128 v[20:23], v88 offset:36864\n"
    "v_mfma_f32_16x16x32_bf16 v[60:63], a[132:135], v[28:31], v[60:63]\n"
    "s_waitcnt lgkmcnt(5)\n"
    "v_mfma_f32_16x16x32_bf16 v[48:51], a[56:59], v[32:35], v[48:51]\n"
    "ds_read_b128 v[24:27], v88 offset:17408\n"
    "v_mfma_f32_16x16x32_bf16 v[56:59], a[136:139], v[32:35], v[56:59]\n"
    "s_waitcnt lgkmcnt(5)\n"
    "v_mfma_f32_16x16x32_bf16 v[52:55], a[56:59], v[36:39], v[52:55]\n"
    "ds_read_b128 v[28:31], v88 offset:37888\n"
    "v_mfma_f32_16x16x32_bf16 v[60:63], a[136:139], v[36:39], v[60:63]\n"
    "s_waitcnt lgkmcnt(5)\n"
    "v_mfma_f32_16x16x32_bf16 v[48:51], a[60:63], v[40:43], v[48:51]\n"
    "ds_read_b128 v[32:35], v88 offset:18432\n"
    "v_mfma_f32_16x16x32_bf16 v[56:59], a[140:143], v[40:43], v[56:59]\n"
    "s_waitcnt lgkmcnt(5)\n"
    "v_mfma_f32_16x16x32_bf16 v[52:55], a[60:63], v[44:47], v[52:55]\n"
    "ds_read_b128 v[36:39], v88 offset:38912\n"
    "v_mfma_f32_16x16x32_bf16 v[60:63], a[140:143], v[44:47], v[60:63]\n"
    "s_waitcnt lgkmcnt(5)\n"
    "v_mfma_f32_16x16x32_bf16 v[48:51], a[64:67], v[16:19], v[48:51]\n"
    "ds_read_b128 v[40:43], v88 offset:19456\n"
    "v_mfma_f32_16x16x32_bf16 v[56:59], a[144:147], v[16:19], v[56:59]\n"
    "s_waitcnt lgkmcnt(5)\n"
    "v_mfma_f32_16x16x32_bf16 v[52:55], a[64:67], v[20:23], v[52:55]\n"
    "ds_read_b128 v[44:47], v88 offset:39936\n"
    "v_mfma_f32_16x16x32_bf16 v[60:63], a[144:147], v[20:23], v[60:63]\n"
    "s_waitcnt lgkmcnt(5)\n"
    "v_mfma_f32_16x16x32_bf16 v[48:51], a[68:71], v[24:27], v[48:51]\n"
    "s_waitcnt vmcnt(2)\n"
    "s_sub_u32 s42, s40, 1\n"
    "s_cmp_gt_u32 s40, 4\n"
    "s_cbranch_scc1 Qnw0_%=\n"
    "s_waitcnt vmcnt(0)\n"
    "s_mov_b32 s42, s40\n"
    "Qnw0_%=:\n"
    "s_barrier\n"
    "s_cmp_lg_u32 %[w], 0\n"
    "s_cbranch_scc1 Qnf00_%=\n"
    "v_mov_b32 v92, s42\n"
    "s_mov_b64 exec, 1\n"
    "global_store_dword v93, v92, s[56:57] offset:0 sc1\n"
    "s_mov_b64 exec, -1\n"
    "Qnf00_%=:\n"
    "s_sub_u32 s55, 122880, s53\n"
    "s_sub_u32 s55, s55, s54\n"
    "s_mov_b32 s53, s54\n"
    "s_mov_b32 s54, s55\n"
    "s_add_u32 s55, s55, s52\n"
    "ds_read_b128 v[16:19], v89 offset:0\n"
    "v_mfma_f32_16x16x32_bf16 v[56:59], a[148:151], v[24:27], v[56:59]\n"
    "s_waitcnt lgkmcnt(5)\n"
    "s_add_u32 m0, s55, 0\n"
    "v_mfma_f32_16x16x32_bf16 v[52:55], a[68:71], v[28:31], v[52:55]\n"
    "ds_read_b128 v[20:23], v89 offset:20480\n"
    "v_mfma_f32_16x16x32_bf16 v[60:63], a[148:151], v[28:31], v[60:63]\n"
    "s_waitcnt lgkmcnt(5)\n"
    "global_load_lds_dwordx4 %[vxv], s[46:47] offset:0 sc1\n"
    "v_mfma_f32_16x16x32_bf16 v[48:51], a[72:75], v[32:35], v[48:51]\n"
    "ds_read_b128 v[24:27], v89 offset:1024\n"
    "v_mfma_f32_16x16x32_bf16 v[56:59], a[152:155], v[32:35], v[56:59]\n"
    "s_waitcnt lgkmcnt(5)\n"
    "global_load_lds_dwordx4 %[vxv], s[46:47] offset:1024 sc1\n"
    "v_mfma_f32_16x16x32_bf16 v[52:55], a[72:75], v[36:39], v[52:55]\n"
    "ds_read_b128 v[28:31], v89 offset:21504\n"
    "v_mfma_f32_16x16x32_bf16 v[60:63], a[152:155], v[36:39], v[60:63]\n"
    "s_waitcnt lgkmcnt(5)\n"
    "global_load_lds_dwordx4 %[vxv], s[46:47] offset:2048 sc1\n"
    "v_mfma_f32_16x16x32_bf16 v[48:51], a[76:79], v[40:43], v[48:51]\n"
    "ds_read_b128 v[32:35], v89 offset:2048\n"
    "v_mfma_f32_16x16x32_bf16 v[56:59], a[156:159], v[40:43], v[56:59]\n"
    "s_waitcnt lgkmcnt(5)\n"
    "global_load_lds_dwordx4 %[vxv], s[46:47] offset:3072 sc1\n"
    "v_mfma_f32_16x16x32_bf16 v[52:55], a[76:79], v[44:47], v[52:55]\n"
    "ds_read_b128 v[36:39], v89 offset:22528\n"
    "v_mfma_f32_16x16x32_bf16 v[60:63], a[156:159], v[44:47], v[60:63]\n"
    "s_add_u32 s46, s46, 4096\n"
    "s_addc_u32 s47, s47, 0\n"
    "v_mov_b32 v88, v89\n"
    "v_add_u32 v89, s54, %[vlr]\n"
    "s_add_u32 s40, s40, 1\n"
    "s_cmp_lg_u32 s40, 9\n"
    "s_cbranch_scc1 Qloop_%=\n"
    "Qi3_%=:\n"
    "v_subrev_u32 v104, %[lb], %[vlr]\n"
    "s_cmp_lg_u32 %[w], 1\n"
    "s_cbranch_scc1 Qi4_%=\n"
    "v_lshlrev_b32 v105, 3, v104\n"
    "v_min_u32 v105, 5504, v105\n"
    "v_add_u32 v105, 2152448, v105\n"
    "s_branch Qi1b_%=\n"
    "Qi1_%=:\n"
    "s_sleep 4\n"
    "s_add_u32 s43, s43, 1\n"
    "s_cmp_gt_u32 s43, 524288\n"
    "s_cbranch_scc1 Qdead_%=\n"
    "Qi1b_%=:\n"
    "global_load_dwordx2 v[100:101], v105, %[ws] offset:0 sc1\n"
    "s_waitcnt vmcnt(0)\n"
    "v_mov_b32 v102, v101\n"
    "v_and_b32 v101, -8, v101\n"
    "v_cmp_ne_u64 vcc, s[64:65], v[100:101]\n"
    "s_cbranch_vccnz Qi1_%=\n"
    "Qi4_%=:\n"
    "s_cmp_lg_u32 %[w], 2\n"
    "s_cbranch_scc1 Qloop_%=\n"
    "v_lshlrev_b32 v105, 3, v104\n"
    "v_min_u32 v105, 5504, v105\n"
    "v_add_u32 v105, 2158080, v105\n"
    "s_branch Qi5b_%=\n"
    "Qi5_%=:\n"
    "s_sleep 4\n"
    "s_add_u32 s43, s43, 1\n"
    "s_cmp_gt_u32 s43, 524288\n"
    "s_cbranch_scc1 Qdead_%=\n"
    "Qi5b_%=:\n"
    "global_load_dwordx2 v[100:101], v105, %[ws] offset:0 sc1\n"
    "s_waitcnt vmcnt(0)\n"
    "v_mov_b32 v102, v101\n"
    "v_and_b32 v101, -8, v101\n"
    "v_cmp_ne_u64 vcc, s[64:65], v[100:101]\n"
    "s_cbranch_vccnz Qi5_%=\n"
    "s_branch Qloop_%=\n"
    "Qdone_%=:\n"
    "s_waitcnt lgkmcnt(0)\n"
    "s_nop 7\n"
    "s_add_u32 s50, s48, 4096\n"
    "s_addc_u32 s51, s49, 0\n"
    "v_cvt_pk_bf16_f32 v80, v64, v65\n"
    "v_cvt_pk_bf16_f32 v81, v66, v67\n"
    "v_cvt_pk_bf16_f32 v82, v72, v73\n"
    "v_cvt_pk_bf16_f32 v83, v74, v75\n"
    "global_store_dwordx4 %[vovo], v[80:83], s[48:49] offset:0 sc1\n"
    "v_cvt_pk_bf16_f32 v84, v68, v69\n"
    "v_cvt_pk_bf16_f32 v85, v70, v71\n"
    "v_cvt_pk_bf16_f32 v86, v76, v77\n"
    "v_cvt_pk_bf16_f32 v87, v78, v79\n"
    "global_store_dwordx4 %[vovo], v[84:87], s[50:51] offset:0 sc1\n"
    "s_add_u32 s48, s48, 524288\n"
    "s_addc_u32 s49, s49, 0\n"
    "s_and_b32 s41, s48, 0x180000\n"
    "s_cselect_b32 s41, 0, 8192\n"
    "s_xor_b32 s48, s48, s41\n"
    "s_waitcnt vmcnt(0)\n"
    "s_barrier\n"
    "s_cmp_lg_u32 %[w], 0\n"
    "s_cbranch_scc1 Qend_%=\n"
    "v_mov_b32 v92, 64\n"
    "s_mov_b64 exec, 1\n"
    "global_store_dword v93, v92, s[56:57] offset:0 sc1\n"
    "s_mov_b64 exec, -1\n"
    "s_branch Qend_%=\n"
    "Qdead_%=:\n"
    "v_mov_b32 v254, 1024\n"
    "v_mov_b32 v255, 2098432\n"
    "global_store_dword v255, v254, %[ws] offset:0 sc1\n"
    "s_waitcnt vmcnt(0)\n"
    "s_endpgm\n"
    "Qend_%=:\n"
    "s_mov_b32 m0, s99\n"
    QKV_OPS);
  }
}

template <bool WO>
DI void role_proj(const Params& p, int c, int t, lptr L, int lane, int w) {
  const u32 lb = __builtin_amdgcn_readfirstlane((u32)(size_t)L);
  const int G = lane >> 4, tok = 4 * w + G, pr = lane & 15;
  constexpr int RW = 272, PB = WO ? 131072 : 0;
  const int k0 = WO ? 32 * w : (w == 0 ? 0 : (w == 1 ? 14 : (w == 2 ? 27 : 40)));
  const u32 rd = lb + w * 32768 + G * 256 + ((lane >> 2) & 3) * 64 + (lane & 3) * 8;
  const size_t ro = ((size_t)t * 16 * DM + c * 32) * 2;
#define PROJ_OPS \
    : \
    : [vw] "v"(wlane(lane, 1280, 4)), [vrd0] "v"(rd + (G & 1) * 32), [vrd1] "v"(rd + ((G & 1) ^ 1) * 32), [vg16] "v"(lane * 16), \
      [vflv] "v"(WO ? (lane & 31) * 128 : min(lane, 53) * 128), [vwr] "v"(lb + PB + (w * 16 + pr) * RW + G * 16), [vrr] "v"(lb + PB + tok * RW + pr * 16), \
      [vhv] "v"((pr >> 1) * 256 + tok * 16 + (pr & 1) * 8), [vxv] "v"(tok * (DM * 2) + pr * 8), [vsv] "v"((pr >> 3) * 128 + tok * 8), \
      [ws] "s"(p.ws), [wp] "s"((const char*)(WO ? p.wo : p.wd) + ((size_t)k0 * 32 * DM + c * 32) * 2), \
      [ag] "s"(WO ? p.ws + O_OF + (size_t)(t * 128 + k0) * 2048 : p.ws + O_ACT + (size_t)(t * 54 + k0) * 1024), [hg] "s"(p.ws + O_HF + (t * 20 + c) * 1024), \
      [xg] "s"(WO ? (const char*)p.x + ro : (const char*)p.out + ro), [fo] "s"(p.ws + F_H + (t * 10 + (c >> 1)) * 128), [w] "s"(w), [lb] "s"(lb), \
      [sg] "s"(p.ws + O_ST + (t * 20 + c) * 128), [rot] "s"((c >> 1) & 7), [u0] "s"(__builtin_amdgcn_readfirstlane(t)), [last] "s"(__builtin_amdgcn_readfirstlane(c == 0 && t == 1 && w == 0)) \
    : "memory", "vcc", "scc", "v16", "v17", "v18", "v19", "v20", "v21", "v22", "v23", "v24", "v25", "v26", "v27", "v28", "v29", "v30", "v31", "v32", "v33", "v34", "v35", "v36", "v37", "v38", "v39", "v40", "v41", "v42", "v43", "v44", "v45", "v46", "v47", "v48", "v49", "v50", "v51", "v52", "v53", "v54", "v55", "v56", "v57", "v58", "v59", "v60", "v61", "v62", "v63", "v64", "v65", "v66", "v67", "v68", "v69", "v70", "v71", "v72", "v73", "v74", "v75", "v76", "v77", "v78", "v79", "v80", "v81", "v82", "v83", "v84", "v85", "v86", "v87", "v88", "v89", "v90", "v91", "v92", "v93", "v94", "v95", "v96", "v97", "v98", "v99", "v100", "v101", "v102", "v103", "v104", "v105", "v106", "v107", "v108", "v109", "v110", "v111", "v112", "v113", "v114", "v115", "v116", "v117", "v118", "v119", "v120", "v121", "v122", "v123", "v124", "v125", "v126", "v127", "v128", "v129", "v130", "v131", "v132", "v133", "v134", "v135", "v136", "v137", "v138", "v139", "v140", "v141", "v142", "v143", "v144", "v145", "v146", "v147", "v148", "v149", "v150", "v151", "v152", "v153", "v154", "v155", "v156", "v157", "v158", "v159", "v160", "v161", "v162", "v163", "v164", "v165", "v166", "v167", "v168", "v169", "v170", "v171", "v172", "v173", "v174", "v175", "v176", "v177", "v178", "v179", "v180", "v181", "v182", "v183", "v184", "v185", "v186", "v187", "v188", "v189", "v190", "v191", "v192", "v193", "v194", "v195", "v196", "v197", "v198", "v199", "v200", "v201", "v202", "v203", "v204", "v205", "v206", "v207", "v208", "v209", "v210", "v211", "v212", "v213", "v214", "v215", "v216", "v217", "v218", "v219", "v220", "v221", "v222", "v223", "v224", "v225", "v226", "v227", "v228", "v229", "v230", "v231", "v232", "v233", "v234", "v235", "v236", "v237", "v238", "v239", "v240", "v241", "v242", "v243", "v244", "v245", "v246", "v247", "v248", "v249", "v250", "v251", "v252", "v253", "v254", "v255", "a0", "a1", "a2", "a3", "a4", "a5", "a6", "a7", "a8", "a9", "a10", "a11", "a12", "a13", "a14", "a15", "a16", "a17", "a18", "a19", "a20", "a21", "a22", "a23", "a24", "a25", "a26", "a27", "a28", "a29", "a30", "a31", "a32", "a33", "a34", "a35", "a36", "a37", "a38", "a39", "a40", "a41", "a42", "a43", "a44", "a45", "a46", "a47", "a48", "a49", "a50", "a51", "a52", "a53", "a54", "a55", "a56", "a57", "a58", "a59", "a60", "a61", "a62", "a63", "a64", "a65", "a66", "a67", "a68", "a69", "a70", "a71", "a72", "a73", "a74", "a75", "a76", "a77", "a78", "a79", "a80", "a81", "a82", "a83", "a84", "a85", "a86", "a87", "a88", "a89", "a90", "a91", "a92", "a93", "a94", "a95", "a96", "a97", "a98", "a99", "a100", "a101", "a102", "a103", "a104", "a105", "a106", "a107", "a108", "a109", "a110", "a111", "a112", "a113", "a114", "a115", "a116", "a117", "a118", "a119", "a120", "a121", "a122", "a123", "a124", "a125", "a126", "a127", "a128", "a129", "a130", "a131", "a132", "a133", "a134", "a135", "a136", "a137", "a138", "a139", "a140", "a141", "a142", "a143", "a144", "a145", "a146", "a147", "a148", "a149", "a150", "a151", "a152", "a153", "a154", "a155", "a156", "a157", "a158", "a159", "a160", "a161", "a162", "a163", "a164", "a165", "a166", "a167", "a168", "a169", "a170", "a171", "a172", "a173", "a174", "a175", "a176", "a177", "a178", "a179", "a180", "a181", "a182", "a183", "a184", "a185", "a186", "a187", "a188", "a189", "a190", "a191", "a192", "a193", "a194", "a195", "a196", "a197", "a198", "a199", "a200", "a201", "a202", "a203", "a204", "a205", "a206", "a207", "a208", "a209", "a210", "a211", "a212", "a213", "a214", "a215", "a216", "a217", "a218", "a219", "a220", "a221", "a222", "a223", "a224", "a225", "a226", "a227", "a228", "a229", "a230", "a231", "a232", "a233", "a234", "a235", "a236", "a237", "a238", "a239", "a240", "a241", "a242", "a243", "a244", "a245", "a246", "a247", "a248", "a249", "a250", "a251", "a252", "a253", "a254", "a255", "s40", "s41", "s42", "s43", "s44", "s45", "s46", "s47", "s48", "s49", "s50", "s51", "s52", "s53", "s54", "s55", "s56", "s57", "s58", "s59", "s60", "s61", "s62", "s63", "s64", "s65", "s66", "s67", "s99"
  if (WO) {
    asm volatile(
    "s_mov_b32 s99, m0\n"
    "s_barrier\n"
    "s_lshl_b32 s60, %[w], 15\n"
    "s_add_u32 s60, s60, %[lb]\n"
    "v_add_u32 v230, 19456, %[vw]\n"
    "v_add_u32 v231, s60, %[vg16]\n"
    "s_mov_b64 s[54:55], %[wp]\n"
    "s_add_u32 s41, %[rot], 0\n"
    "s_and_b32 s41, s41, 7\n"
    "s_mul_i32 s41, s41, 163840\n"
    "s_add_u32 s41, s41, 0\n"
    "s_add_u32 s44, s54, s41\n"
    "s_addc_u32 s45, s55, 0\n"
    "s_add_u32 m0, s60, 0\n"
    "s_nop 0\n"
    "global_load_lds_dwordx4 %[vw], s[44:45] offset:0\n"
    "global_load_lds_dwordx4 v230, s[44:45] offset:1024\n"
    "s_add_u32 s44, s44, 64\n"
    "s_addc_u32 s45, s45, 0\n"
    "s_add_u32 m0, s60, 2048\n"
    "s_nop 0\n"
    "global_load_lds_dwordx4 %[vw], s[44:45] offset:0\n"
    "global_load_lds_dwordx4 v230, s[44:45] offset:1024\n"
    "s_add_u32 s44, s44, 64\n"
    "s_addc_u32 s45, s45, 0\n"
    "s_add_u32 s41, %[rot], 0\n"
    "s_and_b32 s41, s41, 7\n"
    "s_mul_i32 s41, s41, 163840\n"
    "s_add_u32 s41, s41, 40960\n"
    "s_add_u32 s44, s54, s41\n"
    "s_addc_u32 s45, s55, 0\n"
    "s_add_u32 m0, s60, 4096\n"
    "s_nop 0\n"
    "global_load_lds_dwordx4 %[vw], s[44:45] offset:0\n"
    "global_load_lds_dwordx4 v230, s[44:45] offset:1024\n"
    "s_add_u32 s44, s44, 64\n"
    "s_addc_u32 s45, s45, 0\n"
    "s_add_u32 m0, s60, 6144\n"
    "s_nop 0\n"
    "global_load_lds_dwordx4 %[vw], s[44:45] offset:0\n"
    "global_load_lds_dwordx4 v230, s[44:45] offset:1024\n"
    "s_add_u32 s44, s44, 64\n"
    "s_addc_u32 s45, s45, 0\n"
    "s_add_u32 s41, %[rot], 0\n"
    "s_and_b32 s41, s41, 7\n"
    "s_mul_i32 s41, s41, 163840\n"
    "s_add_u32 s41, s41, 81920\n"
    "s_add_u32 s44, s54, s41\n"
    "s_addc_u32 s45, s55, 0\n"
    "s_add_u32 m0, s60, 8192\n"
    "s_nop 0\n"
    "global_load_lds_dwordx4 %[vw], s[44:45] offset:0\n"
    "global_load_lds_dwordx4 v230, s[44:45] offset:1024\n"
    "s_add_u32 s44, s44, 64\n"
    "s_addc_u32 s45, s45, 0\n"
    "s_add_u32 m0, s60, 10240\n"
    "s_nop 0\n"
    "global_load_lds_dwordx4 %[vw], s[44:45] offset:0\n"
    "global_load_lds_dwordx4 v230, s[44:45] offset:1024\n"
    "s_add_u32 s44, s44, 64\n"
    "s_addc_u32 s45, s45, 0\n"
    "s_add_u32 s41, %[rot], 1\n"
    "s_and_b32 s41, s41, 7\n"
    "s_mul_i32 s41, s41, 163840\n"
    "s_add_u32 s41, s41, 0\n"
    "s_add_u32 s44, s54, s41\n"
    "s_addc_u32 s45, s55, 0\n"
    "s_add_u32 m0, s60, 12288\n"
    "s_nop 0\n"
    "global_load_lds_dwordx4 %[vw], s[44:45] offset:0\n"
    "global_load_lds_dwordx4 v230, s[44:45] offset:1024\n"
    "s_add_u32 s44, s44, 64\n"
    "s_addc_u32 s45, s45, 0\n"
    "s_add_u32 m0, s60, 14336\n"
    "s_nop 0\n"
    "global_load_lds_dwordx4 %[vw], s[44:45] offset:0\n"
    "global_load_lds_dwordx4 v230, s[44:45] offset:1024\n"
    "s_add_u32 s44, s44, 64\n"
    "s_addc_u32 s45, s45, 0\n"
    "s_waitcnt vmcnt(14)\n"
    "ds_read_b64_tr_b16 a[0:1], %[vrd0] offset:0\n"
    "ds_read_b64_tr_b16 a[2:3], %[vrd0] offset:1024\n"
    "ds_read_b64_tr_b16 a[4:5], %[vrd1] offset:0\n"
    "ds_read_b64_tr_b16 a[6:7], %[vrd1] offset:1024\n"
    "s_waitcnt vmcnt(12)\n"
    "ds_read_b64_tr_b16 a[8:9], %[vrd0] offset:2048\n"
    "ds_read_b64_tr_b16 a[10:11], %[vrd0] offset:3072\n"
    "ds_read_b64_tr_b16 a[12:13], %[vrd1] offset:2048\n"
    "ds_read_b64_tr_b16 a[14:15], %[vrd1] offset:3072\n"
    "s_waitcnt lgkmcnt(4)\n"
    "s_add_u32 s41, %[rot], 1\n"
    "s_and_b32 s41, s41, 7\n"
    "s_mul_i32 s41, s41, 163840\n"
    "s_add_u32 s41, s41, 40960\n"
    "s_add_u32 s44, s54, s41\n"
    "s_addc_u32 s45, s55, 0\n"
    "s_add_u32 m0, s60, 16384\n"
    "s_nop 0\n"
    "global_load_lds_dwordx4 %[vw], s[44:45] offset:0\n"
    "global_load_lds_dwordx4 v230, s[44:45] offset:1024\n"
    "s_add_u32 s44, s44, 64\n"
    "s_addc_u32 s45, s45, 0\n"
    "s_waitcnt vmcnt(12)\n"
    "ds_read_b64_tr_b16 a[16:17], %[vrd0] offset:4096\n"
    "ds_read_b64_tr_b16 a[18:19], %[vrd0] offset:5120\n"
    "ds_read_b64_tr_b16 a[20:21], %[vrd1] offset:4096\n"
    "ds_read_b64_tr_b16 a[22:23], %[vrd1] offset:5120\n"
    "s_waitcnt lgkmcnt(4)\n"
    "s_add_u32 m0, s60, 18432\n"
    "s_nop 0\n"
    "global_load_lds_dwordx4 %[vw], s[44:45] offset:0\n"
    "global_load_lds_dwordx4 v230, s[44:45] offset:1024\n"
    "s_add_u32 s44, s44, 64\n"
    "s_addc_u32 s45, s45, 0\n"
    "s_waitcnt vmcnt(12)\n"
    "ds_read_b64_tr_b16 a[24:25], %[vrd0] offset:6144\n"
    "ds_read_b64_tr_b16 a[26:27], %[vrd0] offset:7168\n"
    "ds_read_b64_tr_b16 a[28:29], %[vrd1] offset:6144\n"
    "ds_read_b64_tr_b16 a[30:31], %[vrd1] offset:7168\n"
    "s_waitcnt lgkmcnt(4)\n"
    "s_add_u32 s41, %[rot], 1\n"
    "s_and_b32 s41, s41, 7\n"
    "s_mul_i32 s41, s41, 163840\n"
    "s_add_u32 s41, s41, 81920\n"
    "s_add_u32 s44, s54, s41\n"
    "s_addc_u32 s45, s55, 0\n"
    "s_add_u32 m0, s60, 20480\n"
    "s_nop 0\n"
    "global_load_lds_dwordx4 %[vw], s[44:45] offset:0\n"
    "global_load_lds_dwordx4 v230, s[44:45] offset:1024\n"
    "s_add_u32 s44, s44, 64\n"
    "s_addc_u32 s45, s45, 0\n"
    "s_waitcnt vmcnt(12)\n"
    "ds_read_b64_tr_b16 a[32:33], %[vrd0] offset:8192\n"
    "ds_read_b64_tr_b16 a[34:35], %[vrd0] offset:9216\n"
    "ds_read_b64_tr_b16 a[36:37], %[vrd1] offset:8192\n"
    "ds_read_b64_tr_b16 a[38:39], %[vrd1] offset:9216\n"
    "s_waitcnt lgkmcnt(4)\n"
    "s_add_u32 m0, s60, 22528\n"
    "s_nop 0\n"
    "global_load_lds_dwordx4 %[vw], s[44:45] offset:0\n"
    "global_load_lds_dwordx4 v230, s[44:45] offset:1024\n"
    "s_add_u32 s44, s44, 64\n"
    "s_addc_u32 s45, s45, 0\n"
    "s_waitcnt vmcnt(12)\n"
    "ds_read_b64_tr_b16 a[40:41], %[vrd0] offset:10240\n"
    "ds_read_b64_tr_b16 a[42:43], %[vrd0] offset:11264\n"
    "ds_read_b64_tr_b16 a[44:45], %[vrd1] offset:10240\n"
    "ds_read_b64_tr_b16 a[46:47], %[vrd1] offset:11264\n"
    "s_waitcnt lgkmcnt(4)\n"
    "s_add_u32 s41, %[rot], 2\n"
    "s_and_b32 s41, s41, 7\n"
    "s_mul_i32 s41, s41, 163840\n"
    "s_add_u32 s41, s41, 0\n"
    "s_add_u32 s44, s54, s41\n"
    "s_addc_u32 s45, s55, 0\n"
    "s_add_u32 m0, s60, 24576\n"
    "s_nop 0\n"
    "global_load_lds_dwordx4 %[vw], s[44:45] offset:0\n"
    "global_load_lds_dwordx4 v230, s[44:45] offset:1024\n"
    "s_add_u32 s44, s44, 64\n"
    "s_addc_u32 s45, s45, 0\n"
    "s_waitcnt vmcnt(12)\n"
    "ds_read_b64_tr_b16 a[48:49], %[vrd0] offset:12288\n"
    "ds_read_b64_tr_b16 a[50:51], %[vrd0] offset:13312\n"
    "ds_read_b64_tr_b16 a[52:53], %[vrd1] offset:12288\n"
    "ds_read_b64_tr_b16 a[54:55], %[vrd1] offset:13312\n"
    "s_waitcnt lgkmcnt(4)\n"
    "s_add_u32 m0, s60, 26624\n"
    "s_nop 0\n"
    "global_load_lds_dwordx4 %[vw], s[44:45] offset:0\n"
    "global_load_lds_dwordx4 v230, s[44:45] offset:1024\n"
    "s_add_u32 s44, s44, 64\n"
    "s_addc_u32 s45, s45, 0\n"
    "s_waitcnt vmcnt(12)\n"
    "ds_read_b64_tr_b16 a[56:57], %[vrd0] offset:14336\n"
    "ds_read_b64_tr_b16 a[58:59], %[vrd0] offset:15360\n"
    "ds_read_b64_tr_b16 a[60:61], %[vrd1] offset:14336\n"
    "ds_read_b64_tr_b16 a[62:63], %[vrd1] offset:15360\n"
    "s_waitcnt lgkmcnt(4)\n"
    "s_add_u32 s41, %[rot], 2\n"
    "s_and_b32 s41, s41, 7\n"
    "s_mul_i32 s41, s41, 163840\n"
    "s_add_u32 s41, s41, 40960\n"
    "s_add_u32 s44, s54, s41\n"
    "s_addc_u32 s45, s55, 0\n"
    "s_add_u32 m0, s60, 28672\n"
    "s_nop 0\n"
    "global_load_lds_dwordx4 %[vw], s[44:45] offset:0\n"
    "global_load_lds_dwordx4 v230, s[44:45] offset:1024\n"
    "s_add_u32 s44, s44, 64\n"
    "s_addc_u32 s45, s45, 0\n"
    "s_waitcnt vmcnt(12)\n"
    "ds_read_b64_tr_b16 a[64:65], %[vrd0] offset:16384\n"
    "ds_read_b64_tr_b16 a[66:67], %[vrd0] offset:17408\n"
    "ds_read_b64_tr_b16 a[68:69], %[vrd1] offset:16384\n"
    "ds_read_b64_tr_b16 a[70:71], %[vrd1] offset:17408\n"
    "s_waitcnt lgkmcnt(4)\n"
    "s_add_u32 m0, s60, 30720\n"
    "s_nop 0\n"
    "global_load_lds_dwordx4 %[vw], s[44:45] offset:0\n"
    "global_load_lds_dwordx4 v230, s[44:45] offset:1024\n"
    "s_add_u32 s44, s44, 64\n"
    "s_addc_u32 s45, s45, 0\n"
    "s_waitcnt vmcnt(12)\n"
    "ds_read_b64_tr_b16 a[72:73], %[vrd0] offset:18432\n"
    "ds_read_b64_tr_b16 a[74:75], %[vrd0] offset:19456\n"
    "ds_read_b64_tr_b16 a[76:77], %[vrd1] offset:18432\n"
    "ds_read_b64_tr_b16 a[78:79], %[vrd1] offset:19456\n"
    "s_waitcnt lgkmcnt(4)\n"
    "s_add_u32 s41, %[rot], 2\n"
    "s_and_b32 s41, s41, 7\n"
    "s_mul_i32 s41, s41, 163840\n"
    "s_add_u32 s41, s41, 81920\n"
    "s_add_u32 s44, s54, s41\n"
    "s_addc_u32 s45, s55, 0\n"
    "s_add_u32 m0, s60, 0\n"
    "s_nop 0\n"
    "global_load_lds_dwordx4 %[vw], s[44:45] offset:0\n"
    "global_load_lds_dwordx4 v230, s[44:45] offset:1024\n"
    "s_add_u32 s44, s44, 64\n"
    "s_addc_u32 s45, s45, 0\n"
    "s_waitcnt vmcnt(12)\n"
    "ds_read_b64_tr_b16 a[80:81], %[vrd0] offset:20480\n"
    "ds_read_b64_tr_b16 a[82:83], %[vrd0] offset:21504\n"
    "ds_read_b64_tr_b16 a[84:85], %[vrd1] offset:20480\n"
    "ds_read_b64_tr_b16 a[86:87], %[vrd1] offset:21504\n"
    "s_waitcnt lgkmcnt(4)\n"
    "s_add_u32 m0, s60, 2048\n"
    "s_nop 0\n"
    "global_load_lds_dwordx4 %[vw], s[44:45] offset:0\n"
    "global_load_lds_dwordx4 v230, s[44:45] offset:1024\n"
    "s_add_u32 s44, s44, 64\n"
    "s_addc_u32 s45, s45, 0\n"
    "s_waitcnt vmcnt(12)\n"
    "ds_read_b64_tr_b16 a[88:89], %[vrd0] offset:22528\n"
    "ds_read_b64_tr_b16 a[90:91], %[vrd0] offset:23552\n"
    "ds_read_b64_tr_b16 a[92:93], %[vrd1] offset:22528\n"
    "ds_read_b64_tr_b16 a[94:95], %[vrd1] offset:23552\n"
    "s_waitcnt lgkmcnt(4)\n"
    "s_add_u32 s41, %[rot], 3\n"
    "s_and_b32 s41, s41, 7\n"
    "s_mul_i32 s41, s41, 163840\n"
    "s_add_u32 s41, s41, 0\n"
    "s_add_u32 s44, s54, s41\n"
    "s_addc_u32 s45, s55, 0\n"
    "s_add_u32 m0, s60, 4096\n"
    "s_nop 0\n"
    "global_load_lds_dwordx4 %[vw], s[44:45] offset:0\n"
    "global_load_lds_dwordx4 v230, s[44:45] offset:1024\n"
    "s_add_u32 s44, s44, 64\n"
    "s_addc_u32 s45, s45, 0\n"
    "s_waitcnt vmcnt(12)\n"
    "ds_read_b64_tr_b16 a[96:97], %[vrd0] offset:24576\n"
    "ds_read_b64_tr_b16 a[98:99], %[vrd0] offset:25600\n"
    "ds_read_b64_tr_b16 a[100:101], %[vrd1] offset:24576\n"
    "ds_read_b64_tr_b16 a[102:103], %[vrd1] offset:25600\n"
    "s_waitcnt lgkmcnt(4)\n"
    "s_add_u32 m0, s60, 6144\n"
    "s_nop 0\n"
    "global_load_lds_dwordx4 %[vw], s[44:45] offset:0\n"
    "global_load_lds_dwordx4 v230, s[44:45] offset:1024\n"
    "s_add_u32 s44, s44, 64\n"
    "s_addc_u32 s45, s45, 0\n"
    "s_waitcnt vmcnt(12)\n"
    "ds_read_b64_tr_b16 a[104:105], %[vrd0] offset:26624\n"
    "ds_read_b64_tr_b16 a[106:107], %[vrd0] offset:27648\n"
    "ds_read_b64_tr_b16 a[108:109], %[vrd1] offset:26624\n"
    "ds_read_b64_tr_b16 a[110:111], %[vrd1] offset:27648\n"
    "s_waitcnt lgkmcnt(4)\n"
    "s_add_u32 s41, %[rot], 3\n"
    "s_and_b32 s41, s41, 7\n"
    "s_mul_i32 s41, s41, 163840\n"
    "s_add_u32 s41, s41, 40960\n"
    "s_add_u32 s44, s54, s41\n"
    "s_addc_u32 s45, s55, 0\n"
    "s_add_u32 m0, s60, 8192\n"
    "s_nop 0\n"
    "global_load_lds_dwordx4 %[vw], s[44:45] offset:0\n"
    "global_load_lds_dwordx4 v230, s[44:45] offset:1024\n"
    "s_add_u32 s44, s44, 64\n"
    "s_addc_u32 s45, s45, 0\n"
    "s_waitcnt vmcnt(12)\n"
    "ds_read_b64_tr_b16 a[112:113], %[vrd0] offset:28672\n"
    "ds_read_b64_tr_b16 a[114:115], %[vrd0] offset:29696\n"
    "ds_read_b64_tr_b16 a[116:117], %[vrd1] offset:28672\n"
    "ds_read_b64_tr_b16 a[118:119], %[vrd1] offset:29696\n"
    "s_waitcnt lgkmcnt(4)\n"
    "s_add_u32 m0, s60, 10240\n"
    "s_nop 0\n"
    "global_load_lds_dwordx4 %[vw], s[44:45] offset:0\n"
    "global_load_lds_dwordx4 v230, s[44:45] offset:1024\n"
    "s_add_u32 s44, s44, 64\n"
    "s_addc_u32 s45, s45, 0\n"
    "s_waitcnt vmcnt(12)\n"
    "ds_read_b64_tr_b16 a[120:121], %[vrd0] offset:30720\n"
    "ds_read_b64_tr_b16 a[122:123], %[vrd0] offset:31744\n"
    "ds_read_b64_tr_b16 a[124:125], %[vrd1] offset:30720\n"
    "ds_read_b64_tr_b16 a[126:127], %[vrd1] offset:31744\n"
    "s_waitcnt lgkmcnt(4)\n"
    "s_add_u32 s41, %[rot], 3\n"
    "s_and_b32 s41, s41, 7\n"
    "s_mul_i32 s41, s41, 163840\n"
    "s_add_u32 s41, s41, 81920\n"
    "s_add_u32 s44, s54, s41\n"
    "s_addc_u32 s45, s55, 0\n"
    "s_add_u32 m0, s60, 12288\n"
    "s_nop 0\n"
    "global_load_lds_dwordx4 %[vw], s[44:45] offset:0\n"
    "global_load_lds_dwordx4 v230, s[44:45] offset:1024\n"
    "s_add_u32 s44, s44, 64\n"
    "s_addc_u32 s45, s45, 0\n"
    "s_waitcnt vmcnt(12)\n"
    "ds_read_b64_tr_b16 a[128:129], %[vrd0] offset:0\n"
    "ds_read_b64_tr_b16 a[130:131], %[vrd0] offset:1024\n"
    "ds_read_b64_tr_b16 a[132:133], %[vrd1] offset:0\n"
    "ds_read_b64_tr_b16 a[134:135], %[vrd1] offset:1024\n"
    "s_waitcnt lgkmcnt(4)\n"
    "s_add_u32 m0, s60, 14336\n"
    "s_nop 0\n"
    "global_load_lds_dwordx4 %[vw], s[44:45] offset:0\n"
    "global_load_lds_dwordx4 v230, s[44:45] offset:1024\n"
    "s_add_u32 s44, s44, 64\n"
    "s_addc_u32 s45, s45, 0\n"
    "s_waitcnt vmcnt(12)\n"
    "ds_read_b64_tr_b16 a[136:137], %[vrd0] offset:2048\n"
    "ds_read_b64_tr_b16 a[138:139], %[vrd0] offset:3072\n"
    "ds_read_b64_tr_b16 a[140:141], %[vrd1] offset:2048\n"
    "ds_read_b64_tr_b16 a[142:143], %[vrd1] offset:3072\n"
    "s_waitcnt lgkmcnt(4)\n"
    "s_add_u32 s41, %[rot], 4\n"
    "s_and_b32 s41, s41, 7\n"
    "s_mul_i32 s41, s41, 163840\n"
    "s_add_u32 s41, s41, 0\n"
    "s_add_u32 s44, s54, s41\n"
    "s_addc_u32 s45, s55, 0\n"
    "s_add_u32 m0, s60, 16384\n"
    "s_nop 0\n"
    "global_load_lds_dwordx4 %[vw], s[44:45] offset:0\n"
    "global_load_lds_dwordx4 v230, s[44:45] offset:1024\n"
    "s_add_u32 s44, s44, 64\n"
    "s_addc_u32 s45, s45, 0\n"
    "s_waitcnt vmcnt(12)\n"
    "ds_read_b64_tr_b16 a[144:145], %[vrd0] offset:4096\n"
    "ds_read_b64_tr_b16 a[146:147], %[vrd0] offset:5120\n"
    "ds_read_b64_tr_b16 a[148:149], %[vrd1] offset:4096\n"
    "ds_read_b64_tr_b16 a[150:151], %[vrd1] offset:5120\n"
    "s_waitcnt lgkmcnt(4)\n"
    "s_add_u32 m0, s60, 18432\n"
    "s_nop 0\n"
    "global_load_lds_dwordx4 %[vw], s[44:45] offset:0\n"
    "global_load_lds_dwordx4 v230, s[44:45] offset:1024\n"
    "s_add_u32 s44, s44, 64\n"
    "s_addc_u32 s45, s45, 0\n"
    "s_waitcnt vmcnt(12)\n"
    "ds_read_b64_tr_b16 a[152:153], %[vrd0] offset:6144\n"
    "ds_read_b64_tr_b16 a[154:155], %[vrd0] offset:7168\n"
    "ds_read_b64_tr_b16 a[156:157], %[vrd1] offset:6144\n"
    "ds_read_b64_tr_b16 a[158:159], %[vrd1] offset:7168\n"
    "s_waitcnt lgkmcnt(4)\n"
    "s_add_u32 s41, %[rot], 4\n"
    "s_and_b32 s41, s41, 7\n"
    "s_mul_i32 s41, s41, 163840\n"
    "s_add_u32 s41, s41, 40960\n"
    "s_add_u32 s44, s54, s41\n"
    "s_addc_u32 s45, s55, 0\n"
    "s_add_u32 m0, s60, 20480\n"
    "s_nop 0\n"
    "global_load_lds_dwordx4 %[vw], s[44:45] offset:0\n"
    "global_load_lds_dwordx4 v230, s[44:45] offset:1024\n"
    "s_add_u32 s44, s44, 64\n"
    "s_addc_u32 s45, s45, 0\n"
    "s_waitcnt vmcnt(12)\n"
    "ds_read_b64_tr_b16 a[160:161], %[vrd0] offset:8192\n"
    "ds_read_b64_tr_b16 a[162:163], %[vrd0] offset:9216\n"
    "ds_read_b64_tr_b16 a[164:165], %[vrd1] offset:8192\n"
    "ds_read_b64_tr_b16 a[166:167], %[vrd1] offset:9216\n"
    "s_waitcnt lgkmcnt(4)\n"
    "s_add_u32 m0, s60, 22528\n"
    "s_nop 0\n"
    "global_load_lds_dwordx4 %[vw], s[44:45] offset:0\n"
    "global_load_lds_dwordx4 v230, s[44:45] offset:1024\n"
    "s_add_u32 s44, s44, 64\n"
    "s_addc_u32 s45, s45, 0\n"
    "s_waitcnt vmcnt(12)\n"
    "ds_read_b64_tr_b16 a[168:169], %[vrd0] offset:10240\n"
    "ds_read_b64_tr_b16 a[170:171], %[vrd0] offset:11264\n"
    "ds_read_b64_tr_b16 a[172:173], %[vrd1] offset:10240\n"
    "ds_read_b64_tr_b16 a[174:175], %[vrd1] offset:11264\n"
    "s_waitcnt lgkmcnt(4)\n"
    "s_add_u32 s41, %[rot], 4\n"
    "s_and_b32 s41, s41, 7\n"
    "s_mul_i32 s41, s41, 163840\n"
    "s_add_u32 s41, s41, 81920\n"
    "s_add_u32 s44, s54, s41\n"
    "s_addc_u32 s45, s55, 0\n"
    "s_add_u32 m0, s60, 24576\n"
    "s_nop 0\n"
    "global_load_lds_dwordx4 %[vw], s[44:45] offset:0\n"
    "global_load_lds_dwordx4 v230, s[44:45] offset:1024\n"
    "s_add_u32 s44, s44, 64\n"
    "s_addc_u32 s45, s45, 0\n"
    "s_waitcnt vmcnt(12)\n"
    "ds_read_b64_tr_b16 a[176:177], %[vrd0] offset:12288\n"
    "ds_read_b64_tr_b16 a[178:179], %[vrd0] offset:13312\n"
    "ds_read_b64_tr_b16 a[180:181], %[vrd1] offset:12288\n"
    "ds_read_b64_tr_b16 a[182:183], %[vrd1] offset:13312\n"
    "s_waitcnt lgkmcnt(4)\n"
    "s_add_u32 m0, s60, 26624\n"
    "s_nop 0\n"
    "global_load_lds_dwordx4 %[vw], s[44:45] offset:0\n"
    "global_load_lds_dwordx4 v230, s[44:45] offset:1024\n"
    "s_add_u32 s44, s44, 64\n"
    "s_addc_u32 s45, s45, 0\n"
    "s_waitcnt vmcnt(12)\n"
    "ds_read_b64_tr_b16 a[184:185], %[vrd0] offset:14336\n"
    "ds_read_b64_tr_b16 a[186:187], %[vrd0] offset:15360\n"
    "ds_read_b64_tr_b16 a[188:189], %[vrd1] offset:14336\n"
    "ds_read_b64_tr_b16 a[190:191], %[vrd1] offset:15360\n"
    "s_waitcnt lgkmcnt(4)\n"
    "s_add_u32 s41, %[rot], 5\n"
    "s_and_b32 s41, s41, 7\n"
    "s_mul_i32 s41, s41, 163840\n"
    "s_add_u32 s41, s41, 0\n"
    "s_add_u32 s44, s54, s41\n"
    "s_addc_u32 s45, s55, 0\n"
    "s_add_u32 m0, s60, 28672\n"
    "s_nop 0\n"
    "global_load_lds_dwordx4 %[vw], s[44:45] offset:0\n"
    "global_load_lds_dwordx4 v230, s[44:45] offset:1024\n"
    "s_add_u32 s44, s44, 64\n"
    "s_addc_u32 s45, s45, 0\n"
    "s_waitcnt vmcnt(12)\n"
    "ds_read_b64_tr_b16 a[192:193], %[vrd0] offset:16384\n"
    "ds_read_b64_tr_b16 a[194:195], %[vrd0] offset:17408\n"
    "ds_read_b64_tr_b16 a[196:197], %[vrd1] offset:16384\n"
    "ds_read_b64_tr_b16 a[198:199], %[vrd1] offset:17408\n"
    "s_waitcnt lgkmcnt(4)\n"
    "s_add_u32 m0, s60, 30720\n"
    "s_nop 0\n"
    "global_load_lds_dwordx4 %[vw], s[44:45] offset:0\n"
    "global_load_lds_dwordx4 v230, s[44:45] offset:1024\n"
    "s_add_u32 s44, s44, 64\n"
    "s_addc_u32 s45, s45, 0\n"
    "s_waitcnt vmcnt(12)\n"
    "ds_read_b64_tr_b16 a[200:201], %[vrd0] offset:18432\n"
    "ds_read_b64_tr_b16 a[202:203], %[vrd0] offset:19456\n"
    "ds_read_b64_tr_b16 a[204:205], %[vrd1] offset:18432\n"
    "ds_read_b64_tr_b16 a[206:207], %[vrd1] offset:19456\n"
    "s_waitcnt lgkmcnt(4)\n"
    "s_add_u32 s41, %[rot], 5\n"
    "s_and_b32 s41, s41, 7\n"
    "s_mul_i32 s41, s41, 163840\n"
    "s_add_u32 s41, s41, 40960\n"
    "s_add_u32 s44, s54, s41\n"
    "s_addc_u32 s45, s55, 0\n"
    "s_add_u32 m0, s60, 0\n"
    "s_nop 0\n"
    "global_load_lds_dwordx4 %[vw], s[44:45] offset:0\n"
    "global_load_lds_dwordx4 v230, s[44:45] offset:1024\n"
    "s_add_u32 s44, s44, 64\n"
    "s_addc_u32 s45, s45, 0\n"
    "s_waitcnt vmcnt(12)\n"
    "ds_read_b64_tr_b16 a[208:209], %[vrd0] offset:20480\n"
    "ds_read_b64_tr_b16 a[210:211], %[vrd0] offset:21504\n"
    "ds_read_b64_tr_b16 a[212:213], %[vrd1] offset:20480\n"
    "ds_read_b64_tr_b16 a[214:215], %[vrd1] offset:21504\n"
    "s_waitcnt lgkmcnt(4)\n"
    "s_add_u32 m0, s60, 2048\n"
    "s_nop 0\n"
    "global_load_lds_dwordx4 %[vw], s[44:45] offset:0\n"
    "global_load_lds_dwordx4 v230, s[44:45] offset:1024\n"
    "s_add_u32 s44, s44, 64\n"
    "s_addc_u32 s45, s45, 0\n"
    "s_waitcnt vmcnt(12)\n"
    "ds_read_b64_tr_b16 a[216:217], %[vrd0] offset:22528\n"
    "ds_read_b64_tr_b16 a[218:219], %[vrd0] offset:23552\n"
    "ds_read_b64_tr_b16 a[220:221], %[vrd1] offset:22528\n"
    "ds_read_b64_tr_b16 a[222:223], %[vrd1] offset:23552\n"
    "s_waitcnt lgkmcnt(4)\n"
    "s_add_u32 s41, %[rot], 5\n"
    "s_and_b32 s41, s41, 7\n"
    "s_mul_i32 s41, s41, 163840\n"
    "s_add_u32 s41, s41, 81920\n"
    "s_add_u32 s44, s54, s41\n"
    "s_addc_u32 s45, s55, 0\n"
    "s_add_u32 m0, s60, 4096\n"
    "s_nop 0\n"
    "global_load_lds_dwordx4 %[vw], s[44:45] offset:0\n"
    "global_load_lds_dwordx4 v230, s[44:45] offset:1024\n"
    "s_add_u32 s44, s44, 64\n"
    "s_addc_u32 s45, s45, 0\n"
    "s_waitcnt vmcnt(12)\n"
    "ds_read_b64_tr_b16 a[224:225], %[vrd0] offset:24576\n"
    "ds_read_b64_tr_b16 a[226:227], %[vrd0] offset:25600\n"
    "ds_read_b64_tr_b16 a[228:229], %[vrd1] offset:24576\n"
    "ds_read_b64_tr_b16 a[230:231], %[vrd1] offset:25600\n"
    "s_waitcnt lgkmcnt(4)\n"
    "s_add_u32 m0, s60, 6144\n"
    "s_nop 0\n"
    "global_load_lds_dwordx4 %[vw], s[44:45] offset:0\n"
    "global_load_lds_dwordx4 v230, s[44:45] offset:1024\n"
    "s_add_u32 s44, s44, 64\n"
    "s_addc_u32 s45, s45, 0\n"
    "s_waitcnt vmcnt(12)\n"
    "ds_read_b64_tr_b16 a[232:233], %[vrd0] offset:26624\n"
    "ds_read_b64_tr_b16 a[234:235], %[vrd0] offset:27648\n"
    "ds_read_b64_tr_b16 a[236:237], %[vrd1] offset:26624\n"
    "ds_read_b64_tr_b16 a[238:239], %[vrd1] offset:27648\n"
    "s_waitcnt lgkmcnt(4)\n"
    "s_add_u32 s41, %[rot], 6\n"
    "s_and_b32 s41, s41, 7\n"
    "s_mul_i32 s41, s41, 163840\n"
    "s_add_u32 s41, s41, 0\n"
    "s_add_u32 s44, s54, s41\n"
    "s_addc_u32 s45, s55, 0\n"
    "s_add_u32 m0, s60, 8192\n"
    "s_nop 0\n"
    "global_load_lds_dwordx4 %[vw], s[44:45] offset:0\n"
    "global_load_lds_dwordx4 v230, s[44:45] offset:1024\n"
    "s_add_u32 s44, s44, 64\n"
    "s_addc_u32 s45, s45, 0\n"
    "s_waitcnt vmcnt(12)\n"
    "ds_read_b64_tr_b16 a[240:241], %[vrd0] offset:28672\n"
    "ds_read_b64_tr_b16 a[242:243], %[vrd0] offset:29696\n"
    "ds_read_b64_tr_b16 a[244:245], %[vrd1] offset:28672\n"
    "ds_read_b64_tr_b16 a[246:247], %[vrd1] offset:29696\n"
    "s_waitcnt lgkmcnt(4)\n"
    "s_add_u32 m0, s60, 10240\n"
    "s_nop 0\n"
    "global_load_lds_dwordx4 %[vw], s[44:45] offset:0\n"
    "global_load_lds_dwordx4 v230, s[44:45] offset:1024\n"
    "s_add_u32 s44, s44, 64\n"
    "s_addc_u32 s45, s45, 0\n"
    "s_waitcnt vmcnt(12)\n"
    "ds_read_b64_tr_b16 a[248:249], %[vrd0] offset:30720\n"
    "ds_read_b64_tr_b16 a[250:251], %[vrd0] offset:31744\n"
    "ds_read_b64_tr_b16 a[252:253], %[vrd1] offset:30720\n"
    "ds_read_b64_tr_b16 a[254:255], %[vrd1] offset:31744\n"
    "s_waitcnt lgkmcnt(4)\n"
    "s_add_u32 s41, %[rot], 6\n"
    "s_and_b32 s41, s41, 7\n"
    "s_mul_i32 s41, s41, 163840\n"
    "s_add_u32 s41, s41, 40960\n"
    "s_add_u32 s44, s54, s41\n"
    "s_addc_u32 s45, s55, 0\n"
    "s_add_u32 m0, s60, 12288\n"
    "s_nop 0\n"
    "global_load_lds_dwordx4 %[vw], s[44:45] offset:0\n"
    "global_load_lds_dwordx4 v230, s[44:45] offset:1024\n"
    "s_add_u32 s44, s44, 64\n"
    "s_addc_u32 s45, s45, 0\n"
    "s_waitcnt vmcnt(12)\n"
    "ds_read_b64_tr_b16 v[16:17], %[vrd0] offset:0\n"
    "ds_read_b64_tr_b16 v[18:19], %[vrd0] offset:1024\n"
    "ds_read_b64_tr_b16 v[20:21], %[vrd1] offset:0\n"
    "ds_read_b64_tr_b16 v[22:23], %[vrd1] offset:1024\n"
    "s_waitcnt lgkmcnt(4)\n"
    "s_add_u32 m0, s60, 14336\n"
    "s_nop 0\n"
    "global_load_lds_dwordx4 %[vw], s[44:45] offset:0\n"
    "global_load_lds_dwordx4 v230, s[44:45] offset:1024\n"
    "s_add_u32 s44, s44, 64\n"
    "s_addc_u32 s45, s45, 0\n"
    "s_waitcnt vmcnt(12)\n"
    "ds_read_b64_tr_b16 v[24:25], %[vrd0] offset:2048\n"
    "ds_read_b64_tr_b16 v[26:27], %[vrd0] offset:3072\n"
    "ds_read_b64_tr_b16 v[28:29], %[vrd1] offset:2048\n"
    "ds_read_b64_tr_b16 v[30:31], %[vrd1] offset:3072\n"
    "s_waitcnt lgkmcnt(4)\n"
    "s_add_u32 s41, %[rot], 6\n"
    "s_and_b32 s41, s41, 7\n"
    "s_mul_i32 s41, s41, 163840\n"
    "s_add_u32 s41, s41, 81920\n"
    "s_add_u32 s44, s54, s41\n"
    "s_addc_u32 s45, s55, 0\n"
    "s_add_u32 m0, s60, 16384\n"
    "s_nop 0\n"
    "global_load_lds_dwordx4 %[vw], s[44:45] offset:0\n"
    "global_load_lds_dwordx4 v230, s[44:45] offset:1024\n"
    "s_add_u32 s44, s44, 64\n"
    "s_addc_u32 s45, s45, 0\n"
    "s_waitcnt vmcnt(12)\n"
    "ds_read_b64_tr_b16 v[32:33], %[vrd0] offset:4096\n"
    "ds_read_b64_tr_b16 v[34:35], %[vrd0] offset:5120\n"
    "ds_read_b64_tr_b16 v[36:37], %[vrd1] offset:4096\n"
    "ds_read_b64_tr_b16 v[38:39], %[vrd1] offset:5120\n"
    "s_waitcnt lgkmcnt(4)\n"
    "s_add_u32 m0, s60, 18432\n"
    "s_nop 0\n"
    "global_load_lds_dwordx4 %[vw], s[44:45] offset:0\n"
    "global_load_lds_dwordx4 v230, s[44:45] offset:1024\n"
    "s_add_u32 s44, s44, 64\n"
    "s_addc_u32 s45, s45, 0\n"
    "s_waitcnt vmcnt(12)\n"
    "ds_read_b64_tr_b16 v[40:41], %[vrd0] offset:6144\n"
    "ds_read_b64_tr_b16 v[42:43], %[vrd0] offset:7168\n"
    "ds_read_b64_tr_b16 v[44:45], %[vrd1] offset:6144\n"
    "ds_read_b64_tr_b16 v[46:47], %[vrd1] offset:7168\n"
    "s_waitcnt lgkmcnt(4)\n"
    "s_add_u32 s41, %[rot], 7\n"
    "s_and_b32 s41, s41, 7\n"
    "s_mul_i32 s41, s41, 163840\n"
    "s_add_u32 s41, s41, 0\n"
    "s_add_u32 s44, s54, s41\n"
    "s_addc_u32 s45, s55, 0\n"
    "s_add_u32 m0, s60, 20480\n"
    "s_nop 0\n"
    "global_load_lds_dwordx4 %[vw], s[44:45] offset:0\n"
    "global_load_lds_dwordx4 v230, s[44:45] offset:1024\n"
    "s_add_u32 s44, s44, 64\n"
    "s_addc_u32 s45, s45, 0\n"
    "s_waitcnt vmcnt(12)\n"
    "ds_read_b64_tr_b16 v[48:49], %[vrd0] offset:8192\n"
    "ds_read_b64_tr_b16 v[50:51], %[vrd0] offset:9216\n"
    "ds_read_b64_tr_b16 v[52:53], %[vrd1] offset:8192\n"
    "ds_read_b64_tr_b16 v[54:55], %[vrd1] offset:9216\n"
    "s_waitcnt lgkmcnt(4)\n"
    "s_add_u32 m0, s60, 22528\n"
    "s_nop 0\n"
    "global_load_lds_dwordx4 %[vw], s[44:45] offset:0\n"
    "global_load_lds_dwordx4 v230, s[44:45] offset:1024\n"
    "s_add_u32 s44, s44, 64\n"
    "s_addc_u32 s45, s45, 0\n"
    "s_waitcnt vmcnt(12)\n"
    "ds_read_b64_tr_b16 v[56:57], %[vrd0] offset:10240\n"
    "ds_read_b64_tr_b16 v[58:59], %[vrd0] offset:11264\n"
    "ds_read_b64_tr_b16 v[60:61], %[vrd1] offset:10240\n"
    "ds_read_b64_tr_b16 v[62:63], %[vrd1] offset:11264\n"
    "s_waitcnt lgkmcnt(4)\n"
    "s_add_u32 s41, %[rot], 7\n"
    "s_and_b32 s41, s41, 7\n"
    "s_mul_i32 s41, s41, 163840\n"
    "s_add_u32 s41, s41, 40960\n"
    "s_add_u32 s44, s54, s41\n"
    "s_addc_u32 s45, s55, 0\n"
    "s_add_u32 m0, s60, 24576\n"
    "s_nop 0\n"
    "global_load_lds_dwordx4 %[vw], s[44:45] offset:0\n"
    "global_load_lds_dwordx4 v230, s[44:45] offset:1024\n"
    "s_add_u32 s44, s44, 64\n"
    "s_addc_u32 s45, s45, 0\n"
    "s_waitcnt vmcnt(12)\n"
    "ds_read_b64_tr_b16 v[64:65], %[vrd0] offset:12288\n"
    "ds_read_b64_tr_b16 v[66:67], %[vrd0] offset:13312\n"
    "ds_read_b64_tr_b16 v[68:69], %[vrd1] offset:12288\n"
    "ds_read_b64_tr_b16 v[70:71], %[vrd1] offset:13312\n"
    "s_waitcnt lgkmcnt(4)\n"
    "s_add_u32 m0, s60, 26624\n"
    "s_nop 0\n"
    "global_load_lds_dwordx4 %[vw], s[44:45] offset:0\n"
    "global_load_lds_dwordx4 v230, s[44:45] offset:1024\n"
    "s_add_u32 s44, s44, 64\n"
    "s_addc_u32 s45, s45, 0\n"
    "s_waitcnt vmcnt(12)\n"
    "ds_read_b64_tr_b16 v[72:73], %[vrd0] offset:14336\n"
    "ds_read_b64_tr_b16 v[74:75], %[vrd0] offset:15360\n"
    "ds_read_b64_tr_b16 v[76:77], %[vrd1] offset:14336\n"
    "ds_read_b64_tr_b16 v[78:79], %[vrd1] offset:15360\n"
    "s_waitcnt lgkmcnt(4)\n"
    "s_add_u32 s41, %[rot], 7\n"
    "s_and_b32 s41, s41, 7\n"
    "s_mul_i32 s41, s41, 163840\n"
    "s_add_u32 s41, s41, 81920\n"
    "s_add_u32 s44, s54, s41\n"
    "s_addc_u32 s45, s55, 0\n"
    "s_add_u32 m0, s60, 28672\n"
    "s_nop 0\n"
    "global_load_lds_dwordx4 %[vw], s[44:45] offset:0\n"
    "global_load_lds_dwordx4 v230, s[44:45] offset:1024\n"
    "s_add_u32 s44, s44, 64\n"
    "s_addc_u32 s45, s45, 0\n"
    "s_waitcnt vmcnt(12)\n"
    "ds_read_b64_tr_b16 v[80:81], %[vrd0] offset:16384\n"
    "ds_read_b64_tr_b16 v[82:83], %[vrd0] offset:17408\n"
    "ds_read_b64_tr_b16 v[84:85], %[vrd1] offset:16384\n"
    "ds_read_b64_tr_b16 v[86:87], %[vrd1] offset:17408\n"
    "s_waitcnt lgkmcnt(4)\n"
    "s_add_u32 m0, s60, 30720\n"
    "s_nop 0\n"
    "global_load_lds_dwordx4 %[vw], s[44:45] offset:0\n"
    "global_load_lds_dwordx4 v230, s[44:45] offset:1024\n"
    "s_add_u32 s44, s44, 64\n"
    "s_addc_u32 s45, s45, 0\n"
    "s_waitcnt vmcnt(12)\n"
    "ds_read_b64_tr_b16 v[88:89], %[vrd0] offset:18432\n"
    "ds_read_b64_tr_b16 v[90:91], %[vrd0] offset:19456\n"
    "ds_read_b64_tr_b16 v[92:93], %[vrd1] offset:18432\n"
    "ds_read_b64_tr_b16 v[94:95], %[vrd1] offset:19456\n"
    "s_waitcnt lgkmcnt(4)\n"
    "s_add_u32 s41, %[rot], 0\n"
    "s_and_b32 s41, s41, 7\n"
    "s_mul_i32 s41, s41, 163840\n"
    "s_add_u32 s41, s41, 122880\n"
    "s_add_u32 s44, s54, s41\n"
    "s_addc_u32 s45, s55, 0\n"
    "s_add_u32 m0, s60, 0\n"
    "s_nop 0\n"
    "global_load_lds_dwordx4 %[vw], s[44:45] offset:0\n"
    "global_load_lds_dwordx4 v230, s[44:45] offset:1024\n"
    "s_add_u32 s44, s44, 64\n"
    "s_addc_u32 s45, s45, 0\n"
    "s_waitcnt vmcnt(12)\n"
    "ds_read_b64_tr_b16 v[96:97], %[vrd0] offset:20480\n"
    "ds_read_b64_tr_b16 v[98:99], %[vrd0] offset:21504\n"
    "ds_read_b64_tr_b16 v[100:101], %[vrd1] offset:20480\n"
    "ds_read_b64_tr_b16 v[102:103], %[vrd1] offset:21504\n"
    "s_waitcnt lgkmcnt(4)\n"
    "s_add_u32 m0, s60, 2048\n"
    "s_nop 0\n"
    "global_load_lds_dwordx4 %[vw], s[44:45] offset:0\n"
    "global_load_lds_dwordx4 v230, s[44:45] offset:1024\n"
    "s_add_u32 s44, s44, 64\n"
    "s_addc_u32 s45, s45, 0\n"
    "s_waitcnt vmcnt(12)\n"
    "ds_read_b64_tr_b16 v[104:105], %[vrd0] offset:22528\n"
    "ds_read_b64_tr_b16 v[106:107], %[vrd0] offset:23552\n"
    "ds_read_b64_tr_b16 v[108:109], %[vrd1] offset:22528\n"
    "ds_read_b64_tr_b16 v[110:111], %[vrd1] offset:23552\n"
    "s_waitcnt lgkmcnt(4)\n"
    "s_add_u32 s41, %[rot], 1\n"
    "s_and_b32 s41, s41, 7\n"
    "s_mul_i32 s41, s41, 163840\n"
    "s_add_u32 s41, s41, 122880\n"
    "s_add_u32 s44, s54, s41\n"
    "s_addc_u32 s45, s55, 0\n"
    "s_add_u32 m0, s60, 4096\n"
    "s_nop 0\n"
    "global_load_lds_dwordx4 %[vw], s[44:45] offset:0\n"
    "global_load_lds_dwordx4 v230, s[44:45] offset:1024\n"
    "s_add_u32 s44, s44, 64\n"
    "s_addc_u32 s45, s45, 0\n"
    "s_waitcnt vmcnt(12)\n"
    "ds_read_b64_tr_b16 v[112:113], %[vrd0] offset:24576\n"
    "ds_read_b64_tr_b16 v[114:115], %[vrd0] offset:25600\n"
    "ds_read_b64_tr_b16 v[116:117], %[vrd1] offset:24576\n"
    "ds_read_b64_tr_b16 v[118:119], %[vrd1] offset:25600\n"
    "s_waitcnt lgkmcnt(4)\n"
    "s_add_u32 m0, s60, 6144\n"
    "s_nop 0\n"
    "global_load_lds_dwordx4 %[vw], s[44:45] offset:0\n"
    "global_load_lds_dwordx4 v230, s[44:45] offset:1024\n"
    "s_add_u32 s44, s44, 64\n"
    "s_addc_u32 s45, s45, 0\n"
    "s_waitcnt vmcnt(12)\n"
    "ds_read_b64_tr_b16 v[120:121], %[vrd0] offset:26624\n"
    "ds_read_b64_tr_b16 v[122:123], %[vrd0] offset:27648\n"
    "ds_read_b64_tr_b16 v[124:125], %[vrd1] offset:26624\n"
    "ds_read_b64_tr_b16 v[126:127], %[vrd1] offset:27648\n"
    "s_waitcnt lgkmcnt(4)\n"
    "s_add_u32 s41, %[rot], 2\n"
    "s_and_b32 s41, s41, 7\n"
    "s_mul_i32 s41, s41, 163840\n"
    "s_add_u32 s41, s41, 122880\n"
    "s_add_u32 s44, s54, s41\n"
    "s_addc_u32 s45, s55, 0\n"
    "s_add_u32 m0, s60, 8192\n"
    "s_nop 0\n"
    "global_load_lds_dwordx4 %[vw], s[44:45] offset:0\n"
    "global_load_lds_dwordx4 v230, s[44:45] offset:1024\n"
    "s_add_u32 s44, s44, 64\n"
    "s_addc_u32 s45, s45, 0\n"
    "s_waitcnt vmcnt(12)\n"
    "ds_read_b64_tr_b16 v[128:129], %[vrd0] offset:28672\n"
    "ds_read_b64_tr_b16 v[130:131], %[vrd0] offset:29696\n"
    "ds_read_b64_tr_b16 v[132:133], %[vrd1] offset:28672\n"
    "ds_read_b64_tr_b16 v[134:135], %[vrd1] offset:29696\n"
    "s_waitcnt lgkmcnt(4)\n"
    "s_add_u32 m0, s60, 10240\n"
    "s_nop 0\n"
    "global_load_lds_dwordx4 %[vw], s[44:45] offset:0\n"
    "global_load_lds_dwordx4 v230, s[44:45] offset:1024\n"
    "s_add_u32 s44, s44, 64\n"
    "s_addc_u32 s45, s45, 0\n"
    "s_waitcnt vmcnt(12)\n"
    "ds_read_b64_tr_b16 v[136:137], %[vrd0] offset:30720\n"
    "ds_read_b64_tr_b16 v[138:139], %[vrd0] offset:31744\n"
    "ds_read_b64_tr_b16 v[140:141], %[vrd1] offset:30720\n"
    "ds_read_b64_tr_b16 v[142:143], %[vrd1] offset:31744\n"
    "s_waitcnt lgkmcnt(4)\n"
    "s_add_u32 s41, %[rot], 3\n"
    "s_and_b32 s41, s41, 7\n"
    "s_mul_i32 s41, s41, 163840\n"
    "s_add_u32 s41, s41, 122880\n"
    "s_add_u32 s44, s54, s41\n"
    "s_addc_u32 s45, s55, 0\n"
    "s_add_u32 m0, s60, 12288\n"
    "s_nop 0\n"
    "global_load_lds_dwordx4 %[vw], s[44:45] offset:0\n"
    "global_load_lds_dwordx4 v230, s[44:45] offset:1024\n"
    "s_add_u32 s44, s44, 64\n"
    "s_addc_u32 s45, s45, 0\n"
    "s_waitcnt vmcnt(12)\n"
    "ds_read_b64_tr_b16 v[208:209], %[vrd0] offset:0\n"
    "ds_read_b64_tr_b16 v[210:211], %[vrd0] offset:1024\n"
    "ds_read_b64_tr_b16 v[212:213], %[vrd1] offset:0\n"
    "ds_read_b64_tr_b16 v[214:215], %[vrd1] offset:1024\n"
    "s_waitcnt lgkmcnt(0)\n"
    "ds_write_b128 v231, v[208:211] offset:0\n"
    "ds_write_b128 v231, v[212:215] offset:1024\n"
    "s_waitcnt lgkmcnt(4)\n"
    "s_add_u32 m0, s60, 14336\n"
    "s_nop 0\n"
    "global_load_lds_dwordx4 %[vw], s[44:45] offset:0\n"
    "global_load_lds_dwordx4 v230, s[44:45] offset:1024\n"
    "s_add_u32 s44, s44, 64\n"
    "s_addc_u32 s45, s45, 0\n"
    "s_waitcnt vmcnt(12)\n"
    "ds_read_b64_tr_b16 v[216:217], %[vrd0] offset:2048\n"
    "ds_read_b64_tr_b16 v[218:219], %[vrd0] offset:3072\n"
    "ds_read_b64_tr_b16 v[220:221], %[vrd1] offset:2048\n"
    "ds_read_b64_tr_b16 v[222:223], %[vrd1] offset:3072\n"
    "s_waitcnt lgkmcnt(0)\n"
    "ds_write_b128 v231, v[216:219] offset:2048\n"
    "ds_write_b128 v231, v[220:223] offset:3072\n"
    "s_waitcnt lgkmcnt(4)\n"
    "s_add_u32 s41, %[rot], 4\n"
    "s_and_b32 s41, s41, 7\n"
    "s_mul_i32 s41, s41, 163840\n"
    "s_add_u32 s41, s41, 122880\n"
    "s_add_u32 s44, s54, s41\n"
    "s_addc_u32 s45, s55, 0\n"
    "s_add_u32 m0, s60, 16384\n"
    "s_nop 0\n"
    "global_load_lds_dwordx4 %[vw], s[44:45] offset:0\n"
    "global_load_lds_dwordx4 v230, s[44:45] offset:1024\n"
    "s_add_u32 s44, s44, 64\n"
    "s_addc_u32 s45, s45, 0\n"
    "s_waitcnt vmcnt(12)\n"
    "ds_read_b64_tr_b16 v[208:209], %[vrd0] offset:4096\n"
    "ds_read_b64_tr_b16 v[210:211], %[vrd0] offset:5120\n"
    "ds_read_b64_tr_b16 v[212:213], %[vrd1] offset:4096\n"
    "ds_read_b64_tr_b16 v[214:215], %[vrd1] offset:5120\n"
    "s_waitcnt lgkmcnt(0)\n"
    "ds_write_b128 v231, v[208:211] offset:4096\n"
    "ds_write_b128 v231, v[212:215] offset:5120\n"
    "s_waitcnt lgkmcnt(4)\n"
    "s_add_u32 m0, s60, 18432\n"
    "s_nop 0\n"
    "global_load_lds_dwordx4 %[vw], s[44:45] offset:0\n"
    "global_load_lds_dwordx4 v230, s[44:45] offset:1024\n"
    "s_add_u32 s44, s44, 64\n"
    "s_addc_u32 s45, s45, 0\n"
    "s_waitcnt vmcnt(12)\n"
    "ds_read_b64_tr_b16 v[216:217], %[vrd0] offset:6144\n"
    "ds_read_b64_tr_b16 v[218:219], %[vrd0] offset:7168\n"
    "ds_read_b64_tr_b16 v[220:221], %[vrd1] offset:6144\n"
    "ds_read_b64_tr_b16 v[222:223], %[vrd1] offset:7168\n"
    "s_waitcnt lgkmcnt(0)\n"
    "ds_write_b128 v231, v[216:219] offset:6144\n"
    "ds_write_b128 v231, v[220:223] offset:7168\n"
    "s_waitcnt lgkmcnt(4)\n"
    "s_add_u32 s41, %[rot], 5\n"
    "s_and_b32 s41, s41, 7\n"
    "s_mul_i32 s41, s41, 163840\n"
    "s_add_u32 s41, s41, 122880\n"
    "s_add_u32 s44, s54, s41\n"
    "s_addc_u32 s45, s55, 0\n"
    "s_add_u32 m0, s60, 20480\n"
    "s_nop 0\n"
    "global_load_lds_dwordx4 %[vw], s[44:45] offset:0\n"
    "global_load_lds_dwordx4 v230, s[44:45] offset:1024\n"
    "s_add_u32 s44, s44, 64\n"
    "s_addc_u32 s45, s45, 0\n"
    "s_waitcnt vmcnt(12)\n"
    "ds_read_b64_tr_b16 v[208:209], %[vrd0] offset:8192\n"
    "ds_read_b64_tr_b16 v[210:211], %[vrd0] offset:9216\n"
    "ds_read_b64_tr_b16 v[212:213], %[vrd1] offset:8192\n"
    "ds_read_b64_tr_b16 v[214:215], %[vrd1] offset:9216\n"
    "s_waitcnt lgkmcnt(0)\n"
    "ds_write_b128 v231, v[208:211] offset:8192\n"
    "ds_write_b128 v231, v[212:215] offset:9216\n"
    "s_waitcnt lgkmcnt(4)\n"
    "s_add_u32 m0, s60, 22528\n"
    "s_nop 0\n"
    "global_load_lds_dwordx4 %[vw], s[44:45] offset:0\n"
    "global_load_lds_dwordx4 v230, s[44:45] offset:1024\n"
    "s_add_u32 s44, s44, 64\n"
    "s_addc_u32 s45, s45, 0\n"
    "s_waitcnt vmcnt(12)\n"
    "ds_read_b64_tr_b16 v[216:217], %[vrd0] offset:10240\n"
    "ds_read_b64_tr_b16 v[218:219], %[vrd0] offset:11264\n"
    "ds_read_b64_tr_b16 v[220:221], %[vrd1] offset:10240\n"
    "ds_read_b64_tr_b16 v[222:223], %[vrd1] offset:11264\n"
    "s_waitcnt lgkmcnt(0)\n"
    "ds_write_b128 v231, v[216:219] offset:10240\n"
    "ds_write_b128 v231, v[220:223] offset:11264\n"
    "s_waitcnt lgkmcnt(4)\n"
    "s_add_u32 s41, %[rot], 6\n"
    "s_and_b32 s41, s41, 7\n"
    "s_mul_i32 s41, s41, 163840\n"
    "s_add_u32 s41, s41, 122880\n"
    "s_add_u32 s44, s54, s41\n"
    "s_addc_u32 s45, s55, 0\n"
    "s_add_u32 m0, s60, 24576\n"
    "s_nop 0\n"
    "global_load_lds_dwordx4 %[vw], s[44:45] offset:0\n"
    "global_load_lds_dwordx4 v230, s[44:45] offset:1024\n"
    "s_add_u32 s44, s44, 64\n"
    "s_addc_u32 s45, s45, 0\n"
    "s_waitcnt vmcnt(12)\n"
    "ds_read_b64_tr_b16 v[208:209], %[vrd0] offset:12288\n"
    "ds_read_b64_tr_b16 v[210:211], %[vrd0] offset:13312\n"
    "ds_read_b64_tr_b16 v[212:213], %[vrd1] offset:12288\n"
    "ds_read_b64_tr_b16 v[214:215], %[vrd1] offset:13312\n"
    "s_waitcnt lgkmcnt(0)\n"
    "ds_write_b128 v231, v[208:211] offset:12288\n"
    "ds_write_b128 v231, v[212:215] offset:13312\n"
    "s_waitcnt lgkmcnt(4)\n"
    "s_add_u32 m0, s60, 26624\n"
    "s_nop 0\n"
    "global_load_lds_dwordx4 %[vw], s[44:45] offset:0\n"
    "global_load_lds_dwordx4 v230, s[44:45] offset:1024\n"
    "s_add_u32 s44, s44, 64\n"
    "s_addc_u32 s45, s45, 0\n"
    "s_waitcnt vmcnt(12)\n"
    "ds_read_b64_tr_b16 v[216:217], %[vrd0] offset:14336\n"
    "ds_read_b64_tr_b16 v[218:219], %[vrd0] offset:15360\n"
    "ds_read_b64_tr_b16 v[220:221], %[vrd1] offset:14336\n"
    "ds_read_b64_tr_b16 v[222:223], %[vrd1] offset:15360\n"
    "s_waitcnt lgkmcnt(0)\n"
    "ds_write_b128 v231, v[216:219] offset:14336\n"
    "ds_write_b128 v231, v[220:223] offset:15360\n"
    "s_waitcnt lgkmcnt(4)\n"
    "s_add_u32 s41, %[rot], 7\n"
    "s_and_b32 s41, s41, 7\n"
    "s_mul_i32 s41, s41, 163840\n"
    "s_add_u32 s41, s41, 122880\n"
    "s_add_u32 s44, s54, s41\n"
    "s_addc_u32 s45, s55, 0\n"
    "s_add_u32 m0, s60, 28672\n"
    "s_nop 0\n"
    "global_load_lds_dwordx4 %[vw], s[44:45] offset:0\n"
    "global_load_lds_dwordx4 v230, s[44:45] offset:1024\n"
    "s_add_u32 s44, s44, 64\n"
    "s_addc_u32 s45, s45, 0\n"
    "s_waitcnt vmcnt(12)\n"
    "ds_read_b64_tr_b16 v[208:209], %[vrd0] offset:16384\n"
    "ds_read_b64_tr_b16 v[210:211], %[vrd0] offset:17408\n"
    "ds_read_b64_tr_b16 v[212:213], %[vrd1] offset:16384\n"
    "ds_read_b64_tr_b16 v[214:215], %[vrd1] offset:17408\n"
    "s_waitcnt lgkmcnt(0)\n"
    "ds_write_b128 v231, v[208:211] offset:16384\n"
    "ds_write_b128 v231, v[212:215] offset:17408\n"
    "s_waitcnt lgkmcnt(4)\n"
    "s_add_u32 m0, s60, 30720\n"
    "s_nop 0\n"
    "global_load_lds_dwordx4 %[vw], s[44:45] offset:0\n"
    "global_load_lds_dwordx4 v230, s[44:45] offset:1024\n"
    "s_add_u32 s44, s44, 64\n"
    "s_addc_u32 s45, s45, 0\n"
    "s_waitcnt vmcnt(12)\n"
    "ds_read_b64_tr_b16 v[216:217], %[vrd0] offset:18432\n"
    "ds_read_b64_tr_b16 v[218:219], %[vrd0] offset:19456\n"
    "ds_read_b64_tr_b16 v[220:221], %[vrd1] offset:18432\n"
    "ds_read_b64_tr_b16 v[222:223], %[vrd1] offset:19456\n"
    "s_waitcnt lgkmcnt(0)\n"
    "ds_write_b128 v231, v[216:219] offset:18432\n"
    "ds_write_b128 v231, v[220:223] offset:19456\n"
    "s_waitcnt vmcnt(10)\n"
    "ds_read_b64_tr_b16 v[208:209], %[vrd0] offset:20480\n"
    "ds_read_b64_tr_b16 v[210:211], %[vrd0] offset:21504\n"
    "ds_read_b64_tr_b16 v[212:213], %[vrd1] offset:20480\n"
    "ds_read_b64_tr_b16 v[214:215], %[vrd1] offset:21504\n"
    "s_waitcnt lgkmcnt(0)\n"
    "ds_write_b128 v231, v[208:211] offset:20480\n"
    "ds_write_b128 v231, v[212:215] offset:21504\n"
    "s_waitcnt vmcnt(8)\n"
    "ds_read_b64_tr_b16 v[216:217], %[vrd0] offset:22528\n"
    "ds_read_b64_tr_b16 v[218:219], %[vrd0] offset:23552\n"
    "ds_read_b64_tr_b16 v[220:221], %[vrd1] offset:22528\n"
    "ds_read_b64_tr_b16 v[222:223], %[vrd1] offset:23552\n"
    "s_waitcnt lgkmcnt(0)\n"
    "ds_write_b128 v231, v[216:219] offset:22528\n"
    "ds_write_b128 v231, v[220:223] offset:23552\n"
    "s_waitcnt vmcnt(6)\n"
    "ds_read_b64_tr_b16 v[208:209], %[vrd0] offset:24576\n"
    "ds_read_b64_tr_b16 v[210:211], %[vrd0] offset:25600\n"
    "ds_read_b64_tr_b16 v[212:213], %[vrd1] offset:24576\n"
    "ds_read_b64_tr_b16 v[214:215], %[vrd1] offset:25600\n"
    "s_waitcnt lgkmcnt(0)\n"
    "ds_write_b128 v231, v[208:211] offset:24576\n"
    "ds_write_b128 v231, v[212:215] offset:25600\n"
    "s_waitcnt vmcnt(4)\n"
    "ds_read_b64_tr_b16 v[216:217], %[vrd0] offset:26624\n"
    "ds_read_b64_tr_b16 v[218:219], %[vrd0] offset:27648\n"
    "ds_read_b64_tr_b16 v[220:221], %[vrd1] offset:26624\n"
    "ds_read_b64_tr_b16 v[222:223], %[vrd1] offset:27648\n"
    "s_waitcnt lgkmcnt(0)\n"
    "ds_write_b128 v231, v[216:219] offset:26624\n"
    "ds_write_b128 v231, v[220:223] offset:27648\n"
    "s_waitcnt vmcnt(2)\n"
    "ds_read_b64_tr_b16 v[208:209], %[vrd0] offset:28672\n"
    "ds_read_b64_tr_b16 v[210:211], %[vrd0] offset:29696\n"
    "ds_read_b64_tr_b16 v[212:213], %[vrd1] offset:28672\n"
    "ds_read_b64_tr_b16 v[214:215], %[vrd1] offset:29696\n"
    "s_waitcnt lgkmcnt(0)\n"
    "ds_write_b128 v231, v[208:211] offset:28672\n"
    "ds_write_b128 v231, v[212:215] offset:29696\n"
    "s_waitcnt vmcnt(0)\n"
    "ds_read_b64_tr_b16 v[216:217], %[vrd0] offset:30720\n"
    "ds_read_b64_tr_b16 v[218:219], %[vrd0] offset:31744\n"
    "ds_read_b64_tr_b16 v[220:221], %[vrd1] offset:30720\n"
    "ds_read_b64_tr_b16 v[222:223], %[vrd1] offset:31744\n"
    "s_waitcnt lgkmcnt(0)\n"
    "ds_write_b128 v231, v[216:219] offset:30720\n"
    "ds_write_b128 v231, v[220:223] offset:31744\n"
    "s_waitcnt lgkmcnt(0)\n"
    "s_mov_b64 s[46:47], %[ag]\n"
    "s_mov_b64 s[48:49], %[hg]\n"
    "s_mov_b64 s[50:51], %[xg]\n"
    "s_mov_b64 s[58:59], %[sg]\n"
    "s_mov_b64 s[52:53], %[ws]\n"
    "s_add_u32 s52, s52, 2117632\n"
    "s_addc_u32 s53, s53, 0\n"
    "s_mov_b32 s62, 0x01010101\n"
    "s_mov_b32 s63, 0x01010101\n"
    "s_mov_b32 s40, %[u0]\n"
    "s_mov_b32 s43, 0\n"
    "s_mov_b32 s64, 2097152\n"
    "s_load_dwordx2 s[64:65], %[ws], s64\n"
    "s_waitcnt lgkmcnt(0)\n"
    "s_add_u32 s64, s64, 1\n"
    "s_and_b32 s65, s65, -8\n"
    "v_lshlrev_b32 v148, 3, %[vg16]\n"
    "v_min_u32 v148, 128, v148\n"
    "v_add_u32 v148, 2146304, v148\n"
    "s_branch Pinb_%=\n"
    "Pin_%=:\n"
    "s_sleep 4\n"
    "s_add_u32 s43, s43, 1\n"
    "s_cmp_gt_u32 s43, 524288\n"
    "s_cbranch_scc1 Pdead_%=\n"
    "Pinb_%=:\n"
    "global_load_dwordx2 v[144:145], v148, %[ws] offset:0 sc1\n"
    "s_waitcnt vmcnt(0)\n"
    "v_mov_b32 v146, v145\n"
    "v_and_b32 v145, -8, v145\n"
    "v_cmp_ne_u64 vcc, s[64:65], v[144:145]\n"
    "s_cbranch_vccnz Pin_%=\n"
    "v_readlane_b32 s42, v146, 1\n"
    "s_nop 4\n"
    "s_and_b32 s42, s42, 1\n"
    "s_xor_b32 s42, s42, 1\n"
    "s_lshl_b32 s42, s42, 13\n"
    "s_add_u32 s46, s46, s42\n"
    "s_addc_u32 s47, s47, 0\n"
    "v_mov_b32 v229, 0\n"
    "v_mov_b32 v226, 0\n"
    "Ploop_%=:\n"
    "global_load_dwordx2 v[224:225], %[vxv], s[50:51] offset:0\n"
    "s_lshr_b32 s56, s40, 1\n"
    "s_add_u32 s56, s56, 1\n"
    "s_cmp_lg_u32 %[w], 0\n"
    "s_cbranch_scc1 Pr_%=\n"
    "Pw_%=:\n"
    "v_cmp_gt_u32 vcc, s56, v226\n"
    "s_cbranch_vccz Pr_%=\n"
    "s_add_u32 s43, s43, 1\n"
    "s_cmp_gt_u32 s43, 524288\n"
    "s_cbranch_scc1 Pdead_%=\n"
    "global_load_dword v226, %[vflv], s[52:53] sc1\n"
    "s_waitcnt vmcnt(0)\n"
    "v_cmp_gt_u32 vcc, s56, v226\n"
    "s_cbranch_vccz Pr_%=\n"
    "s_sleep 2\n"
    "s_branch Pw_%=\n"
    "Pr_%=:\n"
    "s_barrier\n"
    "s_add_u32 s41, %[rot], 0\n"
    "s_and_b32 s42, s41, 6\n"
    "s_and_b32 s41, s41, 7\n"
    "s_add_u32 s41, s41, s42\n"
    "s_lshl_b32 s41, s41, 12\n"
    "s_add_u32 s54, s46, s41\n"
    "s_addc_u32 s55, s47, 0\n"
    "global_load_dwordx4 v[144:147], %[vg16], s[54:55] offset:0 sc1\n"
    "global_load_dwordx4 v[148:151], %[vg16], s[54:55] offset:1024 sc1\n"
    "global_load_dwordx4 v[152:155], %[vg16], s[54:55] offset:2048 sc1\n"
    "global_load_dwordx4 v[156:159], %[vg16], s[54:55] offset:3072 sc1\n"
    "s_add_u32 s41, %[rot], 1\n"
    "s_and_b32 s42, s41, 6\n"
    "s_and_b32 s41, s41, 7\n"
    "s_add_u32 s41, s41, s42\n"
    "s_lshl_b32 s41, s41, 12\n"
    "s_add_u32 s54, s46, s41\n"
    "s_addc_u32 s55, s47, 0\n"
    "global_load_dwordx4 v[160:163], %[vg16], s[54:55] offset:0 sc1\n"
    "global_load_dwordx4 v[164:167], %[vg16], s[54:55] offset:1024 sc1\n"
    "global_load_dwordx4 v[168:171], %[vg16], s[54:55] offset:2048 sc1\n"
    "global_load_dwordx4 v[172:175], %[vg16], s[54:55] offset:3072 sc1\n"
    "s_add_u32 s41, %[rot], 2\n"
    "s_and_b32 s42, s41, 6\n"
    "s_and_b32 s41, s41, 7\n"
    "s_add_u32 s41, s41, s42\n"
    "s_lshl_b32 s41, s41, 12\n"
    "s_add_u32 s54, s46, s41\n"
    "s_addc_u32 s55, s47, 0\n"
    "global_load_dwordx4 v[176:179], %[vg16], s[54:55] offset:0 sc1\n"
    "global_load_dwordx4 v[180:183], %[vg16], s[54:55] offset:1024 sc1\n"
    "global_load_dwordx4 v[184:187], %[vg16], s[54:55] offset:2048 sc1\n"
    "global_load_dwordx4 v[188:191], %[vg16], s[54:55] offset:3072 sc1\n"
    "s_waitcnt vmcnt(11)\n"
    "v_mfma_f32_16x16x32_bf16 v[192:195], a[0:3], v[144:147], 0\n"
    "ds_read_b128 v[208:211], v231 offset:0\n"
    "v_mfma_f32_16x16x32_bf16 v[196:199], a[4:7], v[144:147], 0\n"
    "v_mfma_f32_16x16x32_bf16 v[200:203], a[8:11], v[144:147], 0\n"
    "ds_read_b128 v[212:215], v231 offset:1024\n"
    "v_mfma_f32_16x16x32_bf16 v[204:207], a[12:15], v[144:147], 0\n"
    "s_add_u32 s41, %[rot], 3\n"
    "s_and_b32 s42, s41, 6\n"
    "s_and_b32 s41, s41, 7\n"
    "s_add_u32 s41, s41, s42\n"
    "s_lshl_b32 s41, s41, 12\n"
    "s_add_u32 s54, s46, s41\n"
    "s_addc_u32 s55, s47, 0\n"
    "global_load_dwordx4 v[144:147], %[vg16], s[54:55] offset:0 sc1\n"
    "s_waitcnt vmcnt(11)\n"
    "v_mfma_f32_16x16x32_bf16 v[192:195], a[16:19], v[148:151], v[192:195]\n"
    "ds_read_b128 v[216:219], v231 offset:2048\n"
    "v_mfma_f32_16x16x32_bf16 v[196:199], a[20:23], v[148:151], v[196:199]\n"
    "v_mfma_f32_16x16x32_bf16 v[200:203], a[24:27], v[148:151], v[200:203]\n"
    "ds_read_b128 v[220:223], v231 offset:3072\n"
    "v_mfma_f32_16x16x32_bf16 v[204:207], a[28:31], v[148:151], v[204:207]\n"
    "global_load_dwordx4 v[148:151], %[vg16], s[54:55] offset:1024 sc1\n"
    "s_waitcnt vmcnt(11)\n"
    "v_mfma_f32_16x16x32_bf16 v[192:195], a[32:35], v[152:155], v[192:195]\n"
    "v_mfma_f32_16x16x32_bf16 v[196:199], a[36:39], v[152:155], v[196:199]\n"
    "v_mfma_f32_16x16x32_bf16 v[200:203], a[40:43], v[152:155], v[200:203]\n"
    "v_mfma_f32_16x16x32_bf16 v[204:207], a[44:47], v[152:155], v[204:207]\n"
    "global_load_dwordx4 v[152:155], %[vg16], s[54:55] offset:2048 sc1\n"
    "s_waitcnt vmcnt(11)\n"
    "s_waitcnt lgkmcnt(3)\n"
    "v_mfma_f32_16x16x32_bf16 v[192:195], v[208:211], v[156:159], v[192:195]\n"
    "s_waitcnt lgkmcnt(2)\n"
    "v_mfma_f32_16x16x32_bf16 v[196:199], v[212:215], v[156:159], v[196:199]\n"
    "s_waitcnt lgkmcnt(1)\n"
    "v_mfma_f32_16x16x32_bf16 v[200:203], v[216:219], v[156:159], v[200:203]\n"
    "s_waitcnt lgkmcnt(0)\n"
    "v_mfma_f32_16x16x32_bf16 v[204:207], v[220:223], v[156:159], v[204:207]\n"
    "global_load_dwordx4 v[156:159], %[vg16], s[54:55] offset:3072 sc1\n"
    "s_waitcnt vmcnt(11)\n"
    "v_mfma_f32_16x16x32_bf16 v[192:195], a[48:51], v[160:163], v[192:195]\n"
    "ds_read_b128 v[208:211], v231 offset:4096\n"
    "v_mfma_f32_16x16x32_bf16 v[196:199], a[52:55], v[160:163], v[196:199]\n"
    "v_mfma_f32_16x16x32_bf16 v[200:203], a[56:59], v[160:163], v[200:203]\n"
    "ds_read_b128 v[212:215], v231 offset:5120\n"
    "v_mfma_f32_16x16x32_bf16 v[204:207], a[60:63], v[160:163], v[204:207]\n"
    "s_add_u32 s41, %[rot], 4\n"
    "s_and_b32 s42, s41, 6\n"
    "s_and_b32 s41, s41, 7\n"
    "s_add_u32 s41, s41, s42\n"
    "s_lshl_b32 s41, s41, 12\n"
    "s_add_u32 s54, s46, s41\n"
    "s_addc_u32 s55, s47, 0\n"
    "global_load_dwordx4 v[160:163], %[vg16], s[54:55] offset:0 sc1\n"
    "s_waitcnt vmcnt(11)\n"
    "v_mfma_f32_16x16x32_bf16 v[192:195], a[64:67], v[164:167], v[192:195]\n"
    "ds_read_b128 v[216:219], v231 offset:6144\n"
    "v_mfma_f32_16x16x32_bf16 v[196:199], a[68:71], v[164:167], v[196:199]\n"
    "v_mfma_f32_16x16x32_bf16 v[200:203], a[72:75], v[164:167], v[200:203]\n"
    "ds_read_b128 v[220:223], v231 offset:7168\n"
    "v_mfma_f32_16x16x32_bf16 v[204:207], a[76:79], v[164:167], v[204:207]\n"
    "global_load_dwordx4 v[164:167], %[vg16], s[54:55] offset:1024 sc1\n"
    "s_waitcnt vmcnt(11)\n"
    "v_mfma_f32_16x16x32_bf16 v[192:195], a[80:83], v[168:171], v[192:195]\n"
    "v_mfma_f32_16x16x32_bf16 v[196:199], a[84:87], v[168:171], v[196:199]\n"
    "v_mfma_f32_16x16x32_bf16 v[200:203], a[88:91], v[168:171], v[200:203]\n"
    "v_mfma_f32_16x16x32_bf16 v[204:207], a[92:95], v[168:171], v[204:207]\n"
    "global_load_dwordx4 v[168:171], %[vg16], s[54:55] offset:2048 sc1\n"
    "s_waitcnt vmcnt(11)\n"
    "s_waitcnt lgkmcnt(3)\n"
    "v_mfma_f32_16x16x32_bf16 v[192:195], v[208:211], v[172:175], v[192:195]\n"
    "s_waitcnt lgkmcnt(2)\n"
    "v_mfma_f32_16x16x32_bf16 v[196:199], v[212:215], v[172:175], v[196:199]\n"
    "s_waitcnt lgkmcnt(1)\n"
    "v_mfma_f32_16x16x32_bf16 v[200:203], v[216:219], v[172:175], v[200:203]\n"
    "s_waitcnt lgkmcnt(0)\n"
    "v_mfma_f32_16x16x32_bf16 v[204:207], v[220:223], v[172:175], v[204:207]\n"
    "global_load_dwordx4 v[172:175], %[vg16], s[54:55] offset:3072 sc1\n"
    "s_waitcnt vmcnt(11)\n"
    "v_mfma_f32_16x16x32_bf16 v[192:195], a[96:99], v[176:179], v[192:195]\n"
    "ds_read_b128 v[208:211], v231 offset:8192\n"
    "v_mfma_f32_16x16x32_bf16 v[196:199], a[100:103], v[176:179], v[196:199]\n"
    "v_mfma_f32_16x16x32_bf16 v[200:203], a[104:107], v[176:179], v[200:203]\n"
    "ds_read_b128 v[212:215], v231 offset:9216\n"
    "v_mfma_f32_16x16x32_bf16 v[204:207], a[108:111], v[176:179], v[204:207]\n"
    "s_add_u32 s41, %[rot], 5\n"
    "s_and_b32 s42, s41, 6\n"
    "s_and_b32 s41, s41, 7\n"
    "s_add_u32 s41, s41, s42\n"
    "s_lshl_b32 s41, s41, 12\n"
    "s_add_u32 s54, s46, s41\n"
    "s_addc_u32 s55, s47, 0\n"
    "global_load_dwordx4 v[176:179], %[vg16], s[54:55] offset:0 sc1\n"
    "s_waitcnt vmcnt(11)\n"
    "v_mfma_f32_16x16x32_bf16 v[192:195], a[112:115], v[180:183], v[192:195]\n"
    "ds_read_b128 v[216:219], v231 offset:10240\n"
    "v_mfma_f32_16x16x32_bf16 v[196:199], a[116:119], v[180:183], v[196:199]\n"
    "v_mfma_f32_16x16x32_bf16 v[200:203], a[120:123], v[180:183], v[200:203]\n"
    "ds_read_b128 v[220:223], v231 offset:11264\n"
    "v_mfma_f32_16x16x32_bf16 v[204:207], a[124:127], v[180:183], v[204:207]\n"
    "global_load_dwordx4 v[180:183], %[vg16], s[54:55] offset:1024 sc1\n"
    "s_waitcnt vmcnt(11)\n"
    "v_mfma_f32_16x16x32_bf16 v[192:195], a[128:131], v[184:187], v[192:195]\n"
    "v_mfma_f32_16x16x32_bf16 v[196:199], a[132:135], v[184:187], v[196:199]\n"
    "v_mfma_f32_16x16x32_bf16 v[200:203], a[136:139], v[184:187], v[200:203]\n"
    "v_mfma_f32_16x16x32_bf16 v[204:207], a[140:143], v[184:187], v[204:207]\n"
    "global_load_dwordx4 v[184:187], %[vg16], s[54:55] offset:2048 sc1\n"
    "s_waitcnt vmcnt(11)\n"
    "s_waitcnt lgkmcnt(3)\n"
    "v_mfma_f32_16x16x32_bf16 v[192:195], v[208:211], v[188:191], v[192:195]\n"
    "s_waitcnt lgkmcnt(2)\n"
    "v_mfma_f32_16x16x32_bf16 v[196:199], v[212:215], v[188:191], v[196:199]\n"
    "s_waitcnt lgkmcnt(1)\n"
    "v_mfma_f32_16x16x32_bf16 v[200:203], v[216:219], v[188:191], v[200:203]\n"
    "s_waitcnt lgkmcnt(0)\n"
    "v_mfma_f32_16x16x32_bf16 v[204:207], v[220:223], v[188:191], v[204:207]\n"
    "global_load_dwordx4 v[188:191], %[vg16], s[54:55] offset:3072 sc1\n"
    "s_waitcnt vmcnt(11)\n"
    "v_mfma_f32_16x16x32_bf16 v[192:195], a[144:147], v[144:147], v[192:195]\n"
    "ds_read_b128 v[208:211], v231 offset:12288\n"
    "v_mfma_f32_16x16x32_bf16 v[196:199], a[148:151], v[144:147], v[196:199]\n"
    "v_mfma_f32_16x16x32_bf16 v[200:203], a[152:155], v[144:147], v[200:203]\n"
    "ds_read_b128 v[212:215], v231 offset:13312\n"
    "v_mfma_f32_16x16x32_bf16 v[204:207], a[156:159], v[144:147], v[204:207]\n"
    "s_add_u32 s41, %[rot], 6\n"
    "s_and_b32 s42, s41, 6\n"
    "s_and_b32 s41, s41, 7\n"
    "s_add_u32 s41, s41, s42\n"
    "s_lshl_b32 s41, s41, 12\n"
    "s_add_u32 s54, s46, s41\n"
    "s_addc_u32 s55, s47, 0\n"
    "global_load_dwordx4 v[144:147], %[vg16], s[54:55] offset:0 sc1\n"
    "s_waitcnt vmcnt(11)\n"
    "v_mfma_f32_16x16x32_bf16 v[192:195], a[160:163], v[148:151], v[192:195]\n"
    "ds_read_b128 v[216:219], v231 offset:14336\n"
    "v_mfma_f32_16x16x32_bf16 v[196:199], a[164:167], v[148:151], v[196:199]\n"
    "v_mfma_f32_16x16x32_bf16 v[200:203], a[168:171], v[148:151], v[200:203]\n"
    "ds_read_b128 v[220:223], v231 offset:15360\n"
    "v_mfma_f32_16x16x32_bf16 v[204:207], a[172:175], v[148:151], v[204:207]\n"
    "global_load_dwordx4 v[148:151], %[vg16], s[54:55] offset:1024 sc1\n"
    "s_waitcnt vmcnt(11)\n"
    "v_mfma_f32_16x16x32_bf16 v[192:195], a[176:179], v[152:155], v[192:195]\n"
    "v_mfma_f32_16x16x32_bf16 v[196:199], a[180:183], v[152:155], v[196:199]\n"
    "v_mfma_f32_16x16x32_bf16 v[200:203], a[184:187], v[152:155], v[200:203]\n"
    "v_mfma_f32_16x16x32_bf16 v[204:207], a[188:191], v[152:155], v[204:207]\n"
    "global_load_dwordx4 v[152:155], %[vg16], s[54:55] offset:2048 sc1\n"
    "s_waitcnt vmcnt(11)\n"
    "s_waitcnt lgkmcnt(3)\n"
    "v_mfma_f32_16x16x32_bf16 v[192:195], v[208:211], v[156:159], v[192:195]\n"
    "s_waitcnt lgkmcnt(2)\n"
    "v_mfma_f32_16x16x32_bf16 v[196:199], v[212:215], v[156:159], v[196:199]\n"
    "s_waitcnt lgkmcnt(1)\n"
    "v_mfma_f32_16x16x32_bf16 v[200:203], v[216:219], v[156:159], v[200:203]\n"
    "s_waitcnt lgkmcnt(0)\n"
    "v_mfma_f32_16x16x32_bf16 v[204:207], v[220:223], v[156:159], v[204:207]\n"
    "global_load_dwordx4 v[156:159], %[vg16], s[54:55] offset:3072 sc1\n"
    "s_waitcnt vmcnt(11)\n"
    "v_mfma_f32_16x16x32_bf16 v[192:195], a[192:195], v[160:163], v[192:195]\n"
    "ds_read_b128 v[208:211], v231 offset:16384\n"
    "v_mfma_f32_16x16x32_bf16 v[196:199], a[196:199], v[160:163], v[196:199]\n"
    "v_mfma_f32_16x16x32_bf16 v[200:203], a[200:203], v[160:163], v[200:203]\n"
    "ds_read_b128 v[212:215], v231 offset:17408\n"
    "v_mfma_f32_16x16x32_bf16 v[204:207], a[204:207], v[160:163], v[204:207]\n"
    "s_add_u32 s41, %[rot], 7\n"
    "s_and_b32 s42, s41, 6\n"
    "s_and_b32 s41, s41, 7\n"
    "s_add_u32 s41, s41, s42\n"
    "s_lshl_b32 s41, s41, 12\n"
    "s_add_u32 s54, s46, s41\n"
    "s_addc_u32 s55, s47, 0\n"
    "global_load_dwordx4 v[160:163], %[vg16], s[54:55] offset:0 sc1\n"
    "s_waitcnt vmcnt(11)\n"
    "v_mfma_f32_16x16x32_bf16 v[192:195], a[208:211], v[164:167], v[192:195]\n"
    "ds_read_b128 v[216:219], v231 offset:18432\n"
    "v_mfma_f32_16x16x32_bf16 v[196:199], a[212:215], v[164:167], v[196:199]\n"
    "v_mfma_f32_16x16x32_bf16 v[200:203], a[216:219], v[164:167], v[200:203]\n"
    "ds_read_b128 v[220:223], v231 offset:19456\n"
    "v_mfma_f32_16x16x32_bf16 v[204:207], a[220:223], v[164:167], v[204:207]\n"
    "global_load_dwordx4 v[164:167], %[vg16], s[54:55] offset:1024 sc1\n"
    "s_waitcnt vmcnt(11)\n"
    "v_mfma_f32_16x16x32_bf16 v[192:195], a[224:227], v[168:171], v[192:195]\n"
    "v_mfma_f32_16x16x32_bf16 v[196:199], a[228:231], v[168:171], v[196:199]\n"
    "v_mfma_f32_16x16x32_bf16 v[200:203], a[232:235], v[168:171], v[200:203]\n"
    "v_mfma_f32_16x16x32_bf16 v[204:207], a[236:239], v[168:171], v[204:207]\n"
    "global_load_dwordx4 v[168:171], %[vg16], s[54:55] offset:2048 sc1\n"
    "s_waitcnt vmcnt(11)\n"
    "s_waitcnt lgkmcnt(3)\n"
    "v_mfma_f32_16x16x32_bf16 v[192:195], v[208:211], v[172:175], v[192:195]\n"
    "s_waitcnt lgkmcnt(2)\n"
    "v_mfma_f32_16x16x32_bf16 v[196:199], v[212:215], v[172:175], v[196:199]\n"
    "s_waitcnt lgkmcnt(1)\n"
    "v_mfma_f32_16x16x32_bf16 v[200:203], v[216:219], v[172:175], v[200:203]\n"
    "s_waitcnt lgkmcnt(0)\n"
    "v_mfma_f32_16x16x32_bf16 v[204:207], v[220:223], v[172:175], v[204:207]\n"
    "global_load_dwordx4 v[172:175], %[vg16], s[54:55] offset:3072 sc1\n"
    "s_waitcnt vmcnt(11)\n"
    "v_mfma_f32_16x16x32_bf16 v[192:195], a[240:243], v[176:179], v[192:195]\n"
    "ds_read_b128 v[208:211], v231 offset:20480\n"
    "v_mfma_f32_16x16x32_bf16 v[196:199], a[244:247], v[176:179], v[196:199]\n"
    "v_mfma_f32_16x16x32_bf16 v[200:203], a[248:251], v[176:179], v[200:203]\n"
    "ds_read_b128 v[212:215], v231 offset:21504\n"
    "v_mfma_f32_16x16x32_bf16 v[204:207], a[252:255], v[176:179], v[204:207]\n"
    "s_waitcnt vmcnt(10)\n"
    "v_mfma_f32_16x16x32_bf16 v[192:195], v[16:19], v[180:183], v[192:195]\n"
    "ds_read_b128 v[216:219], v231 offset:22528\n"
    "v_mfma_f32_16x16x32_bf16 v[196:199], v[20:23], v[180:183], v[196:199]\n"
    "v_mfma_f32_16x16x32_bf16 v[200:203], v[24:27], v[180:183], v[200:203]\n"
    "ds_read_b128 v[220:223], v231 offset:23552\n"
    "v_mfma_f32_16x16x32_bf16 v[204:207], v[28:31], v[180:183], v[204:207]\n"
    "s_waitcnt vmcnt(9)\n"
    "v_mfma_f32_16x16x32_bf16 v[192:195], v[32:35], v[184:187], v[192:195]\n"
    "v_mfma_f32_16x16x32_bf16 v[196:199], v[36:39], v[184:187], v[196:199]\n"
    "v_mfma_f32_16x16x32_bf16 v[200:203], v[40:43], v[184:187], v[200:203]\n"
    "v_mfma_f32_16x16x32_bf16 v[204:207], v[44:47], v[184:187], v[204:207]\n"
    "s_waitcnt vmcnt(8)\n"
    "s_waitcnt lgkmcnt(3)\n"
    "v_mfma_f32_16x16x32_bf16 v[192:195], v[208:211], v[188:191], v[192:195]\n"
    "s_waitcnt lgkmcnt(2)\n"
    "v_mfma_f32_16x16x32_bf16 v[196:199], v[212:215], v[188:191], v[196:199]\n"
    "s_waitcnt lgkmcnt(1)\n"
    "v_mfma_f32_16x16x32_bf16 v[200:203], v[216:219], v[188:191], v[200:203]\n"
    "s_waitcnt lgkmcnt(0)\n"
    "v_mfma_f32_16x16x32_bf16 v[204:207], v[220:223], v[188:191], v[204:207]\n"
    "s_waitcnt vmcnt(7)\n"
    "v_mfma_f32_16x16x32_bf16 v[192:195], v[48:51], v[144:147], v[192:195]\n"
    "ds_read_b128 v[208:211], v231 offset:24576\n"
    "v_mfma_f32_16x16x32_bf16 v[196:199], v[52:55], v[144:147], v[196:199]\n"
    "v_mfma_f32_16x16x32_bf16 v[200:203], v[56:59], v[144:147], v[200:203]\n"
    "ds_read_b128 v[212:215], v231 offset:25600\n"
    "v_mfma_f32_16x16x32_bf16 v[204:207], v[60:63], v[144:147], v[204:207]\n"
    "s_waitcnt vmcnt(6)\n"
    "v_mfma_f32_16x16x32_bf16 v[192:195], v[64:67], v[148:151], v[192:195]\n"
    "ds_read_b128 v[216:219], v231 offset:26624\n"
    "v_mfma_f32_16x16x32_bf16 v[196:199], v[68:71], v[148:151], v[196:199]\n"
    "v_mfma_f32_16x16x32_bf16 v[200:203], v[72:75], v[148:151], v[200:203]\n"
    "ds_read_b128 v[220:223], v231 offset:27648\n"
    "v_mfma_f32_16x16x32_bf16 v[204:207], v[76:79], v[148:151], v[204:207]\n"
    "s_waitcnt vmcnt(5)\n"
    "v_mfma_f32_16x16x32_bf16 v[192:195], v[80:83], v[152:155], v[192:195]\n"
    "v_mfma_f32_16x16x32_bf16 v[196:199], v[84:87], v[152:155], v[196:199]\n"
    "v_mfma_f32_16x16x32_bf16 v[200:203], v[88:91], v[152:155], v[200:203]\n"
    "v_mfma_f32_16x16x32_bf16 v[204:207], v[92:95], v[152:155], v[204:207]\n"
    "s_waitcnt vmcnt(4)\n"
    "s_waitcnt lgkmcnt(3)\n"
    "v_mfma_f32_16x16x32_bf16 v[192:195], v[208:211], v[156:159], v[192:195]\n"
    "s_waitcnt lgkmcnt(2)\n"
    "v_mfma_f32_16x16x32_bf16 v[196:199], v[212:215], v[156:159], v[196:199]\n"
    "s_waitcnt lgkmcnt(1)\n"
    "v_mfma_f32_16x16x32_bf16 v[200:203], v[216:219], v[156:159], v[200:203]\n"
    "s_waitcnt lgkmcnt(0)\n"
    "v_mfma_f32_16x16x32_bf16 v[204:207], v[220:223], v[156:159], v[204:207]\n"
    "s_waitcnt vmcnt(3)\n"
    "v_mfma_f32_16x16x32_bf16 v[192:195], v[96:99], v[160:163], v[192:195]\n"
    "ds_read_b128 v[208:211], v231 offset:28672\n"
    "v_mfma_f32_16x16x32_bf16 v[196:199], v[100:103], v[160:163], v[196:199]\n"
    "v_mfma_f32_16x16x32_bf16 v[200:203], v[104:107], v[160:163], v[200:203]\n"
    "ds_read_b128 v[212:215], v231 offset:29696\n"
    "v_mfma_f32_16x16x32_bf16 v[204:207], v[108:111], v[160:163], v[204:207]\n"
    "s_waitcnt vmcnt(2)\n"
    "v_mfma_f32_16x16x32_bf16 v[192:195], v[112:115], v[164:167], v[192:195]\n"
    "ds_read_b128 v[216:219], v231 offset:30720\n"
    "v_mfma_f32_16x16x32_bf16 v[196:199], v[116:119], v[164:167], v[196:199]\n"
    "v_mfma_f32_16x16x32_bf16 v[200:203], v[120:123], v[164:167], v[200:203]\n"
    "ds_read_b128 v[220:223], v231 offset:31744\n"
    "v_mfma_f32_16x16x32_bf16 v[204:207], v[124:127], v[164:167], v[204:207]\n"
    "s_waitcnt vmcnt(1)\n"
    "v_mfma_f32_16x16x32_bf16 v[192:195], v[128:131], v[168:171], v[192:195]\n"
    "v_mfma_f32_16x16x32_bf16 v[196:199], v[132:135], v[168:171], v[196:199]\n"
    "v_mfma_f32_16x16x32_bf16 v[200:203], v[136:139], v[168:171], v[200:203]\n"
    "v_mfma_f32_16x16x32_bf16 v[204:207], v[140:143], v[168:171], v[204:207]\n"
    "s_waitcnt vmcnt(0)\n"
    "s_waitcnt lgkmcnt(3)\n"
    "v_mfma_f32_16x16x32_bf16 v[192:195], v[208:211], v[172:175], v[192:195]\n"
    "s_waitcnt lgkmcnt(2)\n"
    "v_mfma_f32_16x16x32_bf16 v[196:199], v[212:215], v[172:175], v[196:199]\n"
    "s_waitcnt lgkmcnt(1)\n"
    "v_mfma_f32_16x16x32_bf16 v[200:203], v[216:219], v[172:175], v[200:203]\n"
    "s_waitcnt lgkmcnt(0)\n"
    "v_mfma_f32_16x16x32_bf16 v[204:207], v[220:223], v[172:175], v[204:207]\n"
    "s_add_u32 s46, s46, 1048576\n"
    "s_addc_u32 s47, s47, 0\n"
    "s_and_b32 s41, s46, 0x100000\n"
    "s_cselect_b32 s41, 0, 8192\n"
    "s_xor_b32 s46, s46, s41\n"
    "v_lshlrev_b32 v160, 16, v224\n"
    "v_and_b32 v161, 0xffff0000, v224\n"
    "v_lshlrev_b32 v162, 16, v225\n"
    "v_and_b32 v163, 0xffff0000, v225\n"
    "ds_write_b128 %[vwr], v[192:195] offset:0\n"
    "ds_write_b128 %[vwr], v[196:199] offset:64\n"
    "ds_write_b128 %[vwr], v[200:203] offset:128\n"
    "ds_write_b128 %[vwr], v[204:207] offset:192\n"
    "s_waitcnt lgkmcnt(0)\n"
    "s_barrier\n"
    "ds_read_b128 v[144:147], %[vrr] offset:0\n"
    "ds_read_b128 v[148:151], %[vrr] offset:4352\n"
    "ds_read_b128 v[152:155], %[vrr] offset:8704\n"
    "ds_read_b128 v[156:159], %[vrr] offset:13056\n"
    "s_waitcnt lgkmcnt(2)\n"
    "v_pk_add_f32 v[144:145], v[144:145], v[148:149]\n"
    "s_waitcnt lgkmcnt(0)\n"
    "v_pk_add_f32 v[152:153], v[152:153], v[156:157]\n"
    "v_pk_add_f32 v[144:145], v[144:145], v[152:153]\n"
    "v_cvt_pk_bf16_f32 v144, v144, v145\n"
    "v_and_b32 v145, 0xffff0000, v144\n"
    "v_lshlrev_b32 v144, 16, v144\n"
    "v_pk_add_f32 v[144:145], v[144:145], v[160:161]\n"
    "v_cvt_pk_bf16_f32 v164, v144, v145\n"
    "v_pk_add_f32 v[146:147], v[146:147], v[150:151]\n"
    "v_pk_add_f32 v[154:155], v[154:155], v[158:159]\n"
    "v_pk_add_f32 v[146:147], v[146:147], v[154:155]\n"
    "v_cvt_pk_bf16_f32 v146, v146, v147\n"
    "v_and_b32 v147, 0xffff0000, v146\n"
    "v_lshlrev_b32 v146, 16, v146\n"
    "v_pk_add_f32 v[146:147], v[146:147], v[162:163]\n"
    "v_cvt_pk_bf16_f32 v165, v146, v147\n"
    "global_store_dwordx2 %[vhv], v[164:165], s[48:49] offset:0 sc1\n"
    "v_lshlrev_b32 v144, 16, v164\n"
    "v_and_b32 v145, 0xffff0000, v164\n"
    "v_lshlrev_b32 v146, 16, v165\n"
    "v_and_b32 v147, 0xffff0000, v165\n"
    "v_pk_add_f32 v[148:149], v[144:145], v[146:147]\n"
    "v_add_f32 v148, v148, v149\n"
    "s_nop 1\n"
    "v_add_f32_dpp v148, v148, v148 quad_perm:[1,0,3,2] row_mask:0xf bank_mask:0xf\n"
    "s_nop 1\n"
    "v_add_f32_dpp v148, v148, v148 quad_perm:[2,3,0,1] row_mask:0xf bank_mask:0xf\n"
    "s_nop 1\n"
    "v_add_f32_dpp v148, v148, v148 row_half_mirror row_mask:0xf bank_mask:0xf\n"
    "v_fmamk_f32 v144, v148, 0xbd000000, v144\n"
    "v_fmamk_f32 v145, v148, 0xbd000000, v145\n"
    "v_fmamk_f32 v146, v148, 0xbd000000, v146\n"
    "v_fmamk_f32 v147, v148, 0xbd000000, v147\n"
    "v_pk_mul_f32 v[150:151], v[144:145], v[144:145]\n"
    "v_pk_fma_f32 v[150:151], v[146:147], v[146:147], v[150:151]\n"
    "v_add_f32 v149, v150, v151\n"
    "s_nop 1\n"
    "v_add_f32_dpp v149, v149, v149 quad_perm:[1,0,3,2] row_mask:0xf bank_mask:0xf\n"
    "s_nop 1\n"
    "v_add_f32_dpp v149, v149, v149 quad_perm:[2,3,0,1] row_mask:0xf bank_mask:0xf\n"
    "s_nop 1\n"
    "v_add_f32_dpp v149, v149, v149 row_half_mirror row_mask:0xf bank_mask:0xf\n"
    "s_mov_b64 exec, s[62:63]\n"
    "global_store_dwordx2 %[vsv], v[148:149], s[58:59] offset:0 sc1\n"
    "s_mov_b64 exec, -1\n"
    "s_add_u32 s58, s58, 10240\n"
    "s_addc_u32 s59, s59, 0\n"
    "s_add_u32 s48, s48, 81920\n"
    "s_addc_u32 s49, s49, 0\n"
    "s_add_u32 s50, s50, 81920\n"
    "s_addc_u32 s51, s51, 0\n"
    "v_mov_b32 v228, s56\n"
    "s_waitcnt vmcnt(0)\n"
    "s_barrier\n"
    "s_cmp_lg_u32 %[w], 3\n"
    "s_cbranch_scc1 Pnf_%=\n"
    "s_mov_b64 exec, 1\n"
    "global_store_dword v229, v228, %[fo] offset:0 sc1\n"
    "s_mov_b64 exec, -1\n"
    "Pnf_%=:\n"
    "s_add_u32 s40, s40, 4\n"
    "s_cmp_lt_u32 s40, 128\n"
    "s_cbranch_scc1 Ploop_%=\n"
    "s_branch Pend_%=\n"
    "Pdead_%=:\n"
    "v_mov_b32 v254, 1280\n"
    "v_mov_b32 v255, 2098432\n"
    "global_store_dword v255, v254, %[ws] offset:0 sc1\n"
    "s_waitcnt vmcnt(0)\n"
    "s_endpgm\n"
    "Pend_%=:\n"
    "s_endpgm\n"
    "s_mov_b32 m0, s99\n"
    PROJ_OPS);
  } else {
    asm volatile(
    "s_mov_b32 s99, m0\n"
    "s_barrier\n"
    "s_lshl_b32 s60, %[w], 15\n"
    "s_add_u32 s60, s60, %[lb]\n"
    "v_add_u32 v158, 19456, %[vw]\n"
    "s_mov_b64 s[54:55], %[wp]\n"
    "s_mov_b64 s[44:45], %[wp]\n"
    "s_add_u32 m0, s60, 0\n"
    "s_nop 0\n"
    "global_load_lds_dwordx4 %[vw], s[44:45] offset:0\n"
    "global_load_lds_dwordx4 v158, s[44:45] offset:1024\n"
    "s_add_u32 s44, s44, 40960\n"
    "s_addc_u32 s45, s45, 0\n"
    "s_add_u32 m0, s60, 2048\n"
    "s_nop 0\n"
    "global_load_lds_dwordx4 %[vw], s[44:45] offset:0\n"
    "global_load_lds_dwordx4 v158, s[44:45] offset:1024\n"
    "s_add_u32 s44, s44, 40960\n"
    "s_addc_u32 s45, s45, 0\n"
    "s_add_u32 m0, s60, 4096\n"
    "s_nop 0\n"
    "global_load_lds_dwordx4 %[vw], s[44:45] offset:0\n"
    "global_load_lds_dwordx4 v158, s[44:45] offset:1024\n"
    "s_add_u32 s44, s44, 40960\n"
    "s_addc_u32 s45, s45, 0\n"
    "s_waitcnt vmcnt(4)\n"
    "ds_read_b64_tr_b16 a[0:1], %[vrd0] offset:0\n"
    "ds_read_b64_tr_b16 a[2:3], %[vrd0] offset:1024\n"
    "ds_read_b64_tr_b16 a[56:57], %[vrd1] offset:0\n"
    "ds_read_b64_tr_b16 a[58:59], %[vrd1] offset:1024\n"
    "s_waitcnt vmcnt(2)\n"
    "ds_read_b64_tr_b16 a[4:5], %[vrd0] offset:2048\n"
    "ds_read_b64_tr_b16 a[6:7], %[vrd0] offset:3072\n"
    "ds_read_b64_tr_b16 a[60:61], %[vrd1] offset:2048\n"
    "ds_read_b64_tr_b16 a[62:63], %[vrd1] offset:3072\n"
    "s_waitcnt lgkmcnt(4)\n"
    "s_add_u32 m0, s60, 0\n"
    "s_nop 0\n"
    "global_load_lds_dwordx4 %[vw], s[44:45] offset:0\n"
    "global_load_lds_dwordx4 v158, s[44:45] offset:1024\n"
    "s_add_u32 s44, s44, 40960\n"
    "s_addc_u32 s45, s45, 0\n"
    "s_waitcnt vmcnt(2)\n"
    "ds_read_b64_tr_b16 a[8:9], %[vrd0] offset:4096\n"
    "ds_read_b64_tr_b16 a[10:11], %[vrd0] offset:5120\n"
    "ds_read_b64_tr_b16 a[64:65], %[vrd1] offset:4096\n"
    "ds_read_b64_tr_b16 a[66:67], %[vrd1] offset:5120\n"
    "s_waitcnt lgkmcnt(4)\n"
    "s_add_u32 m0, s60, 2048\n"
    "s_nop 0\n"
    "global_load_lds_dwordx4 %[vw], s[44:45] offset:0\n"
    "global_load_lds_dwordx4 v158, s[44:45] offset:1024\n"
    "s_add_u32 s44, s44, 40960\n"
    "s_addc_u32 s45, s45, 0\n"
    "s_waitcnt vmcnt(2)\n"
    "ds_read_b64_tr_b16 a[12:13], %[vrd0] offset:0\n"
    "ds_read_b64_tr_b16 a[14:15], %[vrd0] offset:1024\n"
    "ds_read_b64_tr_b16 a[68:69], %[vrd1] offset:0\n"
    "ds_read_b64_tr_b16 a[70:71], %[vrd1] offset:1024\n"
    "s_waitcnt lgkmcnt(4)\n"
    "s_add_u32 m0, s60, 4096\n"
    "s_nop 0\n"
    "global_load_lds_dwordx4 %[vw], s[44:45] offset:0\n"
    "global_load_lds_dwordx4 v158, s[44:45] offset:1024\n"
    "s_add_u32 s44, s44, 40960\n"
    "s_addc_u32 s45, s45, 0\n"
    "s_waitcnt vmcnt(2)\n"
    "ds_read_b64_tr_b16 a[16:17], %[vrd0] offset:2048\n"
    "ds_read_b64_tr_b16 a[18:19], %[vrd0] offset:3072\n"
    "ds_read_b64_tr_b16 a[72:73], %[vrd1] offset:2048\n"
    "ds_read_b64_tr_b16 a[74:75], %[vrd1] offset:3072\n"
    "s_waitcnt lgkmcnt(4)\n"
    "s_add_u32 m0, s60, 0\n"
    "s_nop 0\n"
    "global_load_lds_dwordx4 %[vw], s[44:45] offset:0\n"
    "global_load_lds_dwordx4 v158, s[44:45] offset:1024\n"
    "s_add_u32 s44, s44, 40960\n"
    "s_addc_u32 s45, s45, 0\n"
    "s_waitcnt vmcnt(2)\n"
    "ds_read_b64_tr_b16 a[20:21], %[vrd0] offset:4096\n"
    "ds_read_b64_tr_b16 a[22:23], %[vrd0] offset:5120\n"
    "ds_read_b64_tr_b16 a[76:77], %[vrd1] offset:4096\n"
    "ds_read_b64_tr_b16 a[78:79], %[vrd1] offset:5120\n"
    "s_waitcnt lgkmcnt(4)\n"
    "s_add_u32 m0, s60, 2048\n"
    "s_nop 0\n"
    "global_load_lds_dwordx4 %[vw], s[44:45] offset:0\n"
    "global_load_lds_dwordx4 v158, s[44:45] offset:1024\n"
    "s_add_u32 s44, s44, 40960\n"
    "s_addc_u32 s45, s45, 0\n"
    "s_waitcnt vmcnt(2)\n"
    "ds_read_b64_tr_b16 a[24:25], %[vrd0] offset:0\n"
    "ds_read_b64_tr_b16 a[26:27], %[vrd0] offset:1024\n"
    "ds_read_b64_tr_b16 a[80:81], %[vrd1] offset:0\n"
    "ds_read_b64_tr_b16 a[82:83], %[vrd1] offset:1024\n"
    "s_waitcnt lgkmcnt(4)\n"
    "s_add_u32 m0, s60, 4096\n"
    "s_nop 0\n"
    "global_load_lds_dwordx4 %[vw], s[44:45] offset:0\n"
    "global_load_lds_dwordx4 v158, s[44:45] offset:1024\n"
    "s_add_u32 s44, s44, 40960\n"
    "s_addc_u32 s45, s45, 0\n"
    "s_waitcnt vmcnt(2)\n"
    "ds_read_b64_tr_b16 a[28:29], %[vrd0] offset:2048\n"
    "ds_read_b64_tr_b16 a[30:31], %[vrd0] offset:3072\n"
    "ds_read_b64_tr_b16 a[84:85], %[vrd1] offset:2048\n"
    "ds_read_b64_tr_b16 a[86:87], %[vrd1] offset:3072\n"
    "s_waitcnt lgkmcnt(4)\n"
    "s_add_u32 m0, s60, 0\n"
    "s_nop 0\n"
    "global_load_lds_dwordx4 %[vw], s[44:45] offset:0\n"
    "global_load_lds_dwordx4 v158, s[44:45] offset:1024\n"
    "s_add_u32 s44, s44, 40960\n"
    "s_addc_u32 s45, s45, 0\n"
    "s_waitcnt vmcnt(2)\n"
    "ds_read_b64_tr_b16 a[32:33], %[vrd0] offset:4096\n"
    "ds_read_b64_tr_b16 a[34:35], %[vrd0] offset:5120\n"
    "ds_read_b64_tr_b16 a[88:89], %[vrd1] offset:4096\n"
    "ds_read_b64_tr_b16 a[90:91], %[vrd1] offset:5120\n"
    "s_waitcnt lgkmcnt(4)\n"
    "s_add_u32 m0, s60, 2048\n"
    "s_nop 0\n"
    "global_load_lds_dwordx4 %[vw], s[44:45] offset:0\n"
    "global_load_lds_dwordx4 v158, s[44:45] offset:1024\n"
    "s_add_u32 s44, s44, 40960\n"
    "s_addc_u32 s45, s45, 0\n"
    "s_waitcnt vmcnt(2)\n"
    "ds_read_b64_tr_b16 a[36:37], %[vrd0] offset:0\n"
    "ds_read_b64_tr_b16 a[38:39], %[vrd0] offset:1024\n"
    "ds_read_b64_tr_b16 a[92:93], %[vrd1] offset:0\n"
    "ds_read_b64_tr_b16 a[94:95], %[vrd1] offset:1024\n"
    "s_waitcnt lgkmcnt(4)\n"
    "s_add_u32 m0, s60, 4096\n"
    "s_nop 0\n"
    "global_load_lds_dwordx4 %[vw], s[44:45] offset:0\n"
    "global_load_lds_dwordx4 v158, s[44:45] offset:1024\n"
    "s_add_u32 s44, s44, 40960\n"
    "s_addc_u32 s45, s45, 0\n"
    "s_waitcnt vmcnt(2)\n"
    "ds_read_b64_tr_b16 a[40:41], %[vrd0] offset:2048\n"
    "ds_read_b64_tr_b16 a[42:43], %[vrd0] offset:3072\n"
    "ds_read_b64_tr_b16 a[96:97], %[vrd1] offset:2048\n"
    "ds_read_b64_tr_b16 a[98:99], %[vrd1] offset:3072\n"
    "s_waitcnt lgkmcnt(4)\n"
    "s_add_u32 m0, s60, 0\n"
    "s_nop 0\n"
    "global_load_lds_dwordx4 %[vw], s[44:45] offset:0\n"
    "global_load_lds_dwordx4 v158, s[44:45] offset:1024\n"
    "s_add_u32 s44, s44, 40960\n"
    "s_addc_u32 s45, s45, 0\n"
    "s_waitcnt vmcnt(2)\n"
    "ds_read_b64_tr_b16 a[44:45], %[vrd0] offset:4096\n"
    "ds_read_b64_tr_b16 a[46:47], %[vrd0] offset:5120\n"
    "ds_read_b64_tr_b16 a[100:101], %[vrd1] offset:4096\n"
    "ds_read_b64_tr_b16 a[102:103], %[vrd1] offset:5120\n"
    "s_waitcnt lgkmcnt(4)\n"
    "s_add_u32 m0, s60, 2048\n"
    "s_nop 0\n"
    "global_load_lds_dwordx4 %[vw], s[44:45] offset:0\n"
    "global_load_lds_dwordx4 v158, s[44:45] offset:1024\n"
    "s_add_u32 s44, s44, 40960\n"
    "s_addc_u32 s45, s45, 0\n"
    "s_waitcnt vmcnt(2)\n"
    "ds_read_b64_tr_b16 a[48:49], %[vrd0] offset:0\n"
    "ds_read_b64_tr_b16 a[50:51], %[vrd0] offset:1024\n"
    "ds_read_b64_tr_b16 a[104:105], %[vrd1] offset:0\n"
    "ds_read_b64_tr_b16 a[106:107], %[vrd1] offset:1024\n"
    "s_waitcnt lgkmcnt(4)\n"
    "s_add_u32 s44, s54, 64\n"
    "s_addc_u32 s45, s55, 0\n"
    "s_add_u32 m0, s60, 4096\n"
    "s_nop 0\n"
    "global_load_lds_dwordx4 %[vw], s[44:45] offset:0\n"
    "global_load_lds_dwordx4 v158, s[44:45] offset:1024\n"
    "s_add_u32 s44, s44, 40960\n"
    "s_addc_u32 s45, s45, 0\n"
    "s_waitcnt vmcnt(2)\n"
    "ds_read_b64_tr_b16 a[52:53], %[vrd0] offset:2048\n"
    "ds_read_b64_tr_b16 a[54:55], %[vrd0] offset:3072\n"
    "ds_read_b64_tr_b16 a[108:109], %[vrd1] offset:2048\n"
    "ds_read_b64_tr_b16 a[110:111], %[vrd1] offset:3072\n"
    "s_waitcnt lgkmcnt(4)\n"
    "s_add_u32 m0, s60, 0\n"
    "s_nop 0\n"
    "global_load_lds_dwordx4 %[vw], s[44:45] offset:0\n"
    "global_load_lds_dwordx4 v158, s[44:45] offset:1024\n"
    "s_add_u32 s44, s44, 40960\n"
    "s_addc_u32 s45, s45, 0\n"
    "s_waitcnt vmcnt(2)\n"
    "ds_read_b64_tr_b16 a[112:113], %[vrd0] offset:4096\n"
    "ds_read_b64_tr_b16 a[114:115], %[vrd0] offset:5120\n"
    "ds_read_b64_tr_b16 a[168:169], %[vrd1] offset:4096\n"
    "ds_read_b64_tr_b16 a[170:171], %[vrd1] offset:5120\n"
    "s_waitcnt lgkmcnt(4)\n"
    "s_add_u32 m0, s60, 2048\n"
    "s_nop 0\n"
    "global_load_lds_dwordx4 %[vw], s[44:45] offset:0\n"
    "global_load_lds_dwordx4 v158, s[44:45] offset:1024\n"
    "s_add_u32 s44, s44, 40960\n"
    "s_addc_u32 s45, s45, 0\n"
    "s_waitcnt vmcnt(2)\n"
    "ds_read_b64_tr_b16 a[116:117], %[vrd0] offset:0\n"
    "ds_read_b64_tr_b16 a[118:119], %[vrd0] offset:1024\n"
    "ds_read_b64_tr_b16 a[172:173], %[vrd1] offset:0\n"
    "ds_read_b64_tr_b16 a[174:175], %[vrd1] offset:1024\n"
    "s_waitcnt lgkmcnt(4)\n"
    "s_add_u32 m0, s60, 4096\n"
    "s_nop 0\n"
    "global_load_lds_dwordx4 %[vw], s[44:45] offset:0\n"
    "global_load_lds_dwordx4 v158, s[44:45] offset:1024\n"
    "s_add_u32 s44, s44, 40960\n"
    "s_addc_u32 s45, s45, 0\n"
    "s_waitcnt vmcnt(2)\n"
    "ds_read_b64_tr_b16 a[120:121], %[vrd0] offset:2048\n"
    "ds_read_b64_tr_b16 a[122:123], %[vrd0] offset:3072\n"
    "ds_read_b64_tr_b16 a[176:177], %[vrd1] offset:2048\n"
    "ds_read_b64_tr_b16 a[178:179], %[vrd1] offset:3072\n"
    "s_waitcnt lgkmcnt(4)\n"
    "s_add_u32 m0, s60, 0\n"
    "s_nop 0\n"
    "global_load_lds_dwordx4 %[vw], s[44:45] offset:0\n"
    "global_load_lds_dwordx4 v158, s[44:45] offset:1024\n"
    "s_add_u32 s44, s44, 40960\n"
    "s_addc_u32 s45, s45, 0\n"
    "s_waitcnt vmcnt(2)\n"
    "ds_read_b64_tr_b16 a[124:125], %[vrd0] offset:4096\n"
    "ds_read_b64_tr_b16 a[126:127], %[vrd0] offset:5120\n"
    "ds_read_b64_tr_b16 a[180:181], %[vrd1] offset:4096\n"
    "ds_read_b64_tr_b16 a[182:183], %[vrd1] offset:5120\n"
    "s_waitcnt lgkmcnt(4)\n"
    "s_add_u32 m0, s60, 2048\n"
    "s_nop 0\n"
    "global_load_lds_dwordx4 %[vw], s[44:45] offset:0\n"
    "global_load_lds_dwordx4 v158, s[44:45] offset:1024\n"
    "s_add_u32 s44, s44, 40960\n"
    "s_addc_u32 s45, s45, 0\n"
    "s_waitcnt vmcnt(2)\n"
    "ds_read_b64_tr_b16 a[128:129], %[vrd0] offset:0\n"
    "ds_read_b64_tr_b16 a[130:131], %[vrd0] offset:1024\n"
    "ds_read_b64_tr_b16 a[184:185], %[vrd1] offset:0\n"
    "ds_read_b64_tr_b16 a[186:187], %[vrd1] offset:1024\n"
    "s_waitcnt lgkmcnt(4)\n"
    "s_add_u32 m0, s60, 4096\n"
    "s_nop 0\n"
    "global_load_lds_dwordx4 %[vw], s[44:45] offset:0\n"
    "global_load_lds_dwordx4 v158, s[44:45] offset:1024\n"
    "s_add_u32 s44, s44, 40960\n"
    "s_addc_u32 s45, s45, 0\n"
    "s_waitcnt vmcnt(2)\n"
    "ds_read_b64_tr_b16 a[132:133], %[vrd0] offset:2048\n"
    "ds_read_b64_tr_b16 a[134:135], %[vrd0] offset:3072\n"
    "ds_read_b64_tr_b16 a[188:189], %[vrd1] offset:2048\n"
    "ds_read_b64_tr_b16 a[190:191], %[vrd1] offset:3072\n"
    "s_waitcnt lgkmcnt(4)\n"
    "s_add_u32 m0, s60, 0\n"
    "s_nop 0\n"
    "global_load_lds_dwordx4 %[vw], s[44:45] offset:0\n"
    "global_load_lds_dwordx4 v158, s[44:45] offset:1024\n"
    "s_add_u32 s44, s44, 40960\n"
    "s_addc_u32 s45, s45, 0\n"
    "s_waitcnt vmcnt(2)\n"
    "ds_read_b64_tr_b16 a[136:137], %[vrd0] offset:4096\n"
    "ds_read_b64_tr_b16 a[138:139], %[vrd0] offset:5120\n"
    "ds_read_b64_tr_b16 a[192:193], %[vrd1] offset:4096\n"
    "ds_read_b64_tr_b16 a[194:195], %[vrd1] offset:5120\n"
    "s_waitcnt lgkmcnt(4)\n"
    "s_add_u32 m0, s60, 2048\n"
    "s_nop 0\n"
    "global_load_lds_dwordx4 %[vw], s[44:45] offset:0\n"
    "global_load_lds_dwordx4 v158, s[44:45] offset:1024\n"
    "s_add_u32 s44, s44, 40960\n"
    "s_addc_u32 s45, s45, 0\n"
    "s_waitcnt vmcnt(2)\n"
    "ds_read_b64_tr_b16 a[140:141], %[vrd0] offset:0\n"
    "ds_read_b64_tr_b16 a[142:143], %[vrd0] offset:1024\n"
    "ds_read_b64_tr_b16 a[196:197], %[vrd1] offset:0\n"
    "ds_read_b64_tr_b16 a[198:199], %[vrd1] offset:1024\n"
    "s_waitcnt lgkmcnt(4)\n"
    "s_add_u32 m0, s60, 4096\n"
    "s_nop 0\n"
    "global_load_lds_dwordx4 %[vw], s[44:45] offset:0\n"
    "global_load_lds_dwordx4 v158, s[44:45] offset:1024\n"
    "s_add_u32 s44, s44, 40960\n"
    "s_addc_u32 s45, s45, 0\n"
    "s_waitcnt vmcnt(2)\n"
    "ds_read_b64_tr_b16 a[144:145], %[vrd0] offset:2048\n"
    "ds_read_b64_tr_b16 a[146:147], %[vrd0] offset:3072\n"
    "ds_read_b64_tr_b16 a[200:201], %[vrd1] offset:2048\n"
    "ds_read_b64_tr_b16 a[202:203], %[vrd1] offset:3072\n"
    "s_waitcnt lgkmcnt(4)\n"
    "s_add_u32 m0, s60, 0\n"
    "s_nop 0\n"
    "global_load_lds_dwordx4 %[vw], s[44:45] offset:0\n"
    "global_load_lds_dwordx4 v158, s[44:45] offset:1024\n"
    "s_add_u32 s44, s44, 40960\n"
    "s_addc_u32 s45, s45, 0\n"
    "s_waitcnt vmcnt(2)\n"
    "ds_read_b64_tr_b16 a[148:149], %[vrd0] offset:4096\n"
    "ds_read_b64_tr_b16 a[150:151], %[vrd0] offset:5120\n"
    "ds_read_b64_tr_b16 a[204:205], %[vrd1] offset:4096\n"
    "ds_read_b64_tr_b16 a[206:207], %[vrd1] offset:5120\n"
    "s_waitcnt lgkmcnt(4)\n"
    "s_add_u32 m0, s60, 2048\n"
    "s_nop 0\n"
    "global_load_lds_dwordx4 %[vw], s[44:45] offset:0\n"
    "global_load_lds_dwordx4 v158, s[44:45] offset:1024\n"
    "s_add_u32 s44, s44, 40960\n"
    "s_addc_u32 s45, s45, 0\n"
    "s_waitcnt vmcnt(2)\n"
    "ds_read_b64_tr_b16 a[152:153], %[vrd0] offset:0\n"
    "ds_read_b64_tr_b16 a[154:155], %[vrd0] offset:1024\n"
    "ds_read_b64_tr_b16 a[208:209], %[vrd1] offset:0\n"
    "ds_read_b64_tr_b16 a[210:211], %[vrd1] offset:1024\n"
    "s_waitcnt lgkmcnt(4)\n"
    "s_add_u32 m0, s60, 4096\n"
    "s_nop 0\n"
    "global_load_lds_dwordx4 %[vw], s[44:45] offset:0\n"
    "global_load_lds_dwordx4 v158, s[44:45] offset:1024\n"
    "s_add_u32 s44, s44, 40960\n"
    "s_addc_u32 s45, s45, 0\n"
    "s_waitcnt vmcnt(2)\n"
    "ds_read_b64_tr_b16 a[156:157], %[vrd0] offset:2048\n"
    "ds_read_b64_tr_b16 a[158:159], %[vrd0] offset:3072\n"
    "ds_read_b64_tr_b16 a[212:213], %[vrd1] offset:2048\n"
    "ds_read_b64_tr_b16 a[214:215], %[vrd1] offset:3072\n"
    "s_waitcnt lgkmcnt(4)\n"
    "s_add_u32 m0, s60, 0\n"
    "s_nop 0\n"
    "global_load_lds_dwordx4 %[vw], s[44:45] offset:0\n"
    "global_load_lds_dwordx4 v158, s[44:45] offset:1024\n"
    "s_add_u32 s44, s44, 40960\n"
    "s_addc_u32 s45, s45, 0\n"
    "s_waitcnt vmcnt(2)\n"
    "ds_read_b64_tr_b16 a[160:161], %[vrd0] offset:4096\n"
    "ds_read_b64_tr_b16 a[162:163], %[vrd0] offset:5120\n"
    "ds_read_b64_tr_b16 a[216:217], %[vrd1] offset:4096\n"
    "ds_read_b64_tr_b16 a[218:219], %[vrd1] offset:5120\n"
    "s_waitcnt vmcnt(0)\n"
    "ds_read_b64_tr_b16 a[164:165], %[vrd0] offset:0\n"
    "ds_read_b64_tr_b16 a[166:167], %[vrd0] offset:1024\n"
    "ds_read_b64_tr_b16 a[220:221], %[vrd1] offset:0\n"
    "ds_read_b64_tr_b16 a[222:223], %[vrd1] offset:1024\n"
    "s_waitcnt lgkmcnt(0)\n"
    "s_cmp_lt_u32 %[w], 2\n"
    "s_cbranch_scc1 Dnz_%=\n"
    "v_accvgpr_write_b32 a0, 0\n"
    "v_accvgpr_write_b32 a1, 0\n"
    "v_accvgpr_write_b32 a2, 0\n"
    "v_accvgpr_write_b32 a3, 0\n"
    "v_accvgpr_write_b32 a56, 0\n"
    "v_accvgpr_write_b32 a57, 0\n"
    "v_accvgpr_write_b32 a58, 0\n"
    "v_accvgpr_write_b32 a59, 0\n"
    "v_accvgpr_write_b32 a112, 0\n"
    "v_accvgpr_write_b32 a113, 0\n"
    "v_accvgpr_write_b32 a114, 0\n"
    "v_accvgpr_write_b32 a115, 0\n"
    "v_accvgpr_write_b32 a168, 0\n"
    "v_accvgpr_write_b32 a169, 0\n"
    "v_accvgpr_write_b32 a170, 0\n"
    "v_accvgpr_write_b32 a171, 0\n"
    "Dnz_%=:\n"
    "s_barrier\n"
    "s_mov_b64 s[46:47], %[ag]\n"
    "s_mov_b64 s[48:49], %[hg]\n"
    "s_mov_b64 s[50:51], %[xg]\n"
    "s_mov_b64 s[52:53], %[ws]\n"
    "s_add_u32 s52, s52, 2129920\n"
    "s_addc_u32 s53, s53, 0\n"
    "s_mov_b32 s62, 2097152\n"
    "s_load_dwordx2 s[62:63], %[ws], s62\n"
    "s_waitcnt lgkmcnt(0)\n"
    "s_add_u32 s62, s62, 1\n"
    "s_and_b32 s63, s63, -8\n"
    "s_mov_b32 s40, %[u0]\n"
    "s_mov_b32 s43, 0\n"
    "v_mov_b32 v157, 0\n"
    "v_mov_b32 v154, %[vwr]\n"
    "v_mov_b32 v155, %[vrr]\n"
    "v_lshrrev_b32 v159, 2, %[vg16]\n"
    "v_add_u32 v159, %[lb], v159\n"
    "v_add_u32 v159, 20480, v159\n"
    "s_mov_b32 s57, 0\n"
    "s_mov_b32 s61, 0\n"
    "s_mov_b64 s[66:67], s[48:49]\n"
    "Dloop_%=:\n"
    "s_cmp_eq_u32 s57, 1\n"
    "s_cbranch_scc1 Dmul_%=\n"
    "s_add_u32 s56, s40, 2\n"
    "s_lshr_b32 s56, s56, 1\n"
    "s_bitcmp1_b32 s56, 0\n"
    "s_cselect_b64 s[64:65], 0, -1\n"
    "s_xor_b32 s64, s64, 0x07ffffff\n"
    "s_cmp_lg_u32 %[w], 0\n"
    "s_cbranch_scc1 Dr2_%=\n"
    "s_branch Dw1_%=\n"
    "Dw_%=:\n"
    "s_sleep 2\n"
    "s_add_u32 s43, s43, 1\n"
    "s_cmp_gt_u32 s43, 524288\n"
    "s_cbranch_scc1 Ddead_%=\n"
    "Dw1_%=:\n"
    "global_load_dword v153, %[vflv], s[52:53] sc1\n"
    "s_waitcnt vmcnt(0)\n"
    "v_cmp_gt_u32 vcc, s56, v153\n"
    "s_and_b64 vcc, vcc, s[64:65]\n"
    "s_cbranch_scc1 Dw_%=\n"
    "Dr2_%=:\n"
    "s_barrier\n"
    "s_mov_b64 s[54:55], s[46:47]\n"
    "global_load_dwordx4 v[16:19], %[vg16], s[54:55] offset:0 sc1\n"
    "global_load_dwordx4 v[20:23], %[vg16], s[54:55] offset:1024 sc1\n"
    "global_load_dwordx4 v[24:27], %[vg16], s[54:55] offset:2048 sc1\n"
    "global_load_dwordx4 v[28:31], %[vg16], s[54:55] offset:3072 sc1\n"
    "s_add_u32 s54, s54, 4096\n"
    "s_addc_u32 s55, s55, 0\n"
    "global_load_dwordx4 v[32:35], %[vg16], s[54:55] offset:0 sc1\n"
    "global_load_dwordx4 v[36:39], %[vg16], s[54:55] offset:1024 sc1\n"
    "global_load_dwordx4 v[40:43], %[vg16], s[54:55] offset:2048 sc1\n"
    "global_load_dwordx4 v[44:47], %[vg16], s[54:55] offset:3072 sc1\n"
    "s_add_u32 s54, s54, 4096\n"
    "s_addc_u32 s55, s55, 0\n"
    "global_load_dwordx4 v[48:51], %[vg16], s[54:55] offset:0 sc1\n"
    "global_load_dwordx4 v[52:55], %[vg16], s[54:55] offset:1024 sc1\n"
    "Dmul_%=:\n"
    "s_waitcnt vmcnt(9)\n"
    "v_mfma_f32_16x16x32_bf16 v[184:187], a[0:3], v[16:19], 0\n"
    "v_mfma_f32_16x16x32_bf16 v[188:191], a[56:59], v[16:19], 0\n"
    "global_load_dwordx4 v[56:59], %[vg16], s[54:55] offset:2048 sc1\n"
    "v_mfma_f32_16x16x32_bf16 v[192:195], a[112:115], v[16:19], 0\n"
    "v_mfma_f32_16x16x32_bf16 v[196:199], a[168:171], v[16:19], 0\n"
    "s_waitcnt vmcnt(9)\n"
    "v_mfma_f32_16x16x32_bf16 v[184:187], a[4:7], v[20:23], v[184:187]\n"
    "v_mfma_f32_16x16x32_bf16 v[188:191], a[60:63], v[20:23], v[188:191]\n"
    "global_load_dwordx4 v[60:63], %[vg16], s[54:55] offset:3072 sc1\n"
    "v_mfma_f32_16x16x32_bf16 v[192:195], a[116:119], v[20:23], v[192:195]\n"
    "v_mfma_f32_16x16x32_bf16 v[196:199], a[172:175], v[20:23], v[196:199]\n"
    "s_waitcnt vmcnt(9)\n"
    "v_mfma_f32_16x16x32_bf16 v[184:187], a[8:11], v[24:27], v[184:187]\n"
    "v_mfma_f32_16x16x32_bf16 v[188:191], a[64:67], v[24:27], v[188:191]\n"
    "s_add_u32 s54, s54, 4096\n"
    "s_addc_u32 s55, s55, 0\n"
    "global_load_dwordx4 v[64:67], %[vg16], s[54:55] offset:0 sc1\n"
    "v_mfma_f32_16x16x32_bf16 v[192:195], a[120:123], v[24:27], v[192:195]\n"
    "v_mfma_f32_16x16x32_bf16 v[196:199], a[176:179], v[24:27], v[196:199]\n"
    "global_load_dwordx2 v[150:151], %[vhv], s[66:67] offset:0 sc1\n"
    "s_add_u32 s66, s66, 61440\n"
    "s_addc_u32 s67, s67, 0\n"
    "s_waitcnt vmcnt(9)\n"
    "v_mfma_f32_16x16x32_bf16 v[184:187], a[12:15], v[28:31], v[184:187]\n"
    "v_mfma_f32_16x16x32_bf16 v[188:191], a[68:71], v[28:31], v[188:191]\n"
    "global_load_dwordx4 v[68:71], %[vg16], s[54:55] offset:1024 sc1\n"
    "v_mfma_f32_16x16x32_bf16 v[192:195], a[124:127], v[28:31], v[192:195]\n"
    "v_mfma_f32_16x16x32_bf16 v[196:199], a[180:183], v[28:31], v[196:199]\n"
    "s_cmp_lg_u32 %[w], 0\n"
    "s_cbranch_scc1 Dnl_%=\n"
    "global_load_dword v153, %[vflv], s[52:53] sc1\n"
    "Dnl_%=:\n"
    "s_waitcnt vmcnt(9)\n"
    "v_mfma_f32_16x16x32_bf16 v[184:187], a[16:19], v[32:35], v[184:187]\n"
    "v_mfma_f32_16x16x32_bf16 v[188:191], a[72:75], v[32:35], v[188:191]\n"
    "v_mfma_f32_16x16x32_bf16 v[192:195], a[128:131], v[32:35], v[192:195]\n"
    "v_mfma_f32_16x16x32_bf16 v[196:199], a[184:187], v[32:35], v[196:199]\n"
    "s_waitcnt vmcnt(8)\n"
    "v_mfma_f32_16x16x32_bf16 v[184:187], a[20:23], v[36:39], v[184:187]\n"
    "v_mfma_f32_16x16x32_bf16 v[188:191], a[76:79], v[36:39], v[188:191]\n"
    "v_mfma_f32_16x16x32_bf16 v[192:195], a[132:135], v[36:39], v[192:195]\n"
    "v_mfma_f32_16x16x32_bf16 v[196:199], a[188:191], v[36:39], v[196:199]\n"
    "s_waitcnt vmcnt(7)\n"
    "v_mfma_f32_16x16x32_bf16 v[184:187], a[24:27], v[40:43], v[184:187]\n"
    "v_mfma_f32_16x16x32_bf16 v[188:191], a[80:83], v[40:43], v[188:191]\n"
    "v_mfma_f32_16x16x32_bf16 v[192:195], a[136:139], v[40:43], v[192:195]\n"
    "v_mfma_f32_16x16x32_bf16 v[196:199], a[192:195], v[40:43], v[196:199]\n"
    "s_waitcnt vmcnt(6)\n"
    "v_mfma_f32_16x16x32_bf16 v[184:187], a[28:31], v[44:47], v[184:187]\n"
    "v_mfma_f32_16x16x32_bf16 v[188:191], a[84:87], v[44:47], v[188:191]\n"
    "v_mfma_f32_16x16x32_bf16 v[192:195], a[140:143], v[44:47], v[192:195]\n"
    "v_mfma_f32_16x16x32_bf16 v[196:199], a[196:199], v[44:47], v[196:199]\n"
    "s_waitcnt vmcnt(5)\n"
    "v_mfma_f32_16x16x32_bf16 v[184:187], a[32:35], v[48:51], v[184:187]\n"
    "v_mfma_f32_16x16x32_bf16 v[188:191], a[88:91], v[48:51], v[188:191]\n"
    "v_mfma_f32_16x16x32_bf16 v[192:195], a[144:147], v[48:51], v[192:195]\n"
    "v_mfma_f32_16x16x32_bf16 v[196:199], a[200:203], v[48:51], v[196:199]\n"
    "s_waitcnt vmcnt(4)\n"
    "v_mfma_f32_16x16x32_bf16 v[184:187], a[36:39], v[52:55], v[184:187]\n"
    "v_mfma_f32_16x16x32_bf16 v[188:191], a[92:95], v[52:55], v[188:191]\n"
    "v_mfma_f32_16x16x32_bf16 v[192:195], a[148:151], v[52:55], v[192:195]\n"
    "v_mfma_f32_16x16x32_bf16 v[196:199], a[204:207], v[52:55], v[196:199]\n"
    "s_waitcnt vmcnt(3)\n"
    "v_mfma_f32_16x16x32_bf16 v[184:187], a[40:43], v[56:59], v[184:187]\n"
    "v_mfma_f32_16x16x32_bf16 v[188:191], a[96:99], v[56:59], v[188:191]\n"
    "v_mfma_f32_16x16x32_bf16 v[192:195], a[152:155], v[56:59], v[192:195]\n"
    "v_mfma_f32_16x16x32_bf16 v[196:199], a[208:211], v[56:59], v[196:199]\n"
    "s_waitcnt vmcnt(2)\n"
    "v_mfma_f32_16x16x32_bf16 v[184:187], a[44:47], v[60:63], v[184:187]\n"
    "v_mfma_f32_16x16x32_bf16 v[188:191], a[100:103], v[60:63], v[188:191]\n"
    "v_mfma_f32_16x16x32_bf16 v[192:195], a[156:159], v[60:63], v[192:195]\n"
    "v_mfma_f32_16x16x32_bf16 v[196:199], a[212:215], v[60:63], v[196:199]\n"
    "s_waitcnt vmcnt(1)\n"
    "v_mfma_f32_16x16x32_bf16 v[184:187], a[48:51], v[64:67], v[184:187]\n"
    "v_mfma_f32_16x16x32_bf16 v[188:191], a[104:107], v[64:67], v[188:191]\n"
    "v_mfma_f32_16x16x32_bf16 v[192:195], a[160:163], v[64:67], v[192:195]\n"
    "v_mfma_f32_16x16x32_bf16 v[196:199], a[216:219], v[64:67], v[196:199]\n"
    "s_waitcnt vmcnt(0)\n"
    "v_mfma_f32_16x16x32_bf16 v[184:187], a[52:55], v[68:71], v[184:187]\n"
    "v_mfma_f32_16x16x32_bf16 v[188:191], a[108:111], v[68:71], v[188:191]\n"
    "v_mfma_f32_16x16x32_bf16 v[192:195], a[164:167], v[68:71], v[192:195]\n"
    "v_mfma_f32_16x16x32_bf16 v[196:199], a[220:223], v[68:71], v[196:199]\n"
    "s_add_u32 s46, s46, 165888\n"
    "s_addc_u32 s47, s47, 0\n"
    "s_add_u32 s56, s40, 5\n"
    "s_lshr_b32 s56, s56, 1\n"
    "s_bitcmp1_b32 s56, 0\n"
    "s_cselect_b64 s[64:65], 0, -1\n"
    "s_xor_b32 s64, s64, 0x07ffffff\n"
    "s_mov_b32 s57, 0\n"
    "v_lshlrev_b32 v176, 16, v150\n"
    "v_and_b32 v177, 0xffff0000, v150\n"
    "v_lshlrev_b32 v178, 16, v151\n"
    "v_and_b32 v179, 0xffff0000, v151\n"
    "ds_write_b128 v154, v[184:187] offset:0\n"
    "ds_write_b128 v154, v[188:191] offset:64\n"
    "ds_write_b128 v154, v[192:195] offset:128\n"
    "ds_write_b128 v154, v[196:199] offset:192\n"
    "s_cmp_lg_u32 %[w], 0\n"
    "s_cbranch_scc1 Dnw_%=\n"
    "ds_write_b32 v159, v153 offset:0\n"
    "Dnw_%=:\n"
    "s_waitcnt lgkmcnt(0)\n"
    "s_barrier\n"
    "ds_read_b32 v153, v159 offset:0\n"
    "ds_read_b128 v[160:163], v155 offset:0\n"
    "ds_read_b128 v[164:167], v155 offset:4352\n"
    "ds_read_b128 v[168:171], v155 offset:8704\n"
    "ds_read_b128 v[172:175], v155 offset:13056\n"
    "v_xor_b32 v154, 32768, v154\n"
    "v_xor_b32 v155, 32768, v155\n"
    "v_xor_b32 v159, 32768, v159\n"
    "s_waitcnt lgkmcnt(4)\n"
    "v_cmp_gt_u32 vcc, s56, v153\n"
    "s_and_b64 vcc, vcc, s[64:65]\n"
    "s_cbranch_scc1 Dni_%=\n"
    "s_mov_b64 s[54:55], s[46:47]\n"
    "global_load_dwordx4 v[16:19], %[vg16], s[54:55] offset:0 sc1\n"
    "global_load_dwordx4 v[20:23], %[vg16], s[54:55] offset:1024 sc1\n"
    "global_load_dwordx4 v[24:27], %[vg16], s[54:55] offset:2048 sc1\n"
    "global_load_dwordx4 v[28:31], %[vg16], s[54:55] offset:3072 sc1\n"
    "s_add_u32 s54, s54, 4096\n"
    "s_addc_u32 s55, s55, 0\n"
    "global_load_dwordx4 v[32:35], %[vg16], s[54:55] offset:0 sc1\n"
    "global_load_dwordx4 v[36:39], %[vg16], s[54:55] offset:1024 sc1\n"
    "global_load_dwordx4 v[40:43], %[vg16], s[54:55] offset:2048 sc1\n"
    "global_load_dwordx4 v[44:47], %[vg16], s[54:55] offset:3072 sc1\n"
    "s_add_u32 s54, s54, 4096\n"
    "s_addc_u32 s55, s55, 0\n"
    "global_load_dwordx4 v[48:51], %[vg16], s[54:55] offset:0 sc1\n"
    "global_load_dwordx4 v[52:55], %[vg16], s[54:55] offset:1024 sc1\n"
    "s_mov_b32 s57, 1\n"
    "Dni_%=:\n"
    "s_waitcnt lgkmcnt(2)\n"
    "v_pk_add_f32 v[160:161], v[160:161], v[164:165]\n"
    "s_waitcnt lgkmcnt(0)\n"
    "v_pk_add_f32 v[168:169], v[168:169], v[172:173]\n"
    "v_pk_add_f32 v[160:161], v[160:161], v[168:169]\n"
    "v_cvt_pk_bf16_f32 v160, v160, v161\n"
    "v_and_b32 v161, 0xffff0000, v160\n"
    "v_lshlrev_b32 v160, 16, v160\n"
    "v_pk_add_f32 v[160:161], v[160:161], v[176:177]\n"
    "v_cvt_pk_bf16_f32 v180, v160, v161\n"
    "v_pk_add_f32 v[162:163], v[162:163], v[166:167]\n"
    "v_pk_add_f32 v[170:171], v[170:171], v[174:175]\n"
    "v_pk_add_f32 v[162:163], v[162:163], v[170:171]\n"
    "v_cvt_pk_bf16_f32 v162, v162, v163\n"
    "v_and_b32 v163, 0xffff0000, v162\n"
    "v_lshlrev_b32 v162, 16, v162\n"
    "v_pk_add_f32 v[162:163], v[162:163], v[178:179]\n"
    "v_cvt_pk_bf16_f32 v181, v162, v163\n"
    "global_store_dwordx2 %[vxv], v[180:181], s[50:51] offset:0\n"
    "s_add_u32 s48, s48, 61440\n"
    "s_addc_u32 s49, s49, 0\n"
    "s_add_u32 s50, s50, 61440\n"
    "s_addc_u32 s51, s51, 0\n"
    "s_add_u32 s40, s40, 3\n"
    "s_cmp_lt_u32 s40, 128\n"
    "s_cbranch_scc1 Dloop_%=\n"
    "s_cmp_eq_u32 %[last], 0\n"
    "s_cbranch_scc1 Dend_%=\n"
    "v_mov_b32 v156, s62\n"
    "v_mov_b32 v157, 2097152\n"
    "s_mov_b64 exec, 1\n"
    "global_store_dword v157, v156, %[ws] offset:0 sc1\n"
    "s_mov_b64 exec, -1\n"
    "s_branch Dend_%=\n"
    "Ddead_%=:\n"
    "v_mov_b32 v254, 1536\n"
    "v_mov_b32 v255, 2098432\n"
    "global_store_dword v255, v254, %[ws] offset:0 sc1\n"
    "s_waitcnt vmcnt(0)\n"
    "s_endpgm\n"
    "Dend_%=:\n"
    "s_endpgm\n"
    "s_mov_b32 m0, s99\n"
    PROJ_OPS);
  }
}

DI void role_ffn1(const Params& p, int z, int j, lptr L, int lane, int w) {
  const u32 lb = __builtin_amdgcn_readfirstlane((u32)(size_t)L);
  const int G = lane >> 4, fh = w & 1, t = w >> 1;
  const size_t col = (size_t)j * 128;
  asm volatile(
    "s_mov_b32 s99, m0\n"
    "s_barrier\n"
    "s_lshl_b32 s60, %[w], 10\n"
    "s_add_u32 s60, s60, %[lb]\n"
    "s_mov_b64 s[44:45], %[wg]\n"
    "s_add_u32 m0, s60, 0\n"
    "s_nop 0\n"
    "global_load_lds_dwordx4 %[vw], s[44:45] offset:0 nt\n"
    "s_add_u32 s44, s44, 110592\n"
    "s_addc_u32 s45, s45, 0\n"
    "s_add_u32 m0, s60, 4096\n"
    "s_nop 0\n"
    "global_load_lds_dwordx4 %[vw], s[44:45] offset:0 nt\n"
    "s_add_u32 s44, s44, 110592\n"
    "s_addc_u32 s45, s45, 0\n"
    "s_add_u32 m0, s60, 8192\n"
    "s_nop 0\n"
    "global_load_lds_dwordx4 %[vw], s[44:45] offset:0 nt\n"
    "s_add_u32 s44, s44, 110592\n"
    "s_addc_u32 s45, s45, 0\n"
    "s_add_u32 m0, s60, 12288\n"
    "s_nop 0\n"
    "global_load_lds_dwordx4 %[vw], s[44:45] offset:0 nt\n"
    "s_add_u32 s44, s44, 110592\n"
    "s_addc_u32 s45, s45, 0\n"
    "s_waitcnt vmcnt(2)\n"
    "s_waitcnt lgkmcnt(0)\n"
    "s_barrier\n"
    "s_add_u32 m0, s60, 16384\n"
    "s_nop 0\n"
    "global_load_lds_dwordx4 %[vw], s[44:45] offset:0 nt\n"
    "s_add_u32 s44, s44, 110592\n"
    "s_addc_u32 s45, s45, 0\n"
    "s_add_u32 m0, s60, 20480\n"
    "s_nop 0\n"
    "global_load_lds_dwordx4 %[vw], s[44:45] offset:0 nt\n"
    "s_add_u32 s44, s44, 110592\n"
    "s_addc_u32 s45, s45, 0\n"
    "ds_read_b64_tr_b16 a[0:1], %[vrd0] offset:0\n"
    "ds_read_b64_tr_b16 a[2:3], %[vrd0] offset:512\n"
    "ds_read_b64_tr_b16 a[80:81], %[vrd1] offset:0\n"
    "ds_read_b64_tr_b16 a[82:83], %[vrd1] offset:512\n"
    "ds_read_b64_tr_b16 a[4:5], %[vrd0] offset:4096\n"
    "ds_read_b64_tr_b16 a[6:7], %[vrd0] offset:4608\n"
    "ds_read_b64_tr_b16 a[84:85], %[vrd1] offset:4096\n"
    "ds_read_b64_tr_b16 a[86:87], %[vrd1] offset:4608\n"
    "s_waitcnt vmcnt(2)\n"
    "s_waitcnt lgkmcnt(0)\n"
    "s_barrier\n"
    "s_add_u32 m0, s60, 0\n"
    "s_nop 0\n"
    "global_load_lds_dwordx4 %[vw], s[44:45] offset:0 nt\n"
    "s_add_u32 s44, s44, 110592\n"
    "s_addc_u32 s45, s45, 0\n"
    "s_add_u32 m0, s60, 4096\n"
    "s_nop 0\n"
    "global_load_lds_dwordx4 %[vw], s[44:45] offset:0 nt\n"
    "s_add_u32 s44, s44, 110592\n"
    "s_addc_u32 s45, s45, 0\n"
    "ds_read_b64_tr_b16 a[8:9], %[vrd0] offset:8192\n"
    "ds_read_b64_tr_b16 a[10:11], %[vrd0] offset:8704\n"
    "ds_read_b64_tr_b16 a[88:89], %[vrd1] offset:8192\n"
    "ds_read_b64_tr_b16 a[90:91], %[vrd1] offset:8704\n"
    "ds_read_b64_tr_b16 a[12:13], %[vrd0] offset:12288\n"
    "ds_read_b64_tr_b16 a[14:15], %[vrd0] offset:12800\n"
    "ds_read_b64_tr_b16 a[92:93], %[vrd1] offset:12288\n"
    "ds_read_b64_tr_b16 a[94:95], %[vrd1] offset:12800\n"
    "s_waitcnt vmcnt(2)\n"
    "s_waitcnt lgkmcnt(0)\n"
    "s_barrier\n"
    "s_add_u32 m0, s60, 8192\n"
    "s_nop 0\n"
    "global_load_lds_dwordx4 %[vw], s[44:45] offset:0 nt\n"
    "s_add_u32 s44, s44, 110592\n"
    "s_addc_u32 s45, s45, 0\n"
    "s_add_u32 m0, s60, 12288\n"
    "s_nop 0\n"
    "global_load_lds_dwordx4 %[vw], s[44:45] offset:0 nt\n"
    "s_add_u32 s44, s44, 110592\n"
    "s_addc_u32 s45, s45, 0\n"
    "ds_read_b64_tr_b16 a[16:17], %[vrd0] offset:16384\n"
    "ds_read_b64_tr_b16 a[18:19], %[vrd0] offset:16896\n"
    "ds_read_b64_tr_b16 a[96:97], %[vrd1] offset:16384\n"
    "ds_read_b64_tr_b16 a[98:99], %[vrd1] offset:16896\n"
    "ds_read_b64_tr_b16 a[20:21], %[vrd0] offset:20480\n"
    "ds_read_b64_tr_b16 a[22:23], %[vrd0] offset:20992\n"
    "ds_read_b64_tr_b16 a[100:101], %[vrd1] offset:20480\n"
    "ds_read_b64_tr_b16 a[102:103], %[vrd1] offset:20992\n"
    "s_waitcnt vmcnt(2)\n"
    "s_waitcnt lgkmcnt(0)\n"
    "s_barrier\n"
    "s_add_u32 m0, s60, 16384\n"
    "s_nop 0\n"
    "global_load_lds_dwordx4 %[vw], s[44:45] offset:0 nt\n"
    "s_add_u32 s44, s44, 110592\n"
    "s_addc_u32 s45, s45, 0\n"
    "s_add_u32 m0, s60, 20480\n"
    "s_nop 0\n"
    "global_load_lds_dwordx4 %[vw], s[44:45] offset:0 nt\n"
    "s_add_u32 s44, s44, 110592\n"
    "s_addc_u32 s45, s45, 0\n"
    "ds_read_b64_tr_b16 a[24:25], %[vrd0] offset:0\n"
    "ds_read_b64_tr_b16 a[26:27], %[vrd0] offset:512\n"
    "ds_read_b64_tr_b16 a[104:105], %[vrd1] offset:0\n"
    "ds_read_b64_tr_b16 a[106:107], %[vrd1] offset:512\n"
    "ds_read_b64_tr_b16 a[28:29], %[vrd0] offset:4096\n"
    "ds_read_b64_tr_b16 a[30:31], %[vrd0] offset:4608\n"
    "ds_read_b64_tr_b16 a[108:109], %[vrd1] offset:4096\n"
    "ds_read_b64_tr_b16 a[110:111], %[vrd1] offset:4608\n"
    "s_waitcnt vmcnt(2)\n"
    "s_waitcnt lgkmcnt(0)\n"
    "s_barrier\n"
    "s_add_u32 m0, s60, 0\n"
    "s_nop 0\n"
    "global_load_lds_dwordx4 %[vw], s[44:45] offset:0 nt\n"
    "s_add_u32 s44, s44, 110592\n"
    "s_addc_u32 s45, s45, 0\n"
    "s_add_u32 m0, s60, 4096\n"
    "s_nop 0\n"
    "global_load_lds_dwordx4 %[vw], s[44:45] offset:0 nt\n"
    "s_add_u32 s44, s44, 110592\n"
    "s_addc_u32 s45, s45, 0\n"
    "ds_read_b64_tr_b16 a[32:33], %[vrd0] offset:8192\n"
    "ds_read_b64_tr_b16 a[34:35], %[vrd0] offset:8704\n"
    "ds_read_b64_tr_b16 a[112:113], %[vrd1] offset:8192\n"
    "ds_read_b64_tr_b16 a[114:115], %[vrd1] offset:8704\n"
    "ds_read_b64_tr_b16 a[36:37], %[vrd0] offset:12288\n"
    "ds_read_b64_tr_b16 a[38:39], %[vrd0] offset:12800\n"
    "ds_read_b64_tr_b16 a[116:117], %[vrd1] offset:12288\n"
    "ds_read_b64_tr_b16 a[118:119], %[vrd1] offset:12800\n"
    "s_waitcnt vmcnt(2)\n"
    "s_waitcnt lgkmcnt(0)\n"
    "s_barrier\n"
    "s_add_u32 m0, s60, 8192\n"
    "s_nop 0\n"
    "global_load_lds_dwordx4 %[vw], s[44:45] offset:0 nt\n"
    "s_add_u32 s44, s44, 110592\n"
    "s_addc_u32 s45, s45, 0\n"
    "s_add_u32 m0, s60, 12288\n"
    "s_nop 0\n"
    "global_load_lds_dwordx4 %[vw], s[44:45] offset:0 nt\n"
    "s_add_u32 s44, s44, 110592\n"
    "s_addc_u32 s45, s45, 0\n"
    "ds_read_b64_tr_b16 a[40:41], %[vrd0] offset:16384\n"
    "ds_read_b64_tr_b16 a[42:43], %[vrd0] offset:16896\n"
    "ds_read_b64_tr_b16 a[120:121], %[vrd1] offset:16384\n"
    "ds_read_b64_tr_b16 a[122:123], %[vrd1] offset:16896\n"
    "ds_read_b64_tr_b16 a[44:45], %[vrd0] offset:20480\n"
    "ds_read_b64_tr_b16 a[46:47], %[vrd0] offset:20992\n"
    "ds_read_b64_tr_b16 a[124:125], %[vrd1] offset:20480\n"
    "ds_read_b64_tr_b16 a[126:127], %[vrd1] offset:20992\n"
    "s_waitcnt vmcnt(2)\n"
    "s_waitcnt lgkmcnt(0)\n"
    "s_barrier\n"
    "s_add_u32 m0, s60, 16384\n"
    "s_nop 0\n"
    "global_load_lds_dwordx4 %[vw], s[44:45] offset:0 nt\n"
    "s_add_u32 s44, s44, 110592\n"
    "s_addc_u32 s45, s45, 0\n"
    "s_add_u32 m0, s60, 20480\n"
    "s_nop 0\n"
    "global_load_lds_dwordx4 %[vw], s[44:45] offset:0 nt\n"
    "s_add_u32 s44, s44, 110592\n"
    "s_addc_u32 s45, s45, 0\n"
    "ds_read_b64_tr_b16 a[48:49], %[vrd0] offset:0\n"
    "ds_read_b64_tr_b16 a[50:51], %[vrd0] offset:512\n"
    "ds_read_b64_tr_b16 a[128:129], %[vrd1] offset:0\n"
    "ds_read_b64_tr_b16 a[130:131], %[vrd1] offset:512\n"
    "ds_read_b64_tr_b16 a[52:53], %[vrd0] offset:4096\n"
    "ds_read_b64_tr_b16 a[54:55], %[vrd0] offset:4608\n"
    "ds_read_b64_tr_b16 a[132:133], %[vrd1] offset:4096\n"
    "ds_read_b64_tr_b16 a[134:135], %[vrd1] offset:4608\n"
    "s_waitcnt vmcnt(2)\n"
    "s_waitcnt lgkmcnt(0)\n"
    "s_barrier\n"
    "s_add_u32 m0, s60, 0\n"
    "s_nop 0\n"
    "global_load_lds_dwordx4 %[vw], s[44:45] offset:0 nt\n"
    "s_add_u32 s44, s44, 110592\n"
    "s_addc_u32 s45, s45, 0\n"
    "s_add_u32 m0, s60, 4096\n"
    "s_nop 0\n"
    "global_load_lds_dwordx4 %[vw], s[44:45] offset:0 nt\n"
    "s_add_u32 s44, s44, 110592\n"
    "s_addc_u32 s45, s45, 0\n"
    "ds_read_b64_tr_b16 a[56:57], %[vrd0] offset:8192\n"
    "ds_read_b64_tr_b16 a[58:59], %[vrd0] offset:8704\n"
    "ds_read_b64_tr_b16 a[136:137], %[vrd1] offset:8192\n"
    "ds_read_b64_tr_b16 a[138:139], %[vrd1] offset:8704\n"
    "ds_read_b64_tr_b16 a[60:61], %[vrd0] offset:12288\n"
    "ds_read_b64_tr_b16 a[62:63], %[vrd0] offset:12800\n"
    "ds_read_b64_tr_b16 a[140:141], %[vrd1] offset:12288\n"
    "ds_read_b64_tr_b16 a[142:143], %[vrd1] offset:12800\n"
    "s_waitcnt vmcnt(2)\n"
    "s_waitcnt lgkmcnt(0)\n"
    "s_barrier\n"
    "s_mov_b64 s[44:45], %[wu]\n"
    "s_add_u32 m0, s60, 8192\n"
    "s_nop 0\n"
    "global_load_lds_dwordx4 %[vw], s[44:45] offset:0 nt\n"
    "s_add_u32 s44, s44, 110592\n"
    "s_addc_u32 s45, s45, 0\n"
    "s_add_u32 m0, s60, 12288\n"
    "s_nop 0\n"
    "global_load_lds_dwordx4 %[vw], s[44:45] offset:0 nt\n"
    "s_add_u32 s44, s44, 110592\n"
    "s_addc_u32 s45, s45, 0\n"
    "ds_read_b64_tr_b16 a[64:65], %[vrd0] offset:16384\n"
    "ds_read_b64_tr_b16 a[66:67], %[vrd0] offset:16896\n"
    "ds_read_b64_tr_b16 a[144:145], %[vrd1] offset:16384\n"
    "ds_read_b64_tr_b16 a[146:147], %[vrd1] offset:16896\n"
    "ds_read_b64_tr_b16 a[68:69], %[vrd0] offset:20480\n"
    "ds_read_b64_tr_b16 a[70:71], %[vrd0] offset:20992\n"
    "ds_read_b64_tr_b16 a[148:149], %[vrd1] offset:20480\n"
    "ds_read_b64_tr_b16 a[150:151], %[vrd1] offset:20992\n"
    "s_waitcnt vmcnt(2)\n"
    "s_waitcnt lgkmcnt(0)\n"
    "s_barrier\n"
    "s_add_u32 m0, s60, 16384\n"
    "s_nop 0\n"
    "global_load_lds_dwordx4 %[vw], s[44:45] offset:0 nt\n"
    "s_add_u32 s44, s44, 110592\n"
    "s_addc_u32 s45, s45, 0\n"
    "s_add_u32 m0, s60, 20480\n"
    "s_nop 0\n"
    "global_load_lds_dwordx4 %[vw], s[44:45] offset:0 nt\n"
    "s_add_u32 s44, s44, 110592\n"
    "s_addc_u32 s45, s45, 0\n"
    "ds_read_b64_tr_b16 a[72:73], %[vrd0] offset:0\n"
    "ds_read_b64_tr_b16 a[74:75], %[vrd0] offset:512\n"
    "ds_read_b64_tr_b16 a[152:153], %[vrd1] offset:0\n"
    "ds_read_b64_tr_b16 a[154:155], %[vrd1] offset:512\n"
    "ds_read_b64_tr_b16 a[76:77], %[vrd0] offset:4096\n"
    "ds_read_b64_tr_b16 a[78:79], %[vrd0] offset:4608\n"
    "ds_read_b64_tr_b16 a[156:157], %[vrd1] offset:4096\n"
    "ds_read_b64_tr_b16 a[158:159], %[vrd1] offset:4608\n"
    "s_waitcnt vmcnt(2)\n"
    "s_waitcnt lgkmcnt(0)\n"
    "s_barrier\n"
    "s_add_u32 m0, s60, 0\n"
    "s_nop 0\n"
    "global_load_lds_dwordx4 %[vw], s[44:45] offset:0 nt\n"
    "s_add_u32 s44, s44, 110592\n"
    "s_addc_u32 s45, s45, 0\n"
    "s_add_u32 m0, s60, 4096\n"
    "s_nop 0\n"
    "global_load_lds_dwordx4 %[vw], s[44:45] offset:0 nt\n"
    "s_add_u32 s44, s44, 110592\n"
    "s_addc_u32 s45, s45, 0\n"
    "ds_read_b64_tr_b16 a[160:161], %[vrd0] offset:8192\n"
    "ds_read_b64_tr_b16 a[162:163], %[vrd0] offset:8704\n"
    "ds_read_b64_tr_b16 a[240:241], %[vrd1] offset:8192\n"
    "ds_read_b64_tr_b16 a[242:243], %[vrd1] offset:8704\n"
    "ds_read_b64_tr_b16 a[164:165], %[vrd0] offset:12288\n"
    "ds_read_b64_tr_b16 a[166:167], %[vrd0] offset:12800\n"
    "ds_read_b64_tr_b16 a[244:245], %[vrd1] offset:12288\n"
    "ds_read_b64_tr_b16 a[246:247], %[vrd1] offset:12800\n"
    "s_waitcnt vmcnt(2)\n"
    "s_waitcnt lgkmcnt(0)\n"
    "s_barrier\n"
    "s_add_u32 m0, s60, 8192\n"
    "s_nop 0\n"
    "global_load_lds_dwordx4 %[vw], s[44:45] offset:0 nt\n"
    "s_add_u32 s44, s44, 110592\n"
    "s_addc_u32 s45, s45, 0\n"
    "s_add_u32 m0, s60, 12288\n"
    "s_nop 0\n"
    "global_load_lds_dwordx4 %[vw], s[44:45] offset:0 nt\n"
    "s_add_u32 s44, s44, 110592\n"
    "s_addc_u32 s45, s45, 0\n"
    "ds_read_b64_tr_b16 a[168:169], %[vrd0] offset:16384\n"
    "ds_read_b64_tr_b16 a[170:171], %[vrd0] offset:16896\n"
    "ds_read_b64_tr_b16 a[248:249], %[vrd1] offset:16384\n"
    "ds_read_b64_tr_b16 a[250:251], %[vrd1] offset:16896\n"
    "ds_read_b64_tr_b16 a[172:173], %[vrd0] offset:20480\n"
    "ds_read_b64_tr_b16 a[174:175], %[vrd0] offset:20992\n"
    "ds_read_b64_tr_b16 a[252:253], %[vrd1] offset:20480\n"
    "ds_read_b64_tr_b16 a[254:255], %[vrd1] offset:20992\n"
    "s_waitcnt vmcnt(2)\n"
    "s_waitcnt lgkmcnt(0)\n"
    "s_barrier\n"
    "s_add_u32 m0, s60, 16384\n"
    "s_nop 0\n"
    "global_load_lds_dwordx4 %[vw], s[44:45] offset:0 nt\n"
    "s_add_u32 s44, s44, 110592\n"
    "s_addc_u32 s45, s45, 0\n"
    "s_add_u32 m0, s60, 20480\n"
    "s_nop 0\n"
    "global_load_lds_dwordx4 %[vw], s[44:45] offset:0 nt\n"
    "s_add_u32 s44, s44, 110592\n"
    "s_addc_u32 s45, s45, 0\n"
    "ds_read_b64_tr_b16 a[176:177], %[vrd0] offset:0\n"
    "ds_read_b64_tr_b16 a[178:179], %[vrd0] offset:512\n"
    "ds_read_b64_tr_b16 v[16:17], %[vrd1] offset:0\n"
    "ds_read_b64_tr_b16 v[18:19], %[vrd1] offset:512\n"
    "ds_read_b64_tr_b16 a[180:181], %[vrd0] offset:4096\n"
    "ds_read_b64_tr_b16 a[182:183], %[vrd0] offset:4608\n"
    "ds_read_b64_tr_b16 v[20:21], %[vrd1] offset:4096\n"
    "ds_read_b64_tr_b16 v[22:23], %[vrd1] offset:4608\n"
    "s_waitcnt vmcnt(2)\n"
    "s_waitcnt lgkmcnt(0)\n"
    "s_barrier\n"
    "s_add_u32 m0, s60, 0\n"
    "s_nop 0\n"
    "global_load_lds_dwordx4 %[vw], s[44:45] offset:0 nt\n"
    "s_add_u32 s44, s44, 110592\n"
    "s_addc_u32 s45, s45, 0\n"
    "s_add_u32 m0, s60, 4096\n"
    "s_nop 0\n"
    "global_load_lds_dwordx4 %[vw], s[44:45] offset:0 nt\n"
    "s_add_u32 s44, s44, 110592\n"
    "s_addc_u32 s45, s45, 0\n"
    "ds_read_b64_tr_b16 a[184:185], %[vrd0] offset:8192\n"
    "ds_read_b64_tr_b16 a[186:187], %[vrd0] offset:8704\n"
    "ds_read_b64_tr_b16 v[24:25], %[vrd1] offset:8192\n"
    "ds_read_b64_tr_b16 v[26:27], %[vrd1] offset:8704\n"
    "ds_read_b64_tr_b16 a[188:189], %[vrd0] offset:12288\n"
    "ds_read_b64_tr_b16 a[190:191], %[vrd0] offset:12800\n"
    "ds_read_b64_tr_b16 v[28:29], %[vrd1] offset:12288\n"
    "ds_read_b64_tr_b16 v[30:31], %[vrd1] offset:12800\n"
    "s_waitcnt vmcnt(2)\n"
    "s_waitcnt lgkmcnt(0)\n"
    "s_barrier\n"
    "s_add_u32 m0, s60, 8192\n"
    "s_nop 0\n"
    "global_load_lds_dwordx4 %[vw], s[44:45] offset:0 nt\n"
    "s_add_u32 s44, s44, 110592\n"
    "s_addc_u32 s45, s45, 0\n"
    "s_add_u32 m0, s60, 12288\n"
    "s_nop 0\n"
    "global_load_lds_dwordx4 %[vw], s[44:45] offset:0 nt\n"
    "s_add_u32 s44, s44, 110592\n"
    "s_addc_u32 s45, s45, 0\n"
    "ds_read_b64_tr_b16 a[192:193], %[vrd0] offset:16384\n"
    "ds_read_b64_tr_b16 a[194:195], %[vrd0] offset:16896\n"
    "ds_read_b64_tr_b16 v[32:33], %[vrd1] offset:16384\n"
    "ds_read_b64_tr_b16 v[34:35], %[vrd1] offset:16896\n"
    "ds_read_b64_tr_b16 a[196:197], %[vrd0] offset:20480\n"
    "ds_read_b64_tr_b16 a[198:199], %[vrd0] offset:20992\n"
    "ds_read_b64_tr_b16 v[36:37], %[vrd1] offset:20480\n"
    "ds_read_b64_tr_b16 v[38:39], %[vrd1] offset:20992\n"
    "s_waitcnt vmcnt(2)\n"
    "s_waitcnt lgkmcnt(0)\n"
    "s_barrier\n"
    "s_add_u32 m0, s60, 16384\n"
    "s_nop 0\n"
    "global_load_lds_dwordx4 %[vw], s[44:45] offset:0 nt\n"
    "s_add_u32 s44, s44, 110592\n"
    "s_addc_u32 s45, s45, 0\n"
    "s_add_u32 m0, s60, 20480\n"
    "s_nop 0\n"
    "global_load_lds_dwordx4 %[vw], s[44:45] offset:0 nt\n"
    "s_add_u32 s44, s44, 110592\n"
    "s_addc_u32 s45, s45, 0\n"
    "ds_read_b64_tr_b16 a[200:201], %[vrd0] offset:0\n"
    "ds_read_b64_tr_b16 a[202:203], %[vrd0] offset:512\n"
    "ds_read_b64_tr_b16 v[40:41], %[vrd1] offset:0\n"
    "ds_read_b64_tr_b16 v[42:43], %[vrd1] offset:512\n"
    "ds_read_b64_tr_b16 a[204:205], %[vrd0] offset:4096\n"
    "ds_read_b64_tr_b16 a[206:207], %[vrd0] offset:4608\n"
    "ds_read_b64_tr_b16 v[44:45], %[vrd1] offset:4096\n"
    "ds_read_b64_tr_b16 v[46:47], %[vrd1] offset:4608\n"
    "s_waitcnt vmcnt(2)\n"
    "s_waitcnt lgkmcnt(0)\n"
    "s_barrier\n"
    "s_add_u32 m0, s60, 0\n"
    "s_nop 0\n"
    "global_load_lds_dwordx4 %[vw], s[44:45] offset:0 nt\n"
    "s_add_u32 s44, s44, 110592\n"
    "s_addc_u32 s45, s45, 0\n"
    "s_add_u32 m0, s60, 4096\n"
    "s_nop 0\n"
    "global_load_lds_dwordx4 %[vw], s[44:45] offset:0 nt\n"
    "s_add_u32 s44, s44, 110592\n"
    "s_addc_u32 s45, s45, 0\n"
    "ds_read_b64_tr_b16 a[208:209], %[vrd0] offset:8192\n"
    "ds_read_b64_tr_b16 a[210:211], %[vrd0] offset:8704\n"
    "ds_read_b64_tr_b16 v[48:49], %[vrd1] offset:8192\n"
    "ds_read_b64_tr_b16 v[50:51], %[vrd1] offset:8704\n"
    "ds_read_b64_tr_b16 a[212:213], %[vrd0] offset:12288\n"
    "ds_read_b64_tr_b16 a[214:215], %[vrd0] offset:12800\n"
    "ds_read_b64_tr_b16 v[52:53], %[vrd1] offset:12288\n"
    "ds_read_b64_tr_b16 v[54:55], %[vrd1] offset:12800\n"
    "s_waitcnt vmcnt(2)\n"
    "s_waitcnt lgkmcnt(0)\n"
    "s_barrier\n"
    "s_add_u32 m0, s60, 8192\n"
    "s_nop 0\n"
    "global_load_lds_dwordx4 %[vw], s[44:45] offset:0 nt\n"
    "s_add_u32 s44, s44, 110592\n"
    "s_addc_u32 s45, s45, 0\n"
    "s_add_u32 m0, s60, 12288\n"
    "s_nop 0\n"
    "global_load_lds_dwordx4 %[vw], s[44:45] offset:0 nt\n"
    "s_add_u32 s44, s44, 110592\n"
    "s_addc_u32 s45, s45, 0\n"
    "ds_read_b64_tr_b16 a[216:217], %[vrd0] offset:16384\n"
    "ds_read_b64_tr_b16 a[218:219], %[vrd0] offset:16896\n"
    "ds_read_b64_tr_b16 v[56:57], %[vrd1] offset:16384\n"
    "ds_read_b64_tr_b16 v[58:59], %[vrd1] offset:16896\n"
    "ds_read_b64_tr_b16 a[220:221], %[vrd0] offset:20480\n"
    "ds_read_b64_tr_b16 a[222:223], %[vrd0] offset:20992\n"
    "ds_read_b64_tr_b16 v[60:61], %[vrd1] offset:20480\n"
    "ds_read_b64_tr_b16 v[62:63], %[vrd1] offset:20992\n"
    "s_waitcnt vmcnt(2)\n"
    "s_waitcnt lgkmcnt(0)\n"
    "s_barrier\n"
    "ds_read_b64_tr_b16 a[224:225], %[vrd0] offset:0\n"
    "ds_read_b64_tr_b16 a[226:227], %[vrd0] offset:512\n"
    "ds_read_b64_tr_b16 v[64:65], %[vrd1] offset:0\n"
    "ds_read_b64_tr_b16 v[66:67], %[vrd1] offset:512\n"
    "ds_read_b64_tr_b16 a[228:229], %[vrd0] offset:4096\n"
    "ds_read_b64_tr_b16 a[230:231], %[vrd0] offset:4608\n"
    "ds_read_b64_tr_b16 v[68:69], %[vrd1] offset:4096\n"
    "ds_read_b64_tr_b16 v[70:71], %[vrd1] offset:4608\n"
    "s_waitcnt vmcnt(0)\n"
    "s_waitcnt lgkmcnt(0)\n"
    "s_barrier\n"
    "ds_read_b64_tr_b16 a[232:233], %[vrd0] offset:8192\n"
    "ds_read_b64_tr_b16 a[234:235], %[vrd0] offset:8704\n"
    "ds_read_b64_tr_b16 v[72:73], %[vrd1] offset:8192\n"
    "ds_read_b64_tr_b16 v[74:75], %[vrd1] offset:8704\n"
    "ds_read_b64_tr_b16 a[236:237], %[vrd0] offset:12288\n"
    "ds_read_b64_tr_b16 a[238:239], %[vrd0] offset:12800\n"
    "ds_read_b64_tr_b16 v[76:77], %[vrd1] offset:12288\n"
    "ds_read_b64_tr_b16 v[78:79], %[vrd1] offset:12800\n"
    "s_waitcnt lgkmcnt(0)\n"
    "v_mov_b32 v80, 0x3f803f80\n"
    "v_mov_b32 v81, 0x3f803f80\n"
    "v_mov_b32 v82, 0x3f803f80\n"
    "v_mov_b32 v83, 0x3f803f80\n"
    "s_nop 1\n"
    "v_mfma_f32_16x16x32_bf16 v[120:123], a[0:3], v[80:83], 0\n"
    "v_mfma_f32_16x16x32_bf16 v[124:127], a[80:83], v[80:83], 0\n"
    "v_mfma_f32_16x16x32_bf16 v[128:131], a[160:163], v[80:83], 0\n"
    "v_mfma_f32_16x16x32_bf16 v[132:135], a[240:243], v[80:83], 0\n"
    "v_mfma_f32_16x16x32_bf16 v[120:123], a[4:7], v[80:83], v[120:123]\n"
    "v_mfma_f32_16x16x32_bf16 v[124:127], a[84:87], v[80:83], v[124:127]\n"
    "v_mfma_f32_16x16x32_bf16 v[128:131], a[164:167], v[80:83], v[128:131]\n"
    "v_mfma_f32_16x16x32_bf16 v[132:135], a[244:247], v[80:83], v[132:135]\n"
    "v_mfma_f32_16x16x32_bf16 v[120:123], a[8:11], v[80:83], v[120:123]\n"
    "v_mfma_f32_16x16x32_bf16 v[124:127], a[88:91], v[80:83], v[124:127]\n"
    "v_mfma_f32_16x16x32_bf16 v[128:131], a[168:171], v[80:83], v[128:131]\n"
    "v_mfma_f32_16x16x32_bf16 v[132:135], a[248:251], v[80:83], v[132:135]\n"
    "v_mfma_f32_16x16x32_bf16 v[120:123], a[12:15], v[80:83], v[120:123]\n"
    "v_mfma_f32_16x16x32_bf16 v[124:127], a[92:95], v[80:83], v[124:127]\n"
    "v_mfma_f32_16x16x32_bf16 v[128:131], a[172:175], v[80:83], v[128:131]\n"
    "v_mfma_f32_16x16x32_bf16 v[132:135], a[252:255], v[80:83], v[132:135]\n"
    "v_mfma_f32_16x16x32_bf16 v[120:123], a[16:19], v[80:83], v[120:123]\n"
    "v_mfma_f32_16x16x32_bf16 v[124:127], a[96:99], v[80:83], v[124:127]\n"
    "v_mfma_f32_16x16x32_bf16 v[128:131], a[176:179], v[80:83], v[128:131]\n"
    "v_mfma_f32_16x16x32_bf16 v[132:135], v[16:19], v[80:83], v[132:135]\n"
    "v_mfma_f32_16x16x32_bf16 v[120:123], a[20:23], v[80:83], v[120:123]\n"
    "v_mfma_f32_16x16x32_bf16 v[124:127], a[100:103], v[80:83], v[124:127]\n"
    "v_mfma_f32_16x16x32_bf16 v[128:131], a[180:183], v[80:83], v[128:131]\n"
    "v_mfma_f32_16x16x32_bf16 v[132:135], v[20:23], v[80:83], v[132:135]\n"
    "v_mfma_f32_16x16x32_bf16 v[120:123], a[24:27], v[80:83], v[120:123]\n"
    "v_mfma_f32_16x16x32_bf16 v[124:127], a[104:107], v[80:83], v[124:127]\n"
    "v_mfma_f32_16x16x32_bf16 v[128:131], a[184:187], v[80:83], v[128:131]\n"
    "v_mfma_f32_16x16x32_bf16 v[132:135], v[24:27], v[80:83], v[132:135]\n"
    "v_mfma_f32_16x16x32_bf16 v[120:123], a[28:31], v[80:83], v[120:123]\n"
    "v_mfma_f32_16x16x32_bf16 v[124:127], a[108:111], v[80:83], v[124:127]\n"
    "v_mfma_f32_16x16x32_bf16 v[128:131], a[188:191], v[80:83], v[128:131]\n"
    "v_mfma_f32_16x16x32_bf16 v[132:135], v[28:31], v[80:83], v[132:135]\n"
    "v_mfma_f32_16x16x32_bf16 v[120:123], a[32:35], v[80:83], v[120:123]\n"
    "v_mfma_f32_16x16x32_bf16 v[124:127], a[112:115], v[80:83], v[124:127]\n"
    "v_mfma_f32_16x16x32_bf16 v[128:131], a[192:195], v[80:83], v[128:131]\n"
    "v_mfma_f32_16x16x32_bf16 v[132:135], v[32:35], v[80:83], v[132:135]\n"
    "v_mfma_f32_16x16x32_bf16 v[120:123], a[36:39], v[80:83], v[120:123]\n"
    "v_mfma_f32_16x16x32_bf16 v[124:127], a[116:119], v[80:83], v[124:127]\n"
    "v_mfma_f32_16x16x32_bf16 v[128:131], a[196:199], v[80:83], v[128:131]\n"
    "v_mfma_f32_16x16x32_bf16 v[132:135], v[36:39], v[80:83], v[132:135]\n"
    "v_mfma_f32_16x16x32_bf16 v[120:123], a[40:43], v[80:83], v[120:123]\n"
    "v_mfma_f32_16x16x32_bf16 v[124:127], a[120:123], v[80:83], v[124:127]\n"
    "v_mfma_f32_16x16x32_bf16 v[128:131], a[200:203], v[80:83], v[128:131]\n"
    "v_mfma_f32_16x16x32_bf16 v[132:135], v[40:43], v[80:83], v[132:135]\n"
    "v_mfma_f32_16x16x32_bf16 v[120:123], a[44:47], v[80:83], v[120:123]\n"
    "v_mfma_f32_16x16x32_bf16 v[124:127], a[124:127], v[80:83], v[124:127]\n"
    "v_mfma_f32_16x16x32_bf16 v[128:131], a[204:207], v[80:83], v[128:131]\n"
    "v_mfma_f32_16x16x32_bf16 v[132:135], v[44:47], v[80:83], v[132:135]\n"
    "v_mfma_f32_16x16x32_bf16 v[120:123], a[48:51], v[80:83], v[120:123]\n"
    "v_mfma_f32_16x16x32_bf16 v[124:127], a[128:131], v[80:83], v[124:127]\n"
    "v_mfma_f32_16x16x32_bf16 v[128:131], a[208:211], v[80:83], v[128:131]\n"
    "v_mfma_f32_16x16x32_bf16 v[132:135], v[48:51], v[80:83], v[132:135]\n"
    "v_mfma_f32_16x16x32_bf16 v[120:123], a[52:55], v[80:83], v[120:123]\n"
    "v_mfma_f32_16x16x32_bf16 v[124:127], a[132:135], v[80:83], v[124:127]\n"
    "v_mfma_f32_16x16x32_bf16 v[128:131], a[212:215], v[80:83], v[128:131]\n"
    "v_mfma_f32_16x16x32_bf16 v[132:135], v[52:55], v[80:83], v[132:135]\n"
    "v_mfma_f32_16x16x32_bf16 v[120:123], a[56:59], v[80:83], v[120:123]\n"
    "v_mfma_f32_16x16x32_bf16 v[124:127], a[136:139], v[80:83], v[124:127]\n"
    "v_mfma_f32_16x16x32_bf16 v[128:131], a[216:219], v[80:83], v[128:131]\n"
    "v_mfma_f32_16x16x32_bf16 v[132:135], v[56:59], v[80:83], v[132:135]\n"
    "v_mfma_f32_16x16x32_bf16 v[120:123], a[60:63], v[80:83], v[120:123]\n"
    "v_mfma_f32_16x16x32_bf16 v[124:127], a[140:143], v[80:83], v[124:127]\n"
    "v_mfma_f32_16x16x32_bf16 v[128:131], a[220:223], v[80:83], v[128:131]\n"
    "v_mfma_f32_16x16x32_bf16 v[132:135], v[60:63], v[80:83], v[132:135]\n"
    "v_mfma_f32_16x16x32_bf16 v[120:123], a[64:67], v[80:83], v[120:123]\n"
    "v_mfma_f32_16x16x32_bf16 v[124:127], a[144:147], v[80:83], v[124:127]\n"
    "v_mfma_f32_16x16x32_bf16 v[128:131], a[224:227], v[80:83], v[128:131]\n"
    "v_mfma_f32_16x16x32_bf16 v[132:135], v[64:67], v[80:83], v[132:135]\n"
    "v_mfma_f32_16x16x32_bf16 v[120:123], a[68:71], v[80:83], v[120:123]\n"
    "v_mfma_f32_16x16x32_bf16 v[124:127], a[148:151], v[80:83], v[124:127]\n"
    "v_mfma_f32_16x16x32_bf16 v[128:131], a[228:231], v[80:83], v[128:131]\n"
    "v_mfma_f32_16x16x32_bf16 v[132:135], v[68:71], v[80:83], v[132:135]\n"
    "v_mfma_f32_16x16x32_bf16 v[120:123], a[72:75], v[80:83], v[120:123]\n"
    "v_mfma_f32_16x16x32_bf16 v[124:127], a[152:155], v[80:83], v[124:127]\n"
    "v_mfma_f32_16x16x32_bf16 v[128:131], a[232:235], v[80:83], v[128:131]\n"
    "v_mfma_f32_16x16x32_bf16 v[132:135], v[72:75], v[80:83], v[132:135]\n"
    "v_mfma_f32_16x16x32_bf16 v[120:123], a[76:79], v[80:83], v[120:123]\n"
    "v_mfma_f32_16x16x32_bf16 v[124:127], a[156:159], v[80:83], v[124:127]\n"
    "v_mfma_f32_16x16x32_bf16 v[128:131], a[236:239], v[80:83], v[128:131]\n"
    "v_mfma_f32_16x16x32_bf16 v[132:135], v[76:79], v[80:83], v[132:135]\n"
    "s_barrier\n"
    "s_mov_b64 s[46:47], %[hg]\n"
    "s_mov_b64 s[48:49], %[sg]\n"
    "s_mov_b64 s[50:51], %[ag]\n"
    "s_mov_b64 s[52:53], %[ws]\n"
    "s_add_u32 s52, s52, 2121728\n"
    "s_addc_u32 s53, s53, 0\n"
    "s_mul_i32 s57, %[w], 10240\n"
    "s_add_u32 s57, s57, %[lb]\n"
    "s_mov_b32 s40, %[z]\n"
    "s_mov_b32 s43, 0\n"
    "v_mov_b32 v146, 0\n"
    "v_mov_b32 v149, 0\n"
    "s_mov_b32 s58, 0\n"
    "s_mov_b32 s59, 0\n"
    "s_mov_b32 s61, 0\n"
    "v_lshrrev_b32 v147, 2, %[vg16]\n"
    "v_add_u32 v147, %[lb], v147\n"
    "v_add_u32 v147, 81920, v147\n"
    "Floop_%=:\n"
    "s_add_u32 s56, s40, 1\n"
    "s_cmp_eq_u32 s58, 1\n"
    "s_cbranch_scc1 Fgo_%=\n"
    "s_waitcnt vmcnt(0)\n"
    "s_barrier\n"
    "s_cmp_lg_u32 %[w], 3\n"
    "s_cbranch_scc1 Fp1_%=\n"
    "s_cmp_eq_u32 s61, 0\n"
    "s_cbranch_scc1 Fp1_%=\n"
    "v_mov_b32 v148, s61\n"
    "s_mov_b64 exec, 1\n"
    "global_store_dword v149, v148, %[fo] offset:0 sc1\n"
    "s_mov_b64 exec, -1\n"
    "Fp1_%=:\n"
    "s_mov_b32 s61, 0\n"
    "s_cmp_lg_u32 %[w], 0\n"
    "s_cbranch_scc1 Fr_%=\n"
    "Fw_%=:\n"
    "s_add_u32 s43, s43, 1\n"
    "s_cmp_gt_u32 s43, 524288\n"
    "s_cbranch_scc1 Fdead_%=\n"
    "global_load_dword v146, %[vflv], s[52:53] sc1\n"
    "s_waitcnt vmcnt(0)\n"
    "v_cmp_gt_u32 vcc, s56, v146\n"
    "s_cbranch_vccz Fr0_%=\n"
    "s_sleep 2\n"
    "s_branch Fw_%=\n"
    "Fr0_%=:\n"
    "ds_write_b32 v147, v146 offset:0\n"
    "Fr_%=:\n"
    "s_waitcnt lgkmcnt(0)\n"
    "s_barrier\n"
    "global_load_dwordx2 v[192:193], %[vsv], s[48:49] offset:0 sc1\n"
    "global_load_dwordx2 v[194:195], %[vsv], s[48:49] offset:128 sc1\n"
    "global_load_dwordx2 v[196:197], %[vsv], s[48:49] offset:256 sc1\n"
    "global_load_dwordx2 v[198:199], %[vsv], s[48:49] offset:384 sc1\n"
    "global_load_dwordx2 v[200:201], %[vsv], s[48:49] offset:512 sc1\n"
    "s_mov_b64 s[54:55], s[46:47]\n"
    "s_add_u32 s41, s57, s59\n"
    "s_add_u32 m0, s41, 0\n"
    "s_nop 0\n"
    "global_load_lds_dwordx4 %[vxv], s[54:55] offset:0 sc1\n"
    "global_load_lds_dwordx4 %[vxv], s[54:55] offset:1024 sc1\n"
    "global_load_lds_dwordx4 %[vxv], s[54:55] offset:2048 sc1\n"
    "global_load_lds_dwordx4 %[vxv], s[54:55] offset:3072 sc1\n"
    "s_add_u32 s54, s54, 4096\n"
    "s_addc_u32 s55, s55, 0\n"
    "s_add_u32 m0, s41, 4096\n"
    "s_nop 0\n"
    "global_load_lds_dwordx4 %[vxv], s[54:55] offset:0 sc1\n"
    "global_load_lds_dwordx4 %[vxv], s[54:55] offset:1024 sc1\n"
    "global_load_lds_dwordx4 %[vxv], s[54:55] offset:2048 sc1\n"
    "global_load_lds_dwordx4 %[vxv], s[54:55] offset:3072 sc1\n"
    "s_add_u32 s54, s54, 4096\n"
    "s_addc_u32 s55, s55, 0\n"
    "s_add_u32 m0, s41, 8192\n"
    "s_nop 0\n"
    "global_load_lds_dwordx4 %[vxv], s[54:55] offset:0 sc1\n"
    "global_load_lds_dwordx4 %[vxv], s[54:55] offset:1024 sc1\n"
    "s_add_u32 s46, s46, 81920\n"
    "s_addc_u32 s47, s47, 0\n"
    "s_add_u32 s48, s48, 10240\n"
    "s_addc_u32 s49, s49, 0\n"
    "ds_read_b32 v146, v147 offset:0\n"
    "s_waitcnt lgkmcnt(0)\n"
    "s_waitcnt vmcnt(10)\n"
    "s_branch Fgo2_%=\n"
    "Fgo_%=:\n"
    "s_waitcnt vmcnt(1)\n"
    "Fgo2_%=:\n"
    "v_mov_b64 v[136:137], v[192:193]\n"
    "v_mov_b64 v[138:139], v[194:195]\n"
    "v_mov_b64 v[140:141], v[196:197]\n"
    "v_mov_b64 v[142:143], v[198:199]\n"
    "v_mov_b64 v[144:145], v[200:201]\n"
    "v_add_u32 v151, s59, %[vlr]\n"
    "s_xor_b32 s59, s59, 40960\n"
    "v_add_f32 v152, v136, v138\n"
    "v_add_f32 v157, v140, v142\n"
    "v_add_f32 v152, v152, v144\n"
    "v_add_f32 v152, v152, v157\n"
    "v_mov_b32 v156, v152\n"
    "s_nop 1\n"
    "v_permlane16_swap_b32 v152, v156\n"
    "v_add_f32 v152, v152, v156\n"
    "v_mov_b32 v156, v152\n"
    "s_nop 1\n"
    "v_permlane32_swap_b32 v152, v156\n"
    "v_add_f32 v152, v152, v156\n"
    "v_mul_f32 v154, 0x3acccccd, v152\n"
    "v_mov_b32 v153, 0\n"
    "v_fma_f32 v157, v136, %[c32], -v154\n"
    "v_mul_f32 v157, v157, v157\n"
    "v_fmamk_f32 v157, v157, 0x42000000, v137\n"
    "v_add_f32 v153, v153, v157\n"
    "v_fma_f32 v157, v138, %[c32], -v154\n"
    "v_mul_f32 v157, v157, v157\n"
    "v_fmamk_f32 v157, v157, 0x42000000, v139\n"
    "v_add_f32 v153, v153, v157\n"
    "v_fma_f32 v157, v140, %[c32], -v154\n"
    "v_mul_f32 v157, v157, v157\n"
    "v_fmamk_f32 v157, v157, 0x42000000, v141\n"
    "v_add_f32 v153, v153, v157\n"
    "v_fma_f32 v157, v142, %[c32], -v154\n"
    "v_mul_f32 v157, v157, v157\n"
    "v_fmamk_f32 v157, v157, 0x42000000, v143\n"
    "v_add_f32 v153, v153, v157\n"
    "v_fma_f32 v157, v144, %[c32], -v154\n"
    "v_mul_f32 v157, v157, v157\n"
    "v_fmamk_f32 v157, v157, 0x42000000, v145\n"
    "v_add_f32 v153, v153, v157\n"
    "v_mov_b32 v156, v153\n"
    "s_nop 1\n"
    "v_permlane16_swap_b32 v153, v156\n"
    "v_add_f32 v153, v153, v156\n"
    "v_mov_b32 v156, v153\n"
    "s_nop 1\n"
    "v_permlane32_swap_b32 v153, v156\n"
    "v_add_f32 v153, v153, v156\n"
    "v_mul_f32 v155, 0x3acccccd, v153\n"
    "v_cvt_pk_bf16_f32 v155, v155, v155\n"
    "v_and_b32 v155, 0xffff0000, v155\n"
    "v_add_f32 v155, 0x3727c5ac, v155\n"
    "v_cvt_pk_bf16_f32 v155, v155, v155\n"
    "v_and_b32 v155, 0xffff0000, v155\n"
    "v_sqrt_f32 v155, v155\n"
    "s_nop 0\n"
    "v_cvt_pk_bf16_f32 v155, v155, v155\n"
    "v_and_b32 v155, 0xffff0000, v155\n"
    "v_rcp_f32 v155, v155\n"
    "v_cvt_pk_bf16_f32 v154, v154, v154\n"
    "v_and_b32 v154, 0xffff0000, v154\n"
    "s_cmp_eq_u32 s58, 1\n"
    "s_cbranch_scc1 Fb_%=\n"
    "s_waitcnt vmcnt(0)\n"
    "Fb_%=:\n"
    "s_barrier\n"
    "ds_read_b128 v[80:83], v151 offset:0\n"
    "ds_read_b128 v[84:87], v151 offset:1024\n"
    "ds_read_b128 v[88:91], v151 offset:2048\n"
    "ds_read_b128 v[92:95], v151 offset:3072\n"
    "s_add_u32 s42, s40, 3\n"
    "v_cmp_gt_u32 vcc, s42, v146\n"
    "s_cbranch_vccnz Fm0_%=\n"
    "s_mov_b32 s58, 1\n"
    "s_waitcnt lgkmcnt(3)\n"
    "v_mfma_f32_16x16x32_bf16 v[104:107], a[0:3], v[80:83], 0\n"
    "ds_read_b128 v[96:99], v151 offset:4096\n"
    "v_mfma_f32_16x16x32_bf16 v[108:111], a[80:83], v[80:83], 0\n"
    "global_load_dwordx2 v[192:193], %[vsv], s[48:49] offset:0 sc1\n"
    "v_mfma_f32_16x16x32_bf16 v[112:115], a[160:163], v[80:83], 0\n"
    "global_load_dwordx2 v[194:195], %[vsv], s[48:49] offset:128 sc1\n"
    "v_mfma_f32_16x16x32_bf16 v[116:119], a[240:243], v[80:83], 0\n"
    "s_waitcnt lgkmcnt(3)\n"
    "v_mfma_f32_16x16x32_bf16 v[104:107], a[4:7], v[84:87], v[104:107]\n"
    "ds_read_b128 v[100:103], v151 offset:5120\n"
    "v_mfma_f32_16x16x32_bf16 v[108:111], a[84:87], v[84:87], v[108:111]\n"
    "global_load_dwordx2 v[196:197], %[vsv], s[48:49] offset:256 sc1\n"
    "v_mfma_f32_16x16x32_bf16 v[112:115], a[164:167], v[84:87], v[112:115]\n"
    "global_load_dwordx2 v[198:199], %[vsv], s[48:49] offset:384 sc1\n"
    "v_mfma_f32_16x16x32_bf16 v[116:119], a[244:247], v[84:87], v[116:119]\n"
    "s_waitcnt lgkmcnt(3)\n"
    "v_mfma_f32_16x16x32_bf16 v[104:107], a[8:11], v[88:91], v[104:107]\n"
    "ds_read_b128 v[80:83], v151 offset:6144\n"
    "v_mfma_f32_16x16x32_bf16 v[108:111], a[88:91], v[88:91], v[108:111]\n"
    "global_load_dwordx2 v[200:201], %[vsv], s[48:49] offset:512 sc1\n"
    "v_mfma_f32_16x16x32_bf16 v[112:115], a[168:171], v[88:91], v[112:115]\n"
    "s_mov_b64 s[54:55], s[46:47]\n"
    "s_add_u32 s41, s57, s59\n"
    "v_mfma_f32_16x16x32_bf16 v[116:119], a[248:251], v[88:91], v[116:119]\n"
    "s_waitcnt lgkmcnt(3)\n"
    "v_mfma_f32_16x16x32_bf16 v[104:107], a[12:15], v[92:95], v[104:107]\n"
    "ds_read_b128 v[84:87], v151 offset:7168\n"
    "v_mfma_f32_16x16x32_bf16 v[108:111], a[92:95], v[92:95], v[108:111]\n"
    "s_add_u32 m0, s41, 0\n"
    "v_mfma_f32_16x16x32_bf16 v[112:115], a[172:175], v[92:95], v[112:115]\n"
    "global_load_lds_dwordx4 %[vxv], s[54:55] offset:0 sc1\n"
    "v_mfma_f32_16x16x32_bf16 v[116:119], a[252:255], v[92:95], v[116:119]\n"
    "s_waitcnt lgkmcnt(3)\n"
    "v_mfma_f32_16x16x32_bf16 v[104:107], a[16:19], v[96:99], v[104:107]\n"
    "ds_read_b128 v[88:91], v151 offset:8192\n"
    "v_mfma_f32_16x16x32_bf16 v[108:111], a[96:99], v[96:99], v[108:111]\n"
    "global_load_lds_dwordx4 %[vxv], s[54:55] offset:1024 sc1\n"
    "v_mfma_f32_16x16x32_bf16 v[112:115], a[176:179], v[96:99], v[112:115]\n"
    "global_load_lds_dwordx4 %[vxv], s[54:55] offset:2048 sc1\n"
    "v_mfma_f32_16x16x32_bf16 v[116:119], v[16:19], v[96:99], v[116:119]\n"
    "s_waitcnt lgkmcnt(3)\n"
    "v_mfma_f32_16x16x32_bf16 v[104:107], a[20:23], v[100:103], v[104:107]\n"
    "ds_read_b128 v[92:95], v151 offset:9216\n"
    "v_mfma_f32_16x16x32_bf16 v[108:111], a[100:103], v[100:103], v[108:111]\n"
    "global_load_lds_dwordx4 %[vxv], s[54:55] offset:3072 sc1\n"
    "v_mfma_f32_16x16x32_bf16 v[112:115], a[180:183], v[100:103], v[112:115]\n"
    "s_add_u32 s54, s54, 4096\n"
    "s_addc_u32 s55, s55, 0\n"
    "v_mfma_f32_16x16x32_bf16 v[116:119], v[20:23], v[100:103], v[116:119]\n"
    "s_waitcnt lgkmcnt(3)\n"
    "v_mfma_f32_16x16x32_bf16 v[104:107], a[24:27], v[80:83], v[104:107]\n"
    "ds_read_b128 v[96:99], v151 offset:10240\n"
    "v_mfma_f32_16x16x32_bf16 v[108:111], a[104:107], v[80:83], v[108:111]\n"
    "s_add_u32 m0, s41, 4096\n"
    "v_mfma_f32_16x16x32_bf16 v[112:115], a[184:187], v[80:83], v[112:115]\n"
    "global_load_lds_dwordx4 %[vxv], s[54:55] offset:0 sc1\n"
    "v_mfma_f32_16x16x32_bf16 v[116:119], v[24:27], v[80:83], v[116:119]\n"
    "s_waitcnt lgkmcnt(3)\n"
    "v_mfma_f32_16x16x32_bf16 v[104:107], a[28:31], v[84:87], v[104:107]\n"
    "ds_read_b128 v[100:103], v151 offset:11264\n"
    "v_mfma_f32_16x16x32_bf16 v[108:111], a[108:111], v[84:87], v[108:111]\n"
    "global_load_lds_dwordx4 %[vxv], s[54:55] offset:1024 sc1\n"
    "v_mfma_f32_16x16x32_bf16 v[112:115], a[188:191], v[84:87], v[112:115]\n"
    "global_load_lds_dwordx4 %[vxv], s[54:55] offset:2048 sc1\n"
    "v_mfma_f32_16x16x32_bf16 v[116:119], v[28:31], v[84:87], v[116:119]\n"
    "s_waitcnt lgkmcnt(3)\n"
    "v_mfma_f32_16x16x32_bf16 v[104:107], a[32:35], v[88:91], v[104:107]\n"
    "ds_read_b128 v[80:83], v151 offset:12288\n"
    "v_mfma_f32_16x16x32_bf16 v[108:111], a[112:115], v[88:91], v[108:111]\n"
    "global_load_lds_dwordx4 %[vxv], s[54:55] offset:3072 sc1\n"
    "v_mfma_f32_16x16x32_bf16 v[112:115], a[192:195], v[88:91], v[112:115]\n"
    "s_add_u32 s54, s54, 4096\n"
    "s_addc_u32 s55, s55, 0\n"
    "v_mfma_f32_16x16x32_bf16 v[116:119], v[32:35], v[88:91], v[116:119]\n"
    "s_waitcnt lgkmcnt(3)\n"
    "v_mfma_f32_16x16x32_bf16 v[104:107], a[36:39], v[92:95], v[104:107]\n"
    "ds_read_b128 v[84:87], v151 offset:13312\n"
    "v_mfma_f32_16x16x32_bf16 v[108:111], a[116:119], v[92:95], v[108:111]\n"
    "s_add_u32 m0, s41, 8192\n"
    "v_mfma_f32_16x16x32_bf16 v[112:115], a[196:199], v[92:95], v[112:115]\n"
    "global_load_lds_dwordx4 %[vxv], s[54:55] offset:0 sc1\n"
    "v_mfma_f32_16x16x32_bf16 v[116:119], v[36:39], v[92:95], v[116:119]\n"
    "s_waitcnt lgkmcnt(3)\n"
    "v_mfma_f32_16x16x32_bf16 v[104:107], a[40:43], v[96:99], v[104:107]\n"
    "ds_read_b128 v[88:91], v151 offset:14336\n"
    "v_mfma_f32_16x16x32_bf16 v[108:111], a[120:123], v[96:99], v[108:111]\n"
    "global_load_lds_dwordx4 %[vxv], s[54:55] offset:1024 sc1\n"
    "v_mfma_f32_16x16x32_bf16 v[112:115], a[200:203], v[96:99], v[112:115]\n"
    "s_add_u32 s46, s46, 81920\n"
    "s_addc_u32 s47, s47, 0\n"
    "s_add_u32 s48, s48, 10240\n"
    "s_addc_u32 s49, s49, 0\n"
    "v_mfma_f32_16x16x32_bf16 v[116:119], v[40:43], v[96:99], v[116:119]\n"
    "s_waitcnt lgkmcnt(3)\n"
    "v_mfma_f32_16x16x32_bf16 v[104:107], a[44:47], v[100:103], v[104:107]\n"
    "ds_read_b128 v[92:95], v151 offset:15360\n"
    "v_mfma_f32_16x16x32_bf16 v[108:111], a[124:127], v[100:103], v[108:111]\n"
    "v_mfma_f32_16x16x32_bf16 v[112:115], a[204:207], v[100:103], v[112:115]\n"
    "v_mfma_f32_16x16x32_bf16 v[116:119], v[44:47], v[100:103], v[116:119]\n"
    "s_waitcnt lgkmcnt(3)\n"
    "v_mfma_f32_16x16x32_bf16 v[104:107], a[48:51], v[80:83], v[104:107]\n"
    "ds_read_b128 v[96:99], v151 offset:16384\n"
    "v_mfma_f32_16x16x32_bf16 v[108:111], a[128:131], v[80:83], v[108:111]\n"
    "v_mfma_f32_16x16x32_bf16 v[112:115], a[208:211], v[80:83], v[112:115]\n"
    "v_mfma_f32_16x16x32_bf16 v[116:119], v[48:51], v[80:83], v[116:119]\n"
    "s_waitcnt lgkmcnt(3)\n"
    "v_mfma_f32_16x16x32_bf16 v[104:107], a[52:55], v[84:87], v[104:107]\n"
    "ds_read_b128 v[100:103], v151 offset:17408\n"
    "v_mfma_f32_16x16x32_bf16 v[108:111], a[132:135], v[84:87], v[108:111]\n"
    "s_waitcnt vmcnt(15)\n"
    "s_barrier\n"
    "s_cmp_lg_u32 %[w], 3\n"
    "s_cbranch_scc1 Fp2_%=\n"
    "s_cmp_eq_u32 s61, 0\n"
    "s_cbranch_scc1 Fp2_%=\n"
    "v_mov_b32 v148, s61\n"
    "s_mov_b64 exec, 1\n"
    "global_store_dword v149, v148, %[fo] offset:0 sc1\n"
    "s_mov_b64 exec, -1\n"
    "Fp2_%=:\n"
    "s_mov_b32 s61, 0\n"
    "v_mfma_f32_16x16x32_bf16 v[112:115], a[212:215], v[84:87], v[112:115]\n"
    "v_mfma_f32_16x16x32_bf16 v[116:119], v[52:55], v[84:87], v[116:119]\n"
    "s_waitcnt lgkmcnt(3)\n"
    "v_mfma_f32_16x16x32_bf16 v[104:107], a[56:59], v[88:91], v[104:107]\n"
    "ds_read_b128 v[80:83], v151 offset:18432\n"
    "v_mfma_f32_16x16x32_bf16 v[108:111], a[136:139], v[88:91], v[108:111]\n"
    "v_mfma_f32_16x16x32_bf16 v[112:115], a[216:219], v[88:91], v[112:115]\n"
    "v_mfma_f32_16x16x32_bf16 v[116:119], v[56:59], v[88:91], v[116:119]\n"
    "s_waitcnt lgkmcnt(3)\n"
    "v_mfma_f32_16x16x32_bf16 v[104:107], a[60:63], v[92:95], v[104:107]\n"
    "ds_read_b128 v[84:87], v151 offset:19456\n"
    "v_mfma_f32_16x16x32_bf16 v[108:111], a[140:143], v[92:95], v[108:111]\n"
    "v_mfma_f32_16x16x32_bf16 v[112:115], a[220:223], v[92:95], v[112:115]\n"
    "v_mfma_f32_16x16x32_bf16 v[116:119], v[60:63], v[92:95], v[116:119]\n"
    "s_waitcnt lgkmcnt(3)\n"
    "v_mfma_f32_16x16x32_bf16 v[104:107], a[64:67], v[96:99], v[104:107]\n"
    "v_mfma_f32_16x16x32_bf16 v[108:111], a[144:147], v[96:99], v[108:111]\n"
    "v_mfma_f32_16x16x32_bf16 v[112:115], a[224:227], v[96:99], v[112:115]\n"
    "v_mfma_f32_16x16x32_bf16 v[116:119], v[64:67], v[96:99], v[116:119]\n"
    "s_waitcnt lgkmcnt(2)\n"
    "v_mfma_f32_16x16x32_bf16 v[104:107], a[68:71], v[100:103], v[104:107]\n"
    "v_mfma_f32_16x16x32_bf16 v[108:111], a[148:151], v[100:103], v[108:111]\n"
    "v_mfma_f32_16x16x32_bf16 v[112:115], a[228:231], v[100:103], v[112:115]\n"
    "v_mfma_f32_16x16x32_bf16 v[116:119], v[68:71], v[100:103], v[116:119]\n"
    "s_waitcnt lgkmcnt(1)\n"
    "v_mfma_f32_16x16x32_bf16 v[104:107], a[72:75], v[80:83], v[104:107]\n"
    "v_mfma_f32_16x16x32_bf16 v[108:111], a[152:155], v[80:83], v[108:111]\n"
    "v_mfma_f32_16x16x32_bf16 v[112:115], a[232:235], v[80:83], v[112:115]\n"
    "v_mfma_f32_16x16x32_bf16 v[116:119], v[72:75], v[80:83], v[116:119]\n"
    "s_waitcnt lgkmcnt(0)\n"
    "v_mfma_f32_16x16x32_bf16 v[104:107], a[76:79], v[84:87], v[104:107]\n"
    "v_mfma_f32_16x16x32_bf16 v[108:111], a[156:159], v[84:87], v[108:111]\n"
    "v_mfma_f32_16x16x32_bf16 v[112:115], a[236:239], v[84:87], v[112:115]\n"
    "v_mfma_f32_16x16x32_bf16 v[116:119], v[76:79], v[84:87], v[116:119]\n"
    "s_branch Fm1_%=\n"
    "Fm0_%=:\n"
    "s_mov_b32 s58, 0\n"
    "s_waitcnt lgkmcnt(3)\n"
    "v_mfma_f32_16x16x32_bf16 v[104:107], a[0:3], v[80:83], 0\n"
    "ds_read_b128 v[96:99], v151 offset:4096\n"
    "v_mfma_f32_16x16x32_bf16 v[108:111], a[80:83], v[80:83], 0\n"
    "v_mfma_f32_16x16x32_bf16 v[112:115], a[160:163], v[80:83], 0\n"
    "v_mfma_f32_16x16x32_bf16 v[116:119], a[240:243], v[80:83], 0\n"
    "s_waitcnt lgkmcnt(3)\n"
    "v_mfma_f32_16x16x32_bf16 v[104:107], a[4:7], v[84:87], v[104:107]\n"
    "ds_read_b128 v[100:103], v151 offset:5120\n"
    "v_mfma_f32_16x16x32_bf16 v[108:111], a[84:87], v[84:87], v[108:111]\n"
    "v_mfma_f32_16x16x32_bf16 v[112:115], a[164:167], v[84:87], v[112:115]\n"
    "v_mfma_f32_16x16x32_bf16 v[116:119], a[244:247], v[84:87], v[116:119]\n"
    "s_waitcnt lgkmcnt(3)\n"
    "v_mfma_f32_16x16x32_bf16 v[104:107], a[8:11], v[88:91], v[104:107]\n"
    "ds_read_b128 v[80:83], v151 offset:6144\n"
    "v_mfma_f32_16x16x32_bf16 v[108:111], a[88:91], v[88:91], v[108:111]\n"
    "v_mfma_f32_16x16x32_bf16 v[112:115], a[168:171], v[88:91], v[112:115]\n"
    "v_mfma_f32_16x16x32_bf16 v[116:119], a[248:251], v[88:91], v[116:119]\n"
    "s_waitcnt lgkmcnt(3)\n"
    "v_mfma_f32_16x16x32_bf16 v[104:107], a[12:15], v[92:95], v[104:107]\n"
    "ds_read_b128 v[84:87], v151 offset:7168\n"
    "v_mfma_f32_16x16x32_bf16 v[108:111], a[92:95], v[92:95], v[108:111]\n"
    "v_mfma_f32_16x16x32_bf16 v[112:115], a[172:175], v[92:95], v[112:115]\n"
    "v_mfma_f32_16x16x32_bf16 v[116:119], a[252:255], v[92:95], v[116:119]\n"
    "s_waitcnt lgkmcnt(3)\n"
    "v_mfma_f32_16x16x32_bf16 v[104:107], a[16:19], v[96:99], v[104:107]\n"
    "ds_read_b128 v[88:91], v151 offset:8192\n"
    "v_mfma_f32_16x16x32_bf16 v[108:111], a[96:99], v[96:99], v[108:111]\n"
    "v_mfma_f32_16x16x32_bf16 v[112:115], a[176:179], v[96:99], v[112:115]\n"
    "v_mfma_f32_16x16x32_bf16 v[116:119], v[16:19], v[96:99], v[116:119]\n"
    "s_waitcnt lgkmcnt(3)\n"
    "v_mfma_f32_16x16x32_bf16 v[104:107], a[20:23], v[100:103], v[104:107]\n"
    "ds_read_b128 v[92:95], v151 offset:9216\n"
    "v_mfma_f32_16x16x32_bf16 v[108:111], a[100:103], v[100:103], v[108:111]\n"
    "v_mfma_f32_16x16x32_bf16 v[112:115], a[180:183], v[100:103], v[112:115]\n"
    "v_mfma_f32_16x16x32_bf16 v[116:119], v[20:23], v[100:103], v[116:119]\n"
    "s_waitcnt lgkmcnt(3)\n"
    "v_mfma_f32_16x16x32_bf16 v[104:107], a[24:27], v[80:83], v[104:107]\n"
    "ds_read_b128 v[96:99], v151 offset:10240\n"
    "v_mfma_f32_16x16x32_bf16 v[108:111], a[104:107], v[80:83], v[108:111]\n"
    "v_mfma_f32_16x16x32_bf16 v[112:115], a[184:187], v[80:83], v[112:115]\n"
    "v_mfma_f32_16x16x32_bf16 v[116:119], v[24:27], v[80:83], v[116:119]\n"
    "s_waitcnt lgkmcnt(3)\n"
    "v_mfma_f32_16x16x32_bf16 v[104:107], a[28:31], v[84:87], v[104:107]\n"
    "ds_read_b128 v[100:103], v151 offset:11264\n"
    "v_mfma_f32_16x16x32_bf16 v[108:111], a[108:111], v[84:87], v[108:111]\n"
    "v_mfma_f32_16x16x32_bf16 v[112:115], a[188:191], v[84:87], v[112:115]\n"
    "v_mfma_f32_16x16x32_bf16 v[116:119], v[28:31], v[84:87], v[116:119]\n"
    "s_waitcnt lgkmcnt(3)\n"
    "v_mfma_f32_16x16x32_bf16 v[104:107], a[32:35], v[88:91], v[104:107]\n"
    "ds_read_b128 v[80:83], v151 offset:12288\n"
    "v_mfma_f32_16x16x32_bf16 v[108:111], a[112:115], v[88:91], v[108:111]\n"
    "v_mfma_f32_16x16x32_bf16 v[112:115], a[192:195], v[88:91], v[112:115]\n"
    "v_mfma_f32_16x16x32_bf16 v[116:119], v[32:35], v[88:91], v[116:119]\n"
    "s_waitcnt lgkmcnt(3)\n"
    "v_mfma_f32_16x16x32_bf16 v[104:107], a[36:39], v[92:95], v[104:107]\n"
    "ds_read_b128 v[84:87], v151 offset:13312\n"
    "v_mfma_f32_16x16x32_bf16 v[108:111], a[116:119], v[92:95], v[108:111]\n"
    "v_mfma_f32_16x16x32_bf16 v[112:115], a[196:199], v[92:95], v[112:115]\n"
    "v_mfma_f32_16x16x32_bf16 v[116:119], v[36:39], v[92:95], v[116:119]\n"
    "s_waitcnt lgkmcnt(3)\n"
    "v_mfma_f32_16x16x32_bf16 v[104:107], a[40:43], v[96:99], v[104:107]\n"
    "ds_read_b128 v[88:91], v151 offset:14336\n"
    "v_mfma_f32_16x16x32_bf16 v[108:111], a[120:123], v[96:99], v[108:111]\n"
    "v_mfma_f32_16x16x32_bf16 v[112:115], a[200:203], v[96:99], v[112:115]\n"
    "v_mfma_f32_16x16x32_bf16 v[116:119], v[40:43], v[96:99], v[116:119]\n"
    "s_waitcnt lgkmcnt(3)\n"
    "v_mfma_f32_16x16x32_bf16 v[104:107], a[44:47], v[100:103], v[104:107]\n"
    "ds_read_b128 v[92:95], v151 offset:15360\n"
    "v_mfma_f32_16x16x32_bf16 v[108:111], a[124:127], v[100:103], v[108:111]\n"
    "v_mfma_f32_16x16x32_bf16 v[112:115], a[204:207], v[100:103], v[112:115]\n"
    "v_mfma_f32_16x16x32_bf16 v[116:119], v[44:47], v[100:103], v[116:119]\n"
    "s_waitcnt lgkmcnt(3)\n"
    "v_mfma_f32_16x16x32_bf16 v[104:107], a[48:51], v[80:83], v[104:107]\n"
    "ds_read_b128 v[96:99], v151 offset:16384\n"
    "v_mfma_f32_16x16x32_bf16 v[108:111], a[128:131], v[80:83], v[108:111]\n"
    "v_mfma_f32_16x16x32_bf16 v[112:115], a[208:211], v[80:83], v[112:115]\n"
    "v_mfma_f32_16x16x32_bf16 v[116:119], v[48:51], v[80:83], v[116:119]\n"
    "s_waitcnt lgkmcnt(3)\n"
    "v_mfma_f32_16x16x32_bf16 v[104:107], a[52:55], v[84:87], v[104:107]\n"
    "ds_read_b128 v[100:103], v151 offset:17408\n"
    "v_mfma_f32_16x16x32_bf16 v[108:111], a[132:135], v[84:87], v[108:111]\n"
    "s_waitcnt vmcnt(0)\n"
    "s_barrier\n"
    "s_cmp_lg_u32 %[w], 3\n"
    "s_cbranch_scc1 Fp4_%=\n"
    "s_cmp_eq_u32 s61, 0\n"
    "s_cbranch_scc1 Fp4_%=\n"
    "v_mov_b32 v148, s61\n"
    "s_mov_b64 exec, 1\n"
    "global_store_dword v149, v148, %[fo] offset:0 sc1\n"
    "s_mov_b64 exec, -1\n"
    "Fp4_%=:\n"
    "s_mov_b32 s61, 0\n"
    "v_mfma_f32_16x16x32_bf16 v[112:115], a[212:215], v[84:87], v[112:115]\n"
    "v_mfma_f32_16x16x32_bf16 v[116:119], v[52:55], v[84:87], v[116:119]\n"
    "s_waitcnt lgkmcnt(3)\n"
    "v_mfma_f32_16x16x32_bf16 v[104:107], a[56:59], v[88:91], v[104:107]\n"
    "ds_read_b128 v[80:83], v151 offset:18432\n"
    "v_mfma_f32_16x16x32_bf16 v[108:111], a[136:139], v[88:91], v[108:111]\n"
    "v_mfma_f32_16x16x32_bf16 v[112:115], a[216:219], v[88:91], v[112:115]\n"
    "v_mfma_f32_16x16x32_bf16 v[116:119], v[56:59], v[88:91], v[116:119]\n"
    "s_waitcnt lgkmcnt(3)\n"
    "v_mfma_f32_16x16x32_bf16 v[104:107], a[60:63], v[92:95], v[104:107]\n"
    "ds_read_b128 v[84:87], v151 offset:19456\n"
    "v_mfma_f32_16x16x32_bf16 v[108:111], a[140:143], v[92:95], v[108:111]\n"
    "v_mfma_f32_16x16x32_bf16 v[112:115], a[220:223], v[92:95], v[112:115]\n"
    "v_mfma_f32_16x16x32_bf16 v[116:119], v[60:63], v[92:95], v[116:119]\n"
    "s_waitcnt lgkmcnt(3)\n"
    "v_mfma_f32_16x16x32_bf16 v[104:107], a[64:67], v[96:99], v[104:107]\n"
    "v_mfma_f32_16x16x32_bf16 v[108:111], a[144:147], v[96:99], v[108:111]\n"
    "v_mfma_f32_16x16x32_bf16 v[112:115], a[224:227], v[96:99], v[112:115]\n"
    "v_mfma_f32_16x16x32_bf16 v[116:119], v[64:67], v[96:99], v[116:119]\n"
    "s_waitcnt lgkmcnt(2)\n"
    "v_mfma_f32_16x16x32_bf16 v[104:107], a[68:71], v[100:103], v[104:107]\n"
    "v_mfma_f32_16x16x32_bf16 v[108:111], a[148:151], v[100:103], v[108:111]\n"
    "v_mfma_f32_16x16x32_bf16 v[112:115], a[228:231], v[100:103], v[112:115]\n"
    "v_mfma_f32_16x16x32_bf16 v[116:119], v[68:71], v[100:103], v[116:119]\n"
    "s_waitcnt lgkmcnt(1)\n"
    "v_mfma_f32_16x16x32_bf16 v[104:107], a[72:75], v[80:83], v[104:107]\n"
    "v_mfma_f32_16x16x32_bf16 v[108:111], a[152:155], v[80:83], v[108:111]\n"
    "v_mfma_f32_16x16x32_bf16 v[112:115], a[232:235], v[80:83], v[112:115]\n"
    "v_mfma_f32_16x16x32_bf16 v[116:119], v[72:75], v[80:83], v[116:119]\n"
    "s_waitcnt lgkmcnt(0)\n"
    "v_mfma_f32_16x16x32_bf16 v[104:107], a[76:79], v[84:87], v[104:107]\n"
    "v_mfma_f32_16x16x32_bf16 v[108:111], a[156:159], v[84:87], v[108:111]\n"
    "v_mfma_f32_16x16x32_bf16 v[112:115], a[236:239], v[84:87], v[112:115]\n"
    "v_mfma_f32_16x16x32_bf16 v[116:119], v[76:79], v[84:87], v[116:119]\n"
    "Fm1_%=:\n"
    "s_nop 4\n"
    "v_fma_f32 v160, -v154, v120, v104\n"
    "v_fma_f32 v161, -v154, v121, v105\n"
    "v_fma_f32 v162, -v154, v122, v106\n"
    "v_fma_f32 v163, -v154, v123, v107\n"
    "v_fma_f32 v164, -v154, v124, v108\n"
    "v_fma_f32 v165, -v154, v125, v109\n"
    "v_fma_f32 v166, -v154, v126, v110\n"
    "v_fma_f32 v167, -v154, v127, v111\n"
    "v_mul_f32 v160, v160, v155\n"
    "v_mul_f32 v161, v161, v155\n"
    "v_mul_f32 v162, v162, v155\n"
    "v_mul_f32 v163, v163, v155\n"
    "v_mul_f32 v164, v164, v155\n"
    "v_mul_f32 v165, v165, v155\n"
    "v_mul_f32 v166, v166, v155\n"
    "v_mul_f32 v167, v167, v155\n"
    "v_cvt_pk_bf16_f32 v160, v160, v161\n"
    "v_cvt_pk_bf16_f32 v162, v162, v163\n"
    "v_cvt_pk_bf16_f32 v164, v164, v165\n"
    "v_cvt_pk_bf16_f32 v166, v166, v167\n"
    "v_and_b32 v161, 0xffff0000, v160\n"
    "v_lshlrev_b32 v160, 16, v160\n"
    "v_and_b32 v163, 0xffff0000, v162\n"
    "v_lshlrev_b32 v162, 16, v162\n"
    "v_and_b32 v165, 0xffff0000, v164\n"
    "v_lshlrev_b32 v164, 16, v164\n"
    "v_and_b32 v167, 0xffff0000, v166\n"
    "v_lshlrev_b32 v166, 16, v166\n"
    "v_fma_f32 v168, -v154, v128, v112\n"
    "v_fma_f32 v169, -v154, v129, v113\n"
    "v_fma_f32 v170, -v154, v130, v114\n"
    "v_fma_f32 v171, -v154, v131, v115\n"
    "v_fma_f32 v172, -v154, v132, v116\n"
    "v_fma_f32 v173, -v154, v133, v117\n"
    "v_fma_f32 v174, -v154, v134, v118\n"
    "v_fma_f32 v175, -v154, v135, v119\n"
    "v_mul_f32 v168, v168, v155\n"
    "v_mul_f32 v169, v169, v155\n"
    "v_mul_f32 v170, v170, v155\n"
    "v_mul_f32 v171, v171, v155\n"
    "v_mul_f32 v172, v172, v155\n"
    "v_mul_f32 v173, v173, v155\n"
    "v_mul_f32 v174, v174, v155\n"
    "v_mul_f32 v175, v175, v155\n"
    "v_cvt_pk_bf16_f32 v168, v168, v169\n"
    "v_cvt_pk_bf16_f32 v170, v170, v171\n"
    "v_cvt_pk_bf16_f32 v172, v172, v173\n"
    "v_cvt_pk_bf16_f32 v174, v174, v175\n"
    "v_and_b32 v169, 0xffff0000, v168\n"
    "v_lshlrev_b32 v168, 16, v168\n"
    "v_and_b32 v171, 0xffff0000, v170\n"
    "v_lshlrev_b32 v170, 16, v170\n"
    "v_and_b32 v173, 0xffff0000, v172\n"
    "v_lshlrev_b32 v172, 16, v172\n"
    "v_and_b32 v175, 0xffff0000, v174\n"
    "v_lshlrev_b32 v174, 16, v174\n"
    "v_mul_f32 v176, 0xbfb8aa3b, v160\n"
    "v_mul_f32 v177, 0xbfb8aa3b, v161\n"
    "v_mul_f32 v178, 0xbfb8aa3b, v162\n"
    "v_mul_f32 v179, 0xbfb8aa3b, v163\n"
    "v_mul_f32 v180, 0xbfb8aa3b, v164\n"
    "v_mul_f32 v181, 0xbfb8aa3b, v165\n"
    "v_mul_f32 v182, 0xbfb8aa3b, v166\n"
    "v_mul_f32 v183, 0xbfb8aa3b, v167\n"
    "v_exp_f32 v176, v176\n"
    "v_exp_f32 v177, v177\n"
    "v_exp_f32 v178, v178\n"
    "v_exp_f32 v179, v179\n"
    "v_exp_f32 v180, v180\n"
    "v_exp_f32 v181, v181\n"
    "v_exp_f32 v182, v182\n"
    "v_exp_f32 v183, v183\n"
    "v_add_f32 v176, 1.0, v176\n"
    "v_add_f32 v177, 1.0, v177\n"
    "v_add_f32 v178, 1.0, v178\n"
    "v_add_f32 v179, 1.0, v179\n"
    "v_add_f32 v180, 1.0, v180\n"
    "v_add_f32 v181, 1.0, v181\n"
    "v_add_f32 v182, 1.0, v182\n"
    "v_add_f32 v183, 1.0, v183\n"
    "v_rcp_f32 v176, v176\n"
    "v_rcp_f32 v177, v177\n"
    "v_rcp_f32 v178, v178\n"
    "v_rcp_f32 v179, v179\n"
    "v_rcp_f32 v180, v180\n"
    "v_rcp_f32 v181, v181\n"
    "v_rcp_f32 v182, v182\n"
    "v_rcp_f32 v183, v183\n"
    "v_mul_f32 v160, v160, v176\n"
    "v_mul_f32 v161, v161, v177\n"
    "v_mul_f32 v162, v162, v178\n"
    "v_mul_f32 v163, v163, v179\n"
    "v_mul_f32 v164, v164, v180\n"
    "v_mul_f32 v165, v165, v181\n"
    "v_mul_f32 v166, v166, v182\n"
    "v_mul_f32 v167, v167, v183\n"
    "v_cvt_pk_bf16_f32 v160, v160, v161\n"
    "v_cvt_pk_bf16_f32 v162, v162, v163\n"
    "v_cvt_pk_bf16_f32 v164, v164, v165\n"
    "v_cvt_pk_bf16_f32 v166, v166, v167\n"
    "v_and_b32 v161, 0xffff0000, v160\n"
    "v_lshlrev_b32 v160, 16, v160\n"
    "v_and_b32 v163, 0xffff0000, v162\n"
    "v_lshlrev_b32 v162, 16, v162\n"
    "v_and_b32 v165, 0xffff0000, v164\n"
    "v_lshlrev_b32 v164, 16, v164\n"
    "v_and_b32 v167, 0xffff0000, v166\n"
    "v_lshlrev_b32 v166, 16, v166\n"
    "v_mul_f32 v160, v160, v168\n"
    "v_mul_f32 v161, v161, v169\n"
    "v_mul_f32 v162, v162, v170\n"
    "v_mul_f32 v163, v163, v171\n"
    "v_mul_f32 v164, v164, v172\n"
    "v_mul_f32 v165, v165, v173\n"
    "v_mul_f32 v166, v166, v174\n"
    "v_mul_f32 v167, v167, v175\n"
    "v_cvt_pk_bf16_f32 v184, v160, v161\n"
    "v_cvt_pk_bf16_f32 v185, v162, v163\n"
    "v_cvt_pk_bf16_f32 v186, v164, v165\n"
    "v_cvt_pk_bf16_f32 v187, v166, v167\n"
    "global_store_dwordx4 %[vg16], v[184:187], s[50:51] offset:0 sc1\n"
    "s_add_u32 s50, s50, 221184\n"
    "s_addc_u32 s51, s51, 0\n"
    "s_mov_b32 s61, s56\n"
    "s_add_u32 s40, s40, 2\n"
    "s_cmp_lt_u32 s40, 64\n"
    "s_cbranch_scc1 Floop_%=\n"
    "s_waitcnt vmcnt(0)\n"
    "s_barrier\n"
    "s_cmp_lg_u32 %[w], 3\n"
    "s_cbranch_scc1 Fp3_%=\n"
    "s_cmp_eq_u32 s61, 0\n"
    "s_cbranch_scc1 Fp3_%=\n"
    "v_mov_b32 v148, s61\n"
    "s_mov_b64 exec, 1\n"
    "global_store_dword v149, v148, %[fo] offset:0 sc1\n"
    "s_mov_b64 exec, -1\n"
    "Fp3_%=:\n"
    "s_mov_b32 s61, 0\n"
    "s_branch Fend_%=\n"
    "Fdead_%=:\n"
    "v_mov_b32 v254, 1792\n"
    "v_mov_b32 v255, 2098432\n"
    "global_store_dword v255, v254, %[ws] offset:0 sc1\n"
    "s_waitcnt vmcnt(0)\n"
    "s_endpgm\n"
    "Fend_%=:\n"
    "s_endpgm\n"
    "s_mov_b32 m0, s99\n"
    :
    : [vw] "v"(csrc128(lane, w, FF * 2)), [vrd0] "v"(lb + crd128(lane, 2 * fh)), [vrd1] "v"(lb + crd128(lane, 2 * fh + 1)), [vg16] "v"(lane * 16),
      [vflv] "v"((z * 20 + min(lane, 19)) * 128), [vxv] "v"(w * 10240 + lane * 16), [vlr] "v"(lb + t * 20480 + lane * 16), [vsv] "v"(G * 640 + (lane & 15) * 8),
      [ws] "s"(p.ws), [wg] "s"((const char*)p.wg + col), [wu] "s"((const char*)p.wu + col), [hg] "s"(p.ws + O_HF + z * 40960),
      [sg] "s"(p.ws + O_ST + (z * 2 + t) * 2560), [ag] "s"(p.ws + O_ACT + (size_t)((z * 2 + t) * 54 + 2 * j + fh) * 1024), [fo] "s"(p.ws + F_ACT + (z * 27 + j) * 128),
      [w] "s"(w), [lb] "s"(lb), [z] "s"(__builtin_amdgcn_readfirstlane(z)), [c32] "s"(0.03125f)
    : "memory", "vcc", "scc", "v16", "v17", "v18", "v19", "v20", "v21", "v22", "v23", "v24", "v25", "v26", "v27", "v28", "v29", "v30", "v31", "v32", "v33", "v34", "v35", "v36", "v37", "v38", "v39", "v40", "v41", "v42", "v43", "v44", "v45", "v46", "v47", "v48", "v49", "v50", "v51", "v52", "v53", "v54", "v55", "v56", "v57", "v58", "v59", "v60", "v61", "v62", "v63", "v64", "v65", "v66", "v67", "v68", "v69", "v70", "v71", "v72", "v73", "v74", "v75", "v76", "v77", "v78", "v79", "v80", "v81", "v82", "v83", "v84", "v85", "v86", "v87", "v88", "v89", "v90", "v91", "v92", "v93", "v94", "v95", "v96", "v97", "v98", "v99", "v100", "v101", "v102", "v103", "v104", "v105", "v106", "v107", "v108", "v109", "v110", "v111", "v112", "v113", "v114", "v115", "v116", "v117", "v118", "v119", "v120", "v121", "v122", "v123", "v124", "v125", "v126", "v127", "v128", "v129", "v130", "v131", "v132", "v133", "v134", "v135", "v136", "v137", "v138", "v139", "v140", "v141", "v142", "v143", "v144", "v145", "v146", "v147", "v148", "v149", "v150", "v151", "v152", "v153", "v154", "v155", "v156", "v157", "v158", "v159", "v160", "v161", "v162", "v163", "v164", "v165", "v166", "v167", "v168", "v169", "v170", "v171", "v172", "v173", "v174", "v175", "v176", "v177", "v178", "v179", "v180", "v181", "v182", "v183", "v184", "v185", "v186", "v187", "v188", "v189", "v190", "v191", "v192", "v193", "v194", "v195", "v196", "v197", "v198", "v199", "v200", "v201", "v202", "v203", "v204", "v205", "v206", "v207", "v208", "v209", "v210", "v211", "v212", "v213", "v214", "v215", "v216", "v217", "v218", "v219", "v220", "v221", "v222", "v223", "v224", "v225", "v226", "v227", "v228", "v229", "v230", "v231", "v232", "v233", "v234", "v235", "v236", "v237", "v238", "v239", "v240", "v241", "v242", "v243", "v244", "v245", "v246", "v247", "v248", "v249", "v250", "v251", "v252", "v253", "v254", "v255", "a0", "a1", "a2", "a3", "a4", "a5", "a6", "a7", "a8", "a9", "a10", "a11", "a12", "a13", "a14", "a15", "a16", "a17", "a18", "a19", "a20", "a21", "a22", "a23", "a24", "a25", "a26", "a27", "a28", "a29", "a30", "a31", "a32", "a33", "a34", "a35", "a36", "a37", "a38", "a39", "a40", "a41", "a42", "a43", "a44", "a45", "a46", "a47", "a48", "a49", "a50", "a51", "a52", "a53", "a54", "a55", "a56", "a57", "a58", "a59", "a60", "a61", "a62", "a63", "a64", "a65", "a66", "a67", "a68", "a69", "a70", "a71", "a72", "a73", "a74", "a75", "a76", "a77", "a78", "a79", "a80", "a81", "a82", "a83", "a84", "a85", "a86", "a87", "a88", "a89", "a90", "a91", "a92", "a93", "a94", "a95", "a96", "a97", "a98", "a99", "a100", "a101", "a102", "a103", "a104", "a105", "a106", "a107", "a108", "a109", "a110", "a111", "a112", "a113", "a114", "a115", "a116", "a117", "a118", "a119", "a120", "a121", "a122", "a123", "a124", "a125", "a126", "a127", "a128", "a129", "a130", "a131", "a132", "a133", "a134", "a135", "a136", "a137", "a138", "a139", "a140", "a141", "a142", "a143", "a144", "a145", "a146", "a147", "a148", "a149", "a150", "a151", "a152", "a153", "a154", "a155", "a156", "a157", "a158", "a159", "a160", "a161", "a162", "a163", "a164", "a165", "a166", "a167", "a168", "a169", "a170", "a171", "a172", "a173", "a174", "a175", "a176", "a177", "a178", "a179", "a180", "a181", "a182", "a183", "a184", "a185", "a186", "a187", "a188", "a189", "a190", "a191", "a192", "a193", "a194", "a195", "a196", "a197", "a198", "a199", "a200", "a201", "a202", "a203", "a204", "a205", "a206", "a207", "a208", "a209", "a210", "a211", "a212", "a213", "a214", "a215", "a216", "a217", "a218", "a219", "a220", "a221", "a222", "a223", "a224", "a225", "a226", "a227", "a228", "a229", "a230", "a231", "a232", "a233", "a234", "a235", "a236", "a237", "a238", "a239", "a240", "a241", "a242", "a243", "a244", "a245", "a246", "a247", "a248", "a249", "a250", "a251", "a252", "a253", "a254", "a255", "s40", "s41", "s42", "s43", "s44", "s45", "s46", "s47", "s48", "s49", "s50", "s51", "s52", "s53", "s54", "s55", "s56", "s57", "s58", "s59", "s60", "s61", "s62", "s63", "s64", "s65", "s66", "s67", "s99");
}

DI void setup_ln(const Params& p, int li, lptr L, int lane, int w) {
  const u32 lb = __builtin_amdgcn_readfirstlane((u32)(size_t)L);
  asm volatile(
    "global_load_dwordx4 v[100:103], %[vxi], %[lx] offset:0\n"
    "global_load_dwordx4 v[104:107], %[vxi], %[lx] offset:256\n"
    "global_load_dwordx4 v[108:111], %[vxi], %[lx] offset:512\n"
    "global_load_dwordx4 v[112:115], %[vxi], %[lx] offset:768\n"
    "global_load_dwordx4 v[116:119], %[vxi], %[lx] offset:1024\n"
    "s_mov_b32 s64, 2097152\n"
    "s_load_dwordx2 s[64:65], %[ws], s64\n"
    "s_waitcnt vmcnt(0)\n"
    "s_waitcnt lgkmcnt(0)\n"
    "s_add_u32 s64, s64, 1\n"
    "s_and_b32 s65, s65, -8\n"
    "v_lshlrev_b32 v120, 16, v100\n"
    "v_and_b32 v121, 0xffff0000, v100\n"
    "v_lshlrev_b32 v122, 16, v101\n"
    "v_and_b32 v123, 0xffff0000, v101\n"
    "v_lshlrev_b32 v124, 16, v102\n"
    "v_and_b32 v125, 0xffff0000, v102\n"
    "v_lshlrev_b32 v126, 16, v103\n"
    "v_and_b32 v127, 0xffff0000, v103\n"
    "v_lshlrev_b32 v128, 16, v104\n"
    "v_and_b32 v129, 0xffff0000, v104\n"
    "v_lshlrev_b32 v130, 16, v105\n"
    "v_and_b32 v131, 0xffff0000, v105\n"
    "v_lshlrev_b32 v132, 16, v106\n"
    "v_and_b32 v133, 0xffff0000, v106\n"
    "v_lshlrev_b32 v134, 16, v107\n"
    "v_and_b32 v135, 0xffff0000, v107\n"
    "v_lshlrev_b32 v136, 16, v108\n"
    "v_and_b32 v137, 0xffff0000, v108\n"
    "v_lshlrev_b32 v138, 16, v109\n"
    "v_and_b32 v139, 0xffff0000, v109\n"
    "v_lshlrev_b32 v140, 16, v110\n"
    "v_and_b32 v141, 0xffff0000, v110\n"
    "v_lshlrev_b32 v142, 16, v111\n"
    "v_and_b32 v143, 0xffff0000, v111\n"
    "v_lshlrev_b32 v144, 16, v112\n"
    "v_and_b32 v145, 0xffff0000, v112\n"
    "v_lshlrev_b32 v146, 16, v113\n"
    "v_and_b32 v147, 0xffff0000, v113\n"
    "v_lshlrev_b32 v148, 16, v114\n"
    "v_and_b32 v149, 0xffff0000, v114\n"
    "v_lshlrev_b32 v150, 16, v115\n"
    "v_and_b32 v151, 0xffff0000, v115\n"
    "v_lshlrev_b32 v152, 16, v116\n"
    "v_and_b32 v153, 0xffff0000, v116\n"
    "v_lshlrev_b32 v154, 16, v117\n"
    "v_and_b32 v155, 0xffff0000, v117\n"
    "v_lshlrev_b32 v156, 16, v118\n"
    "v_and_b32 v157, 0xffff0000, v118\n"
    "v_lshlrev_b32 v158, 16, v119\n"
    "v_and_b32 v159, 0xffff0000, v119\n"
    "v_pk_add_f32 v[162:163], v[120:121], v[122:123]\n"
    "v_pk_add_f32 v[162:163], v[162:163], v[124:125]\n"
    "v_pk_add_f32 v[162:163], v[162:163], v[126:127]\n"
    "v_pk_add_f32 v[162:163], v[162:163], v[128:129]\n"
    "v_pk_add_f32 v[162:163], v[162:163], v[130:131]\n"
    "v_pk_add_f32 v[162:163], v[162:163], v[132:133]\n"
    "v_pk_add_f32 v[162:163], v[162:163], v[134:135]\n"
    "v_pk_add_f32 v[162:163], v[162:163], v[136:137]\n"
    "v_pk_add_f32 v[162:163], v[162:163], v[138:139]\n"
    "v_pk_add_f32 v[162:163], v[162:163], v[140:141]\n"
    "v_pk_add_f32 v[162:163], v[162:163], v[142:143]\n"
    "v_pk_add_f32 v[162:163], v[162:163], v[144:145]\n"
    "v_pk_add_f32 v[162:163], v[162:163], v[146:147]\n"
    "v_pk_add_f32 v[162:163], v[162:163], v[148:149]\n"
    "v_pk_add_f32 v[162:163], v[162:163], v[150:151]\n"
    "v_pk_add_f32 v[162:163], v[162:163], v[152:153]\n"
    "v_pk_add_f32 v[162:163], v[162:163], v[154:155]\n"
    "v_pk_add_f32 v[162:163], v[162:163], v[156:157]\n"
    "v_pk_add_f32 v[162:163], v[162:163], v[158:159]\n"
    "v_add_f32 v162, v162, v163\n"
    "v_mov_b32 v164, v162\n"
    "s_nop 1\n"
    "v_permlane16_swap_b32 v162, v164\n"
    "v_add_f32 v162, v162, v164\n"
    "v_mov_b32 v164, v162\n"
    "s_nop 1\n"
    "v_permlane32_swap_b32 v162, v164\n"
    "v_add_f32 v162, v162, v164\n"
    "ds_write_b32 %[vrw], v162 offset:0\n"
    "s_waitcnt lgkmcnt(0)\n"
    "s_barrier\n"
    "ds_read_b32 v170, %[vrr] offset:0\n"
    "ds_read_b32 v171, %[vrr] offset:64\n"
    "ds_read_b32 v172, %[vrr] offset:128\n"
    "ds_read_b32 v173, %[vrr] offset:192\n"
    "s_waitcnt lgkmcnt(2)\n"
    "v_add_f32 v170, v170, v171\n"
    "s_waitcnt lgkmcnt(0)\n"
    "v_add_f32 v172, v172, v173\n"
    "v_add_f32 v170, v170, v172\n"
    "v_mul_f32 v160, 0x3acccccd, v170\n"
    "v_pk_add_f32 v[168:169], v[120:121], v[160:161] op_sel_hi:[1,0] neg_lo:[0,1] neg_hi:[0,1]\n"
    "v_pk_mul_f32 v[162:163], v[168:169], v[168:169]\n"
    "v_pk_add_f32 v[168:169], v[122:123], v[160:161] op_sel_hi:[1,0] neg_lo:[0,1] neg_hi:[0,1]\n"
    "v_pk_fma_f32 v[162:163], v[168:169], v[168:169], v[162:163]\n"
    "v_pk_add_f32 v[168:169], v[124:125], v[160:161] op_sel_hi:[1,0] neg_lo:[0,1] neg_hi:[0,1]\n"
    "v_pk_fma_f32 v[162:163], v[168:169], v[168:169], v[162:163]\n"
    "v_pk_add_f32 v[168:169], v[126:127], v[160:161] op_sel_hi:[1,0] neg_lo:[0,1] neg_hi:[0,1]\n"
    "v_pk_fma_f32 v[162:163], v[168:169], v[168:169], v[162:163]\n"
    "v_pk_add_f32 v[168:169], v[128:129], v[160:161] op_sel_hi:[1,0] neg_lo:[0,1] neg_hi:[0,1]\n"
    "v_pk_fma_f32 v[162:163], v[168:169], v[168:169], v[162:163]\n"
    "v_pk_add_f32 v[168:169], v[130:131], v[160:161] op_sel_hi:[1,0] neg_lo:[0,1] neg_hi:[0,1]\n"
    "v_pk_fma_f32 v[162:163], v[168:169], v[168:169], v[162:163]\n"
    "v_pk_add_f32 v[168:169], v[132:133], v[160:161] op_sel_hi:[1,0] neg_lo:[0,1] neg_hi:[0,1]\n"
    "v_pk_fma_f32 v[162:163], v[168:169], v[168:169], v[162:163]\n"
    "v_pk_add_f32 v[168:169], v[134:135], v[160:161] op_sel_hi:[1,0] neg_lo:[0,1] neg_hi:[0,1]\n"
    "v_pk_fma_f32 v[162:163], v[168:169], v[168:169], v[162:163]\n"
    "v_pk_add_f32 v[168:169], v[136:137], v[160:161] op_sel_hi:[1,0] neg_lo:[0,1] neg_hi:[0,1]\n"
    "v_pk_fma_f32 v[162:163], v[168:169], v[168:169], v[162:163]\n"
    "v_pk_add_f32 v[168:169], v[138:139], v[160:161] op_sel_hi:[1,0] neg_lo:[0,1] neg_hi:[0,1]\n"
    "v_pk_fma_f32 v[162:163], v[168:169], v[168:169], v[162:163]\n"
    "v_pk_add_f32 v[168:169], v[140:141], v[160:161] op_sel_hi:[1,0] neg_lo:[0,1] neg_hi:[0,1]\n"
    "v_pk_fma_f32 v[162:163], v[168:169], v[168:169], v[162:163]\n"
    "v_pk_add_f32 v[168:169], v[142:143], v[160:161] op_sel_hi:[1,0] neg_lo:[0,1] neg_hi:[0,1]\n"
    "v_pk_fma_f32 v[162:163], v[168:169], v[168:169], v[162:163]\n"
    "v_pk_add_f32 v[168:169], v[144:145], v[160:161] op_sel_hi:[1,0] neg_lo:[0,1] neg_hi:[0,1]\n"
    "v_pk_fma_f32 v[162:163], v[168:169], v[168:169], v[162:163]\n"
    "v_pk_add_f32 v[168:169], v[146:147], v[160:161] op_sel_hi:[1,0] neg_lo:[0,1] neg_hi:[0,1]\n"
    "v_pk_fma_f32 v[162:163], v[168:169], v[168:169], v[162:163]\n"
    "v_pk_add_f32 v[168:169], v[148:149], v[160:161] op_sel_hi:[1,0] neg_lo:[0,1] neg_hi:[0,1]\n"
    "v_pk_fma_f32 v[162:163], v[168:169], v[168:169], v[162:163]\n"
    "v_pk_add_f32 v[168:169], v[150:151], v[160:161] op_sel_hi:[1,0] neg_lo:[0,1] neg_hi:[0,1]\n"
    "v_pk_fma_f32 v[162:163], v[168:169], v[168:169], v[162:163]\n"
    "v_pk_add_f32 v[168:169], v[152:153], v[160:161] op_sel_hi:[1,0] neg_lo:[0,1] neg_hi:[0,1]\n"
    "v_pk_fma_f32 v[162:163], v[168:169], v[168:169], v[162:163]\n"
    "v_pk_add_f32 v[168:169], v[154:155], v[160:161] op_sel_hi:[1,0] neg_lo:[0,1] neg_hi:[0,1]\n"
    "v_pk_fma_f32 v[162:163], v[168:169], v[168:169], v[162:163]\n"
    "v_pk_add_f32 v[168:169], v[156:157], v[160:161] op_sel_hi:[1,0] neg_lo:[0,1] neg_hi:[0,1]\n"
    "v_pk_fma_f32 v[162:163], v[168:169], v[168:169], v[162:163]\n"
    "v_pk_add_f32 v[168:169], v[158:159], v[160:161] op_sel_hi:[1,0] neg_lo:[0,1] neg_hi:[0,1]\n"
    "v_pk_fma_f32 v[162:163], v[168:169], v[168:169], v[162:163]\n"
    "v_add_f32 v162, v162, v163\n"
    "v_mov_b32 v164, v162\n"
    "s_nop 1\n"
    "v_permlane16_swap_b32 v162, v164\n"
    "v_add_f32 v162, v162, v164\n"
    "v_mov_b32 v164, v162\n"
    "s_nop 1\n"
    "v_permlane32_swap_b32 v162, v164\n"
    "v_add_f32 v162, v162, v164\n"
    "ds_write_b32 %[vrw], v162 offset:256\n"
    "s_waitcnt lgkmcnt(0)\n"
    "s_barrier\n"
    "ds_read_b32 v170, %[vrr] offset:256\n"
    "ds_read_b32 v171, %[vrr] offset:320\n"
    "ds_read_b32 v172, %[vrr] offset:384\n"
    "ds_read_b32 v173, %[vrr] offset:448\n"
    "s_waitcnt lgkmcnt(2)\n"
    "v_add_f32 v170, v170, v171\n"
    "s_waitcnt lgkmcnt(0)\n"
    "v_add_f32 v172, v172, v173\n"
    "v_add_f32 v170, v170, v172\n"
    "v_mul_f32 v166, 0x3acccccd, v170\n"
    "v_cvt_pk_bf16_f32 v166, v166, v166\n"
    "v_and_b32 v166, 0xffff0000, v166\n"
    "v_add_f32 v166, 0x3727c5ac, v166\n"
    "v_cvt_pk_bf16_f32 v166, v166, v166\n"
    "v_and_b32 v166, 0xffff0000, v166\n"
    "v_sqrt_f32 v166, v166\n"
    "s_nop 0\n"
    "v_cvt_pk_bf16_f32 v166, v166, v166\n"
    "v_and_b32 v166, 0xffff0000, v166\n"
    "v_rcp_f32 v166, v166\n"
    "v_cvt_pk_bf16_f32 v160, v160, v160\n"
    "v_and_b32 v160, 0xffff0000, v160\n"
    "s_mov_b64 s[62:63], %[lo]\n"
    "v_pk_add_f32 v[168:169], v[120:121], v[160:161] op_sel_hi:[1,0] neg_lo:[0,1] neg_hi:[0,1]\n"
    "v_cvt_pk_bf16_f32 v169, v168, v169\n"
    "v_lshlrev_b32 v168, 16, v169\n"
    "v_and_b32 v169, 0xffff0000, v169\n"
    "v_pk_mul_f32 v[168:169], v[168:169], v[166:167] op_sel_hi:[1,0]\n"
    "v_cvt_pk_bf16_f32 v100, v168, v169\n"
    "v_pk_add_f32 v[168:169], v[122:123], v[160:161] op_sel_hi:[1,0] neg_lo:[0,1] neg_hi:[0,1]\n"
    "v_cvt_pk_bf16_f32 v169, v168, v169\n"
    "v_lshlrev_b32 v168, 16, v169\n"
    "v_and_b32 v169, 0xffff0000, v169\n"
    "v_pk_mul_f32 v[168:169], v[168:169], v[166:167] op_sel_hi:[1,0]\n"
    "v_cvt_pk_bf16_f32 v101, v168, v169\n"
    "v_pk_add_f32 v[168:169], v[124:125], v[160:161] op_sel_hi:[1,0] neg_lo:[0,1] neg_hi:[0,1]\n"
    "v_cvt_pk_bf16_f32 v169, v168, v169\n"
    "v_lshlrev_b32 v168, 16, v169\n"
    "v_and_b32 v169, 0xffff0000, v169\n"
    "v_pk_mul_f32 v[168:169], v[168:169], v[166:167] op_sel_hi:[1,0]\n"
    "v_cvt_pk_bf16_f32 v102, v168, v169\n"
    "v_pk_add_f32 v[168:169], v[126:127], v[160:161] op_sel_hi:[1,0] neg_lo:[0,1] neg_hi:[0,1]\n"
    "v_cvt_pk_bf16_f32 v169, v168, v169\n"
    "v_lshlrev_b32 v168, 16, v169\n"
    "v_and_b32 v169, 0xffff0000, v169\n"
    "v_pk_mul_f32 v[168:169], v[168:169], v[166:167] op_sel_hi:[1,0]\n"
    "v_cvt_pk_bf16_f32 v103, v168, v169\n"
    "global_store_dwordx4 %[vxo], v[100:103], s[62:63] offset:0 sc1\n"
    "s_add_u32 s62, s62, 4096\n"
    "s_addc_u32 s63, s63, 0\n"
    "v_pk_add_f32 v[168:169], v[128:129], v[160:161] op_sel_hi:[1,0] neg_lo:[0,1] neg_hi:[0,1]\n"
    "v_cvt_pk_bf16_f32 v169, v168, v169\n"
    "v_lshlrev_b32 v168, 16, v169\n"
    "v_and_b32 v169, 0xffff0000, v169\n"
    "v_pk_mul_f32 v[168:169], v[168:169], v[166:167] op_sel_hi:[1,0]\n"
    "v_cvt_pk_bf16_f32 v104, v168, v169\n"
    "v_pk_add_f32 v[168:169], v[130:131], v[160:161] op_sel_hi:[1,0] neg_lo:[0,1] neg_hi:[0,1]\n"
    "v_cvt_pk_bf16_f32 v169, v168, v169\n"
    "v_lshlrev_b32 v168, 16, v169\n"
    "v_and_b32 v169, 0xffff0000, v169\n"
    "v_pk_mul_f32 v[168:169], v[168:169], v[166:167] op_sel_hi:[1,0]\n"
    "v_cvt_pk_bf16_f32 v105, v168, v169\n"
    "v_pk_add_f32 v[168:169], v[132:133], v[160:161] op_sel_hi:[1,0] neg_lo:[0,1] neg_hi:[0,1]\n"
    "v_cvt_pk_bf16_f32 v169, v168, v169\n"
    "v_lshlrev_b32 v168, 16, v169\n"
    "v_and_b32 v169, 0xffff0000, v169\n"
    "v_pk_mul_f32 v[168:169], v[168:169], v[166:167] op_sel_hi:[1,0]\n"
    "v_cvt_pk_bf16_f32 v106, v168, v169\n"
    "v_pk_add_f32 v[168:169], v[134:135], v[160:161] op_sel_hi:[1,0] neg_lo:[0,1] neg_hi:[0,1]\n"
    "v_cvt_pk_bf16_f32 v169, v168, v169\n"
    "v_lshlrev_b32 v168, 16, v169\n"
    "v_and_b32 v169, 0xffff0000, v169\n"
    "v_pk_mul_f32 v[168:169], v[168:169], v[166:167] op_sel_hi:[1,0]\n"
    "v_cvt_pk_bf16_f32 v107, v168, v169\n"
    "global_store_dwordx4 %[vxo], v[104:107], s[62:63] offset:0 sc1\n"
    "s_add_u32 s62, s62, 4096\n"
    "s_addc_u32 s63, s63, 0\n"
    "v_pk_add_f32 v[168:169], v[136:137], v[160:161] op_sel_hi:[1,0] neg_lo:[0,1] neg_hi:[0,1]\n"
    "v_cvt_pk_bf16_f32 v169, v168, v169\n"
    "v_lshlrev_b32 v168, 16, v169\n"
    "v_and_b32 v169, 0xffff0000, v169\n"
    "v_pk_mul_f32 v[168:169], v[168:169], v[166:167] op_sel_hi:[1,0]\n"
    "v_cvt_pk_bf16_f32 v108, v168, v169\n"
    "v_pk_add_f32 v[168:169], v[138:139], v[160:161] op_sel_hi:[1,0] neg_lo:[0,1] neg_hi:[0,1]\n"
    "v_cvt_pk_bf16_f32 v169, v168, v169\n"
    "v_lshlrev_b32 v168, 16, v169\n"
    "v_and_b32 v169, 0xffff0000, v169\n"
    "v_pk_mul_f32 v[168:169], v[168:169], v[166:167] op_sel_hi:[1,0]\n"
    "v_cvt_pk_bf16_f32 v109, v168, v169\n"
    "v_pk_add_f32 v[168:169], v[140:141], v[160:161] op_sel_hi:[1,0] neg_lo:[0,1] neg_hi:[0,1]\n"
    "v_cvt_pk_bf16_f32 v169, v168, v169\n"
    "v_lshlrev_b32 v168, 16, v169\n"
    "v_and_b32 v169, 0xffff0000, v169\n"
    "v_pk_mul_f32 v[168:169], v[168:169], v[166:167] op_sel_hi:[1,0]\n"
    "v_cvt_pk_bf16_f32 v110, v168, v169\n"
    "v_pk_add_f32 v[168:169], v[142:143], v[160:161] op_sel_hi:[1,0] neg_lo:[0,1] neg_hi:[0,1]\n"
    "v_cvt_pk_bf16_f32 v169, v168, v169\n"
    "v_lshlrev_b32 v168, 16, v169\n"
    "v_and_b32 v169, 0xffff0000, v169\n"
    "v_pk_mul_f32 v[168:169], v[168:169], v[166:167] op_sel_hi:[1,0]\n"
    "v_cvt_pk_bf16_f32 v111, v168, v169\n"
    "global_store_dwordx4 %[vxo], v[108:111], s[62:63] offset:0 sc1\n"
    "s_add_u32 s62, s62, 4096\n"
    "s_addc_u32 s63, s63, 0\n"
    "v_pk_add_f32 v[168:169], v[144:145], v[160:161] op_sel_hi:[1,0] neg_lo:[0,1] neg_hi:[0,1]\n"
    "v_cvt_pk_bf16_f32 v169, v168, v169\n"
    "v_lshlrev_b32 v168, 16, v169\n"
    "v_and_b32 v169, 0xffff0000, v169\n"
    "v_pk_mul_f32 v[168:169], v[168:169], v[166:167] op_sel_hi:[1,0]\n"
    "v_cvt_pk_bf16_f32 v112, v168, v169\n"
    "v_pk_add_f32 v[168:169], v[146:147], v[160:161] op_sel_hi:[1,0] neg_lo:[0,1] neg_hi:[0,1]\n"
    "v_cvt_pk_bf16_f32 v169, v168, v169\n"
    "v_lshlrev_b32 v168, 16, v169\n"
    "v_and_b32 v169, 0xffff0000, v169\n"
    "v_pk_mul_f32 v[168:169], v[168:169], v[166:167] op_sel_hi:[1,0]\n"
    "v_cvt_pk_bf16_f32 v113, v168, v169\n"
    "v_pk_add_f32 v[168:169], v[148:149], v[160:161] op_sel_hi:[1,0] neg_lo:[0,1] neg_hi:[0,1]\n"
    "v_cvt_pk_bf16_f32 v169, v168, v169\n"
    "v_lshlrev_b32 v168, 16, v169\n"
    "v_and_b32 v169, 0xffff0000, v169\n"
    "v_pk_mul_f32 v[168:169], v[168:169], v[166:167] op_sel_hi:[1,0]\n"
    "v_cvt_pk_bf16_f32 v114, v168, v169\n"
    "v_pk_add_f32 v[168:169], v[150:151], v[160:161] op_sel_hi:[1,0] neg_lo:[0,1] neg_hi:[0,1]\n"
    "v_cvt_pk_bf16_f32 v169, v168, v169\n"
    "v_lshlrev_b32 v168, 16, v169\n"
    "v_and_b32 v169, 0xffff0000, v169\n"
    "v_pk_mul_f32 v[168:169], v[168:169], v[166:167] op_sel_hi:[1,0]\n"
    "v_cvt_pk_bf16_f32 v115, v168, v169\n"
    "global_store_dwordx4 %[vxo], v[112:115], s[62:63] offset:0 sc1\n"
    "s_add_u32 s62, s62, 4096\n"
    "s_addc_u32 s63, s63, 0\n"
    "v_pk_add_f32 v[168:169], v[152:153], v[160:161] op_sel_hi:[1,0] neg_lo:[0,1] neg_hi:[0,1]\n"
    "v_cvt_pk_bf16_f32 v169, v168, v169\n"
    "v_lshlrev_b32 v168, 16, v169\n"
    "v_and_b32 v169, 0xffff0000, v169\n"
    "v_pk_mul_f32 v[168:169], v[168:169], v[166:167] op_sel_hi:[1,0]\n"
    "v_cvt_pk_bf16_f32 v116, v168, v169\n"
    "v_pk_add_f32 v[168:169], v[154:155], v[160:161] op_sel_hi:[1,0] neg_lo:[0,1] neg_hi:[0,1]\n"
    "v_cvt_pk_bf16_f32 v169, v168, v169\n"
    "v_lshlrev_b32 v168, 16, v169\n"
    "v_and_b32 v169, 0xffff0000, v169\n"
    "v_pk_mul_f32 v[168:169], v[168:169], v[166:167] op_sel_hi:[1,0]\n"
    "v_cvt_pk_bf16_f32 v117, v168, v169\n"
    "v_pk_add_f32 v[168:169], v[156:157], v[160:161] op_sel_hi:[1,0] neg_lo:[0,1] neg_hi:[0,1]\n"
    "v_cvt_pk_bf16_f32 v169, v168, v169\n"
    "v_lshlrev_b32 v168, 16, v169\n"
    "v_and_b32 v169, 0xffff0000, v169\n"
    "v_pk_mul_f32 v[168:169], v[168:169], v[166:167] op_sel_hi:[1,0]\n"
    "v_cvt_pk_bf16_f32 v118, v168, v169\n"
    "v_pk_add_f32 v[168:169], v[158:159], v[160:161] op_sel_hi:[1,0] neg_lo:[0,1] neg_hi:[0,1]\n"
    "v_cvt_pk_bf16_f32 v169, v168, v169\n"
    "v_lshlrev_b32 v168, 16, v169\n"
    "v_and_b32 v169, 0xffff0000, v169\n"
    "v_pk_mul_f32 v[168:169], v[168:169], v[166:167] op_sel_hi:[1,0]\n"
    "v_cvt_pk_bf16_f32 v119, v168, v169\n"
    "global_store_dwordx4 %[vxo], v[116:119], s[62:63] offset:0 sc1\n"
    "s_add_u32 s62, s62, 4096\n"
    "s_addc_u32 s63, s63, 0\n"
    "s_waitcnt vmcnt(0)\n"
    "s_barrier\n"
    "s_cmp_lg_u32 %[w], 0\n"
    "s_cbranch_scc1 Lp_%=\n"
    "v_mov_b32 v120, s64\n"
    "v_mov_b32 v121, s65\n"
    "v_mov_b32 v122, 0\n"
    "s_mov_b64 exec, 1\n"
    "global_store_dwordx2 v122, v[120:121], %[iw] offset:0 sc1\n"
    "s_mov_b64 exec, -1\n"
    "Lp_%=:\n"
    "s_sleep 64\n"
    "s_sleep 64\n"
    :
    : LN_OPS, [w] "s"(w), [ws] "s"(p.ws)
    : "memory", "vcc", "scc", "v16", "v17", "v18", "v19", "v20", "v21", "v22", "v23", "v24", "v25", "v26", "v27", "v28", "v29", "v30", "v31", "v32", "v33", "v34", "v35", "v36", "v37", "v38", "v39", "v40", "v41", "v42", "v43", "v44", "v45", "v46", "v47", "v48", "v49", "v50", "v51", "v52", "v53", "v54", "v55", "v56", "v57", "v58", "v59", "v60", "v61", "v62", "v63", "v64", "v65", "v66", "v67", "v68", "v69", "v70", "v71", "v72", "v73", "v74", "v75", "v76", "v77", "v78", "v79", "v80", "v81", "v82", "v83", "v84", "v85", "v86", "v87", "v88", "v89", "v90", "v91", "v92", "v93", "v94", "v95", "v96", "v97", "v98", "v99", "v100", "v101", "v102", "v103", "v104", "v105", "v106", "v107", "v108", "v109", "v110", "v111", "v112", "v113", "v114", "v115", "v116", "v117", "v118", "v119", "v120", "v121", "v122", "v123", "v124", "v125", "v126", "v127", "v128", "v129", "v130", "v131", "v132", "v133", "v134", "v135", "v136", "v137", "v138", "v139", "v140", "v141", "v142", "v143", "v144", "v145", "v146", "v147", "v148", "v149", "v150", "v151", "v152", "v153", "v154", "v155", "v156", "v157", "v158", "v159", "v160", "v161", "v162", "v163", "v164", "v165", "v166", "v167", "v168", "v169", "v170", "v171", "v172", "v173", "v174", "v175", "v176", "v177", "v178", "v179", "v180", "v181", "v182", "v183", "v184", "v185", "v186", "v187", "v188", "v189", "v190", "v191", "v192", "v193", "v194", "v195", "v196", "v197", "v198", "v199", "v200", "v201", "v202", "v203", "v204", "v205", "v206", "v207", "v208", "v209", "v210", "v211", "v212", "v213", "v214", "v215", "v216", "v217", "v218", "v219", "v220", "v221", "v222", "v223", "v224", "v225", "v226", "v227", "v228", "v229", "v230", "v231", "v232", "v233", "v234", "v235", "v236", "v237", "v238", "v239", "v240", "v241", "v242", "v243", "v244", "v245", "v246", "v247", "v248", "v249", "v250", "v251", "v252", "v253", "v254", "v255", "s40", "s41", "s42", "s43", "s44", "s45", "s46", "s47", "s48", "s49", "s50", "s51", "s52", "s53", "s54", "s55", "s56", "s57", "s58", "s59", "s60", "s61", "s62", "s63", "s64", "s65", "s66", "s67", "s99");
}
DI void warm_wk(const Params& p, int x, int j, int lane, int w) {
  asm volatile(
    "s_mov_b64 s[44:45], %[src]\n"
    "global_load_dwordx4 v[16:19], %[vo], s[44:45] offset:0\n"
    "global_load_dwordx4 v[144:147], %[vo], s[44:45] offset:1024\n"
    "s_add_u32 s44, s44, 196608\n"
    "s_addc_u32 s45, s45, 0\n"
    "global_load_dwordx4 v[20:23], %[vo], s[44:45] offset:0\n"
    "global_load_dwordx4 v[148:151], %[vo], s[44:45] offset:1024\n"
    "s_add_u32 s44, s44, 196608\n"
    "s_addc_u32 s45, s45, 0\n"
    "global_load_dwordx4 v[24:27], %[vo], s[44:45] offset:0\n"
    "global_load_dwordx4 v[152:155], %[vo], s[44:45] offset:1024\n"
    "s_add_u32 s44, s44, 196608\n"
    "s_addc_u32 s45, s45, 0\n"
    "global_load_dwordx4 v[28:31], %[vo], s[44:45] offset:0\n"
    "global_load_dwordx4 v[156:159], %[vo], s[44:45] offset:1024\n"
    "s_add_u32 s44, s44, 196608\n"
    "s_addc_u32 s45, s45, 0\n"
    "global_load_dwordx4 v[32:35], %[vo], s[44:45] offset:0\n"
    "global_load_dwordx4 v[160:163], %[vo], s[44:45] offset:1024\n"
    "s_add_u32 s44, s44, 196608\n"
    "s_addc_u32 s45, s45, 0\n"
    "global_load_dwordx4 v[36:39], %[vo], s[44:45] offset:0\n"
    "global_load_dwordx4 v[164:167], %[vo], s[44:45] offset:1024\n"
    "s_add_u32 s44, s44, 196608\n"
    "s_addc_u32 s45, s45, 0\n"
    "global_load_dwordx4 v[40:43], %[vo], s[44:45] offset:0\n"
    "global_load_dwordx4 v[168:171], %[vo], s[44:45] offset:1024\n"
    "s_add_u32 s44, s44, 196608\n"
    "s_addc_u32 s45, s45, 0\n"
    "global_load_dwordx4 v[44:47], %[vo], s[44:45] offset:0\n"
    "global_load_dwordx4 v[172:175], %[vo], s[44:45] offset:1024\n"
    "s_add_u32 s44, s44, 196608\n"
    "s_addc_u32 s45, s45, 0\n"
    "global_load_dwordx4 v[48:51], %[vo], s[44:45] offset:0\n"
    "global_load_dwordx4 v[176:179], %[vo], s[44:45] offset:1024\n"
    "s_add_u32 s44, s44, 196608\n"
    "s_addc_u32 s45, s45, 0\n"
    "global_load_dwordx4 v[52:55], %[vo], s[44:45] offset:0\n"
    "global_load_dwordx4 v[180:183], %[vo], s[44:45] offset:1024\n"
    "s_add_u32 s44, s44, 196608\n"
    "s_addc_u32 s45, s45, 0\n"
    "global_load_dwordx4 v[56:59], %[vo], s[44:45] offset:0\n"
    "global_load_dwordx4 v[184:187], %[vo], s[44:45] offset:1024\n"
    "s_add_u32 s44, s44, 196608\n"
    "s_addc_u32 s45, s45, 0\n"
    "global_load_dwordx4 v[60:63], %[vo], s[44:45] offset:0\n"
    "global_load_dwordx4 v[188:191], %[vo], s[44:45] offset:1024\n"
    "s_add_u32 s44, s44, 196608\n"
    "s_addc_u32 s45, s45, 0\n"
    "s_waitcnt vmcnt(0)\n"
    :
    : [vo] "v"(lane * 16), [src] "s"((const char*)p.wk + (size_t)(352 + j * 4 + w) * 8192 + x * 2048)
    : "memory", "scc", "v16", "v17", "v18", "v19", "v20", "v21", "v22", "v23", "v24", "v25", "v26", "v27", "v28", "v29", "v30", "v31", "v32", "v33", "v34", "v35", "v36", "v37", "v38", "v39", "v40", "v41", "v42", "v43", "v44", "v45", "v46", "v47", "v48", "v49", "v50", "v51", "v52", "v53", "v54", "v55", "v56", "v57", "v58", "v59", "v60", "v61", "v62", "v63", "v64", "v65", "v66", "v67", "v68", "v69", "v70", "v71", "v72", "v73", "v74", "v75", "v76", "v77", "v78", "v79", "v80", "v81", "v82", "v83", "v84", "v85", "v86", "v87", "v88", "v89", "v90", "v91", "v92", "v93", "v94", "v95", "v96", "v97", "v98", "v99", "v100", "v101", "v102", "v103", "v104", "v105", "v106", "v107", "v108", "v109", "v110", "v111", "v112", "v113", "v114", "v115", "v116", "v117", "v118", "v119", "v120", "v121", "v122", "v123", "v124", "v125", "v126", "v127", "v128", "v129", "v130", "v131", "v132", "v133", "v134", "v135", "v136", "v137", "v138", "v139", "v140", "v141", "v142", "v143", "v144", "v145", "v146", "v147", "v148", "v149", "v150", "v151", "v152", "v153", "v154", "v155", "v156", "v157", "v158", "v159", "v160", "v161", "v162", "v163", "v164", "v165", "v166", "v167", "v168", "v169", "v170", "v171", "v172", "v173", "v174", "v175", "v176", "v177", "v178", "v179", "v180", "v181", "v182", "v183", "v184", "v185", "v186", "v187", "v188", "v189", "v190", "v191", "v192", "v193", "v194", "v195", "v196", "v197", "v198", "v199", "v200", "v201", "v202", "v203", "v204", "v205", "v206", "v207", "v208", "v209", "v210", "v211", "v212", "v213", "v214", "v215", "v216", "v217", "v218", "v219", "v220", "v221", "v222", "v223", "v224", "v225", "v226", "v227", "v228", "v229", "v230", "v231", "v232", "v233", "v234", "v235", "v236", "v237", "v238", "v239", "v240", "v241", "v242", "v243", "v244", "v245", "v246", "v247", "v248", "v249", "v250", "v251", "v252", "v253", "v254", "v255", "s40", "s41", "s42", "s43", "s44", "s45", "s46", "s47", "s48", "s49", "s50", "s51", "s52", "s53", "s54", "s55", "s56", "s57", "s58", "s59", "s60", "s61", "s62", "s63", "s64", "s65", "s66", "s67", "s99");
}
DI void setup_misc(const Params& p, int j, int tid, int w) {
  char* ws = p.ws;
  u32 payload = 0;
  u64 ep = *(const volatile u64*)(ws + F_EP);
  ep = (u64)((u32)ep + 1u) | (ep & 0xfffffff800000000ull);
  if (j == 0) {
    for (u32 i = 80 + tid; i < CTRL_BYTES / 16; i += 256) gst16(ws + CTRL, i * 16, u4_t{0, 0, 0, 0});
  } else if (j == 1) {
    if (w == 0) {
      u32 best[2] = {0xffffffffu, 0xffffffffu};
#pragma unroll
      for (int r = 0; r < 12; ++r) {
        u64 c0, c1;
        asm volatile("s_memtime %0\n\ts_waitcnt lgkmcnt(0)" : "=s"(c0) :: "memory");
        gst4m(ws + PROBE, (u32)((r & 1) * 8192 + r * 256), (u32)r, 1); VMW(0);
        asm volatile("s_memtime %0\n\ts_waitcnt lgkmcnt(0)" : "=s"(c1) :: "memory");
        best[r & 1] = min(best[r & 1], (u32)(c1 - c0));
      }
      payload = best[1] < best[0] ? 1u : 0u;
    }
  } else {
    {
      const u32 ch = j < 7 ? C_MEM + j - 2 : C_HINT;
      if (ch >= C_MEM && ch < C_MEM + 5) {
        const u32* m = (const u32*)p.mem0;
        u32 v[8];
#pragma unroll
        for (int k = 0; k < 8; ++k) {
          const u32 i = tid + 256 * k, d = ch < C_MEM + 4 ? i : 2048 + (i & 511);
          const u32 cc = d >> 6, g = (d >> 5) & 1, ck = (d >> 2) & 7, mm = d & 3;
          v[k] = m[((4 * g + (ck >> 1)) * DM + 16 * cc + 4 * mm + 2 * (ck & 1)) >> 1];
        }
#pragma unroll
        for (int k = 0; k < 8; ++k) {
          if (v[k] == SENT) v[k] |= 0x00400040u;
          gst4(ws + (ch << 14), (tid + 256 * k) * 4, v[k]); gst4(ws + (ch << 14), 8192 + (tid + 256 * k) * 4, v[k]);
        }
      } else {
#pragma unroll
        for (int i = 0; i < 4; ++i) gst16(ws + (ch << 14), (tid + 256 * i) * 16, u4_t{0, 0, 0, 0});
      }
    }
  }
  post_init(ws, j, ep, payload, w);
}

__global__ void __launch_bounds__(256, 1) rmt_main(Params p) {
  extern __shared__ __attribute__((aligned(16))) char smem[];
  lptr L = (lptr)smem;
  const int w = __builtin_amdgcn_readfirstlane(threadIdx.x >> 6);
  const int x = blockIdx.x & 7, t = blockIdx.x >> 3, slot = x * 32 + t;
  if (t < 3) {
    const u32 o = min((t * 256 + threadIdx.x) * 128, CODE_BYTES - 128);
    asm volatile("s_getpc_b64 s[40:41]\n\tglobal_load_dword v255, %0, s[40:41]" :: "v"(o) : "memory", "s40", "s41", "v255");
  }
  (void)slot;
  STAMP(0);
  int wi;
  if (x < 4) {
    if (t < 8) { if (ROLEMASK & 1) { role_a(p, x * 8 + t, x, L, lane_id(), w); DUMP(); } return; }
    if (t >= 24 && t < 28) warm_wk(p, x, t - 24, lane_id(), w);
    if (t < 28) { if (ROLEMASK & 1) { role_b(p, (x & 1) * 20 + (t - 8), x, t < 24 ? x * 16 + t - 8 : -1, L, lane_id(), w); DUMP(); } return; }
    if (t >= 30) { if (ROLEMASK & 2) setup_misc(p, x * 2 + (t - 30), w * 64 + lane_id(), w); warm_wk(p, x, t - 26, lane_id(), w); return; }
    wi = 88 + x * 2 + (t - 28);
  } else wi = t < 22 ? (x - 4) * 22 + t : 96 + (x - 4) * 10 + (t - 22);
  if (wi < 96) {
    if (ROLEMASK & 2) {
      if (wi >= 64) role_qkv<true>(p, wi, wi < 88 ? 40 + wi : -1, L, lane_id(), w);
      else role_qkv<false>(p, wi, 40 + wi, L, lane_id(), w);
    }
    STAMP(2);
    for (int i = 0; i < (wi & 7); ++i) __builtin_amdgcn_s_sleep(24);
    if (wi < 54) { if (ROLEMASK & 8) role_ffn1(p, wi / 27, wi % 27, L, lane_id(), w); }
    else if (wi < 84) {
      if (ROLEMASK & 16) {
        role_proj<false>(p, 2 * ((wi - 54) % 10), (wi - 54) / 10, L, lane_id(), w);
      }
    }
  } else {
    if (ROLEMASK & 2) setup_ln(p, wi - 96, L, lane_id(), w);
    if (ROLEMASK & 4) role_proj<true>(p, 2 * ((wi - 96) % 10), (wi - 96) / 10, L, lane_id(), w);
  }
  DUMP();
}

#define DA_DEF 36
#define DB_DEF 36
#define DP_DEF 0

static void launch_all(void* const* d_in, void* d_out, void* d_ws, hipStream_t stream, int split, int dA, int dB, int dP) {
  Params p;
  p.x = (const u16*)d_in[0]; p.mem0 = (const u16*)d_in[1]; p.wq = (const u16*)d_in[2]; p.wk = (const u16*)d_in[3];
  p.wv = (const u16*)d_in[4]; p.wo = (const u16*)d_in[5]; p.wg = (const u16*)d_in[8]; p.wu = (const u16*)d_in[9];
  p.wd = (const u16*)d_in[10]; p.out = (u16*)d_out;
  p.ws = (char*)(((uintptr_t)d_ws + (2u << 20) - 1) & ~(uintptr_t)((2u << 20) - 1));
  p.rolemask = 31; p.dA = dA; p.dB = dB; p.dP = dP;
  (void)hipFuncSetAttribute((const void*)rmt_main, hipFuncAttributeMaxDynamicSharedMemorySize, LDS_BYTES);
  if (!split) { rmt_main<<<256, 256, LDS_BYTES, stream>>>(p); return; }
  const int order[5] = {2, 1, 4, 8, 16};
  for (int i = 0; i < 5; ++i) {
    p.rolemask = order[i];
    rmt_main<<<256, 256, LDS_BYTES, stream>>>(p);
  }
}

extern "C" void kernel_launch(void* const* d_in, const int* in_sizes, int n_in, void* d_out, int out_size, void* d_ws, size_t ws_size, hipStream_t stream) {
  launch_all(d_in, d_out, d_ws, stream, 0, DA_DEF, DB_DEF, DP_DEF);
}
```
